```python
import torch

def _softmax(x, axis=-1):
    return torch.softmax(x, dim=axis)
def _gelu(x):
    return torch.nn.functional.gelu(x, approximate="tanh")
def _silu(x):
    return torch.nn.functional.silu(x)
def _sigmoid(x):
    return torch.sigmoid(x)
def _layernorm(x, axis=-1, eps=1e-5):
    m = x.mean(dim=axis, keepdim=True)
    v = x.var(dim=axis, keepdim=True, unbiased=False)
    return (x - m) / torch.sqrt(v + eps)
def _scatter_add(tgt, idx, src, axis=0):
    return tgt.clone().index_add_(axis, idx, src)
def _segment_sum(src, seg, n):
    out = torch.zeros((n,) + src.shape[1:], dtype=src.dtype)
    return out.index_add_(0, seg, src)
def _pad(x, pw):
    flat = tuple(v for pair in reversed(pw) for v in pair)
    return torch.nn.functional.pad(x, flat)

kernel_name = 'standalone__mha_block_cached__near_ridge'

def setup_inputs():

    _w = (lambda a: (lambda t: t.to(torch.bfloat16) if t.is_floating_point() else t)(torch.as_tensor(a)))
    B, Sq, D = 32, 192, 448
    H, Sk, Dh = 16, 512, 256
    return {
        "x": _w((lambda *s: torch.randn(*s, dtype=torch.bfloat16) * 0.8)(B, Sq, D)),
        "k_cache": _w((lambda *s: torch.randn(*s, dtype=torch.bfloat16) * 0.8)(B, H, Sk, Dh)),
        "v_cache": _w((lambda *s: torch.randn(*s, dtype=torch.bfloat16) * 0.05)(B, H, Sk, Dh)),
        "wq": _w((lambda *s: torch.randn(*s, dtype=torch.bfloat16) * 0.8)(D, H * Dh) * (D ** -0.5)),
        "wo": _w((lambda *s: torch.randn(*s, dtype=torch.bfloat16) * 0.05)(H * Dh, D) * ((H * Dh) ** -0.5)),
    }

def reference(x, k_cache, v_cache, wq, wo):
    B, Sq, D = x.shape
    H, Dh = 16, 256
    q = (lambda x, *d: x.permute(*d))((x @ wq).reshape(B, Sq, H, Dh), 0, 2, 1, 3)
    scores = torch.einsum("bhqd,bhkd->bhqk", q, k_cache) * (Dh ** -0.5)
    p = _softmax(scores, axis=-1)
    o = torch.einsum("bhqk,bhkd->bhqd", p, v_cache)
    return (lambda x, *d: x.permute(*d))(o, 0, 2, 1, 3).reshape(B, Sq, H * Dh) @ wo
```

```cpp
#include <hip/hip_runtime.h>
#include <hip/hip_bf16.h>
typedef __attribute__((ext_vector_type(8))) __bf16 bf16x8;
typedef __attribute__((ext_vector_type(4))) float f32x4;
static constexpr int B=32,SQ=192,D=448,H=16,SK=512,DH=256;
__device__ inline float bf2f(unsigned short u){ return __uint_as_float(((unsigned)u)<<16);}
__device__ inline unsigned short f2bf(float f){ __hip_bfloat16 b=__float2bfloat16(f); return *(unsigned short*)&b;}
__global__ void gemm_naive(const unsigned short* A,const unsigned short* Bm,unsigned short* C,int M,int N,int K){
  int lane=threadIdx.x&63; int w=threadIdx.x>>6;
  int tn=blockIdx.x*4+w; int tm=blockIdx.y;
  if(tn*16>=N) return;
  f32x4 acc={0,0,0,0};
  int r=lane&15,g=lane>>4;
  for(int k=0;k<K;k+=32){
    bf16x8 a=*(const bf16x8*)(A+(size_t)(tm*16+r)*K+k+8*g);
    bf16x8 b;
    for(int j=0;j<8;j++){ unsigned short u=Bm[(size_t)(k+8*g+j)*N+tn*16+r]; b[j]=__builtin_bit_cast(__bf16,u);}
    acc=__builtin_amdgcn_mfma_f32_16x16x32_bf16(a,b,acc,0,0,0);
  }
  for(int j=0;j<4;j++) C[(size_t)(tm*16+g*4+j)*N+tn*16+r]=f2bf(acc[j]);
}
__global__ void attn_naive(const unsigned short* Q,const unsigned short* K,const unsigned short* V,unsigned short* O){
  __shared__ float S[16][SK+1];
  __shared__ float Qs[16][DH];
  int b=blockIdx.z,h=blockIdx.y,q0=blockIdx.x*16;
  int t=threadIdx.x;
  for(int i=t;i<16*DH;i+=256){int r=i/DH,d=i%DH; Qs[r][d]=bf2f(Q[(size_t)(b*SQ+q0+r)*(H*DH)+h*DH+d]);}
  __syncthreads();
  const unsigned short* Kp=K+(size_t)(b*H+h)*SK*DH; const unsigned short* Vp=V+(size_t)(b*H+h)*SK*DH;
  for(int k=t;k<SK;k+=256){
    float acc[16]; for(int r=0;r<16;r++)acc[r]=0;
    for(int d=0;d<DH;d++){float kv=bf2f(Kp[(size_t)k*DH+d]); for(int r=0;r<16;r++)acc[r]+=Qs[r][d]*kv;}
    for(int r=0;r<16;r++)S[r][k]=acc[r]*0.0625f;
  }
  __syncthreads();
  if(t<16){float m=-1e30f; for(int k=0;k<SK;k++)m=fmaxf(m,S[t][k]); float l=0; for(int k=0;k<SK;k++){float e=__expf(S[t][k]-m); S[t][k]=e; l+=e;} float inv=1.f/l; for(int k=0;k<SK;k++)S[t][k]=bf2f(f2bf(S[t][k]*inv));}
  __syncthreads();
  {int d=t; float acc[16]; for(int r=0;r<16;r++)acc[r]=0;
   for(int k=0;k<SK;k++){float v=bf2f(Vp[(size_t)k*DH+d]); for(int r=0;r<16;r++)acc[r]+=S[r][k]*v;}
   for(int r=0;r<16;r++)O[(size_t)(b*SQ+q0+r)*(H*DH)+h*DH+d]=f2bf(acc[r]);}
}
extern "C" void kernel_launch(void* const* d_in, const int* in_sizes, int n_in, void* d_out, int out_size, void* d_ws, size_t ws_size, hipStream_t stream) {
  const unsigned short* x=(const unsigned short*)d_in[0];
  const unsigned short* k=(const unsigned short*)d_in[1];
  const unsigned short* v=(const unsigned short*)d_in[2];
  const unsigned short* wq=(const unsigned short*)d_in[3];
  const unsigned short* wo=(const unsigned short*)d_in[4];
  unsigned short* Q=(unsigned short*)d_ws; unsigned short* O=Q+(size_t)B*SQ*H*DH;
  gemm_naive<<<dim3(H*DH/64,B*SQ/16),256,0,stream>>>(x,wq,Q,B*SQ,H*DH,D);
  attn_naive<<<dim3(SQ/16,H,B),256,0,stream>>>(Q,k,v,O);
  gemm_naive<<<dim3(D/64,B*SQ/16),256,0,stream>>>(O,wo,(unsigned short*)d_out,B*SQ,D,H*DH);
}
```

```cpp
#include <hip/hip_runtime.h>
#include <hip/hip_bf16.h>

__global__ __launch_bounds__(256, 1) void mha_fused_kernel(const void* x, const void* k, const void* v, const void* wq, const void* wo, void* out, void* ws) {
  __shared__ __attribute__((aligned(16))) char lds[163840];
  unsigned tid = threadIdx.x;
  unsigned ldsb = (unsigned)(size_t)lds;
  asm volatile(
    "v_readfirstlane_b32 s79, v0\n"
    "s_nop 0\n"
    "s_lshr_b32 s64, s79, 6\n"
    "s_and_b32 s79, %[bid], 7\n"
    "s_lshr_b32 s80, %[bid], 3\n"
    "s_and_b32 s66, s80, 7\n"
    "s_lshr_b32 s80, s80, 3\n"
    "s_lshl_b32 s79, s79, 2\n"
    "s_add_u32 s65, s79, s80\n"
    "s_lshr_b32 s79, %[bid], 3\n"
    "s_lshl_b32 s79, s79, 2\n"
    "s_add_u32 s79, s79, s64\n"
    "s_cmp_ge_u32 s79, 46\n"
    "s_cbranch_scc1 LIPF\n"
    "s_getpc_b64 s[86:87]\n"
    "s_lshl_b32 s79, s79, 10\n"
    "v_and_b32 v4, 63, v0\n"
    "v_lshlrev_b32 v4, 4, v4\n"
    "v_add_u32 v4, s79, v4\n"
    "global_load_dwordx4 v[8:11], v4, s[86:87]\n"
    "LIPF:\n"
    "s_mov_b32 s44, %[xlo]\n"
    "s_and_b32 s45, %[xhi], 65535\n"
    "s_mov_b32 s46, 5505024\n"
    "s_mov_b32 s47, 131072\n"
    "s_mov_b32 s48, %[wslo]\n"
    "s_and_b32 s49, %[wshi], 65535\n"
    "s_mov_b32 s50, 67108864\n"
    "s_mov_b32 s51, 131072\n"
    "s_mov_b32 s56, %[wqlo]\n"
    "s_and_b32 s57, %[wqhi], 65535\n"
    "s_mov_b32 s58, 3670016\n"
    "s_mov_b32 s59, 131072\n"
    "s_mul_i32 s70, s64, 4224\n"
    "s_add_u32 s70, s70, %[ldsb]\n"
    "v_and_b32 v240, 63, v0\n"
    "v_and_b32 v241, 15, v240\n"
    "v_mul_u32_u24 v224, 1056, v241\n"
    "v_lshrrev_b32 v242, 4, v240\n"
    "v_lshl_add_u32 v224, v242, 4, v224\n"
    "v_add_u32 v224, %[ldsb], v224\n"
    "v_lshrrev_b32 v243, 3, v241\n"
    "v_mul_u32_u24 v243, 3, v243\n"
    "v_xor_b32 v243, v243, v242\n"
    "v_lshlrev_b32 v243, 4, v243\n"
    "v_lshl_add_u32 v252, v241, 6, v243\n"
    "v_add_u32 v252, s70, v252\n"
    "v_lshrrev_b32 v241, 2, v240\n"
    "v_mul_u32_u24 v225, 1056, v241\n"
    "v_and_b32 v242, 3, v240\n"
    "v_lshl_add_u32 v225, v242, 3, v225\n"
    "v_add_u32 v225, %[ldsb], v225\n"
    "v_lshrrev_b32 v243, 3, v241\n"
    "v_mul_u32_u24 v243, 3, v243\n"
    "v_xor_b32 v243, v243, v242\n"
    "v_lshlrev_b32 v243, 4, v243\n"
    "v_mul_u32_u24 v239, 896, v241\n"
    "v_add_u32 v239, v239, v243\n"
    "v_lshlrev_b32 v241, 4, v240\n"
    "v_add_u32 v228, 96, v241\n"
    "v_add_u32 v229, 64, v241\n"
    "v_add_u32 v230, 32, v241\n"
    "v_add_u32 v231, 0, v241\n"
    "s_mul_i32 s79, s65, 172032\n"
    "s_mul_i32 s80, s64, 43008\n"
    "s_add_u32 s76, s79, s80\n"
    "s_add_u32 s77, s76, 14336\n"
    "s_add_u32 s78, s76, 28672\n"
    "v_and_b32 v241, 496, v231\n"
    "v_lshrrev_b32 v242, 9, v231\n"
    "v_lshl_add_u32 v232, v242, 15, v241\n"
    "v_add_u32 v233, 7136, v232\n"
    "v_add_u32 v234, 14272, v232\n"
    "v_add_u32 v235, 21408, v232\n"
    "s_lshl_b32 s73, s64, 16\n"
    "s_lshl_b32 s79, s66, 10\n"
    "s_add_u32 s73, s73, s79\n"
    "s_mov_b32 s74, 262144\n"
    "s_mov_b32 s81, 262144\n"
    "s_mov_b32 s68, 0\n"
    "s_mov_b32 s69, 0\n"
    "s_mov_b32 s40, s68\n"
    "s_add_u32 s41, s40, 16896\n"
    "s_cmp_eq_u32 s41, 152064\n"
    "s_cselect_b32 s41, 0, s41\n"
    "s_add_u32 s42, s41, 16896\n"
    "s_cmp_eq_u32 s42, 152064\n"
    "s_cselect_b32 s42, 0, s42\n"
    "s_add_u32 s43, s42, 16896\n"
    "s_cmp_eq_u32 s43, 152064\n"
    "s_cselect_b32 s43, 0, s43\n"
    "s_add_u32 s52, s43, 16896\n"
    "s_cmp_eq_u32 s52, 152064\n"
    "s_cselect_b32 s52, 0, s52\n"
    "s_add_u32 s53, s52, 16896\n"
    "s_cmp_eq_u32 s53, 152064\n"
    "s_cselect_b32 s53, 0, s53\n"
    "s_add_u32 s54, s53, 16896\n"
    "s_cmp_eq_u32 s54, 152064\n"
    "s_cselect_b32 s54, 0, s54\n"
    "s_add_u32 s55, s54, 16896\n"
    "s_cmp_eq_u32 s55, 152064\n"
    "s_cselect_b32 s55, 0, s55\n"
    "s_add_u32 s33, s55, 16896\n"
    "s_cmp_eq_u32 s33, 152064\n"
    "s_cselect_b32 s33, 0, s33\n"
    "s_add_u32 s34, s33, s70\n"
    "s_add_u32 m0, s34, 0\n"
    "s_add_u32 s35, s76, 0\n"
    "buffer_load_dwordx4 v239, s[44:47], s35 offen lds\n"
    "s_add_u32 m0, s34, 1056\n"
    "s_add_u32 s35, s77, 0\n"
    "buffer_load_dwordx4 v239, s[44:47], s35 offen lds\n"
    "s_add_u32 m0, s34, 2112\n"
    "s_add_u32 s35, s78, 0\n"
    "buffer_load_dwordx4 v239, s[44:47], s35 offen lds\n"
    "s_add_u32 m0, s34, 3168\n"
    "s_add_u32 s35, s76, 64\n"
    "buffer_load_dwordx4 v239, s[44:47], s35 offen lds\n"
    "s_add_u32 m0, s69, s70\n"
    "s_nop 0\n"
    "buffer_load_dwordx4 v232, s[56:59], s73 offen lds\n"
    "buffer_load_dwordx4 v233, s[56:59], s73 offen offset:1056 lds\n"
    "buffer_load_dwordx4 v234, s[56:59], s73 offen offset:2112 lds\n"
    "buffer_load_dwordx4 v235, s[56:59], s73 offen offset:3168 lds\n"
    "s_add_u32 s69, s69, 16896\n"
    "s_cmp_eq_u32 s69, 152064\n"
    "s_cselect_b32 s69, 0, s69\n"
    "s_add_u32 s73, s73, s74\n"
    "s_add_u32 s34, s54, s70\n"
    "s_add_u32 m0, s34, 0\n"
    "s_add_u32 s35, s77, 64\n"
    "buffer_load_dwordx4 v239, s[44:47], s35 offen lds\n"
    "s_add_u32 m0, s34, 1056\n"
    "s_add_u32 s35, s78, 64\n"
    "buffer_load_dwordx4 v239, s[44:47], s35 offen lds\n"
    "s_add_u32 m0, s34, 2112\n"
    "s_add_u32 s35, s76, 128\n"
    "buffer_load_dwordx4 v239, s[44:47], s35 offen lds\n"
    "s_add_u32 m0, s34, 3168\n"
    "s_add_u32 s35, s77, 128\n"
    "buffer_load_dwordx4 v239, s[44:47], s35 offen lds\n"
    "s_add_u32 m0, s69, s70\n"
    "s_nop 0\n"
    "buffer_load_dwordx4 v232, s[56:59], s73 offen lds\n"
    "buffer_load_dwordx4 v233, s[56:59], s73 offen offset:1056 lds\n"
    "buffer_load_dwordx4 v234, s[56:59], s73 offen offset:2112 lds\n"
    "buffer_load_dwordx4 v235, s[56:59], s73 offen offset:3168 lds\n"
    "s_add_u32 s69, s69, 16896\n"
    "s_cmp_eq_u32 s69, 152064\n"
    "s_cselect_b32 s69, 0, s69\n"
    "s_add_u32 s73, s73, s74\n"
    "s_add_u32 s34, s53, s70\n"
    "s_add_u32 m0, s34, 0\n"
    "s_add_u32 s35, s78, 128\n"
    "buffer_load_dwordx4 v239, s[44:47], s35 offen lds\n"
    "s_add_u32 m0, s34, 1056\n"
    "s_add_u32 s35, s76, 192\n"
    "buffer_load_dwordx4 v239, s[44:47], s35 offen lds\n"
    "s_add_u32 m0, s34, 2112\n"
    "s_add_u32 s35, s77, 192\n"
    "buffer_load_dwordx4 v239, s[44:47], s35 offen lds\n"
    "s_add_u32 m0, s34, 3168\n"
    "s_add_u32 s35, s78, 192\n"
    "buffer_load_dwordx4 v239, s[44:47], s35 offen lds\n"
    "s_add_u32 m0, s69, s70\n"
    "s_nop 0\n"
    "buffer_load_dwordx4 v232, s[56:59], s73 offen lds\n"
    "buffer_load_dwordx4 v233, s[56:59], s73 offen offset:1056 lds\n"
    "buffer_load_dwordx4 v234, s[56:59], s73 offen offset:2112 lds\n"
    "buffer_load_dwordx4 v235, s[56:59], s73 offen offset:3168 lds\n"
    "s_add_u32 s69, s69, 16896\n"
    "s_cmp_eq_u32 s69, 152064\n"
    "s_cselect_b32 s69, 0, s69\n"
    "s_add_u32 s73, s73, s74\n"
    "s_add_u32 m0, s69, s70\n"
    "s_nop 0\n"
    "buffer_load_dwordx4 v232, s[56:59], s73 offen lds\n"
    "buffer_load_dwordx4 v233, s[56:59], s73 offen offset:1056 lds\n"
    "buffer_load_dwordx4 v234, s[56:59], s73 offen offset:2112 lds\n"
    "buffer_load_dwordx4 v235, s[56:59], s73 offen offset:3168 lds\n"
    "s_add_u32 s69, s69, 16896\n"
    "s_cmp_eq_u32 s69, 152064\n"
    "s_cselect_b32 s69, 0, s69\n"
    "s_add_u32 s73, s73, s74\n"
    "s_add_u32 m0, s69, s70\n"
    "s_nop 0\n"
    "buffer_load_dwordx4 v232, s[56:59], s73 offen lds\n"
    "buffer_load_dwordx4 v233, s[56:59], s73 offen offset:1056 lds\n"
    "buffer_load_dwordx4 v234, s[56:59], s73 offen offset:2112 lds\n"
    "buffer_load_dwordx4 v235, s[56:59], s73 offen offset:3168 lds\n"
    "s_add_u32 s69, s69, 16896\n"
    "s_cmp_eq_u32 s69, 152064\n"
    "s_cselect_b32 s69, 0, s69\n"
    "s_add_u32 s73, s73, s74\n"
    "s_add_u32 s34, s55, s70\n"
    "s_add_u32 m0, s34, 0\n"
    "s_add_u32 s35, s76, 256\n"
    "buffer_load_dwordx4 v239, s[44:47], s35 offen lds\n"
    "s_add_u32 m0, s34, 1056\n"
    "s_add_u32 s35, s77, 256\n"
    "buffer_load_dwordx4 v239, s[44:47], s35 offen lds\n"
    "s_add_u32 m0, s34, 2112\n"
    "s_add_u32 s35, s78, 256\n"
    "buffer_load_dwordx4 v239, s[44:47], s35 offen lds\n"
    "s_add_u32 m0, s34, 3168\n"
    "s_add_u32 s35, s76, 320\n"
    "buffer_load_dwordx4 v239, s[44:47], s35 offen lds\n"
    "s_lshl_b32 s34, s64, 11\n"
    "s_add_u32 s34, s34, 153088\n"
    "s_add_u32 s34, s34, %[ldsb]\n"
    "s_add_u32 m0, s34, 0\n"
    "s_add_u32 s35, s77, 832\n"
    "buffer_load_dwordx4 v239, s[44:47], s35 offen lds\n"
    "s_add_u32 m0, s34, 1024\n"
    "s_add_u32 s35, s78, 832\n"
    "buffer_load_dwordx4 v239, s[44:47], s35 offen lds\n"
    "s_lshl_b32 s79, %[bid], 2\n"
    "s_add_u32 s79, s79, s64\n"
    "s_lshl_b32 s79, s79, 12\n"
    "s_add_u32 m0, %[ldsb], 152064\n"
    "s_lshl_b32 s35, s66, 2\n"
    "s_add_u32 s35, s35, s64\n"
    "s_mul_i32 s35, s35, 5376\n"
    "s_mul_i32 s34, s65, 172032\n"
    "s_add_u32 s34, s34, s35\n"
    "buffer_load_dwordx4 v231, s[44:47], s34 offen lds\n"
    "s_add_u32 s35, s34, 1024\n"
    "buffer_load_dwordx4 v231, s[44:47], s35 offen lds\n"
    "s_add_u32 s35, s34, 2048\n"
    "buffer_load_dwordx4 v231, s[44:47], s35 offen lds\n"
    "s_add_u32 s35, s34, 3072\n"
    "buffer_load_dwordx4 v231, s[44:47], s35 offen lds\n"
    "s_add_u32 s35, s34, 4096\n"
    "buffer_load_dwordx4 v231, s[44:47], s35 offen lds\n"
    "s_add_u32 s35, s34, 5120\n"
    "buffer_load_dwordx4 v231, s[44:47], s35 offen lds\n"
    "s_mov_b32 s24, %[wqlo]\n"
    "s_and_b32 s25, %[wqhi], 65535\n"
    "s_mov_b32 s26, 3670016\n"
    "s_mov_b32 s27, 131072\n"
    "buffer_load_dwordx4 v231, s[24:27], s79 offen lds\n"
    "s_add_u32 s80, s79, 1024\n"
    "buffer_load_dwordx4 v231, s[24:27], s80 offen lds\n"
    "s_add_u32 s80, s79, 2048\n"
    "buffer_load_dwordx4 v231, s[24:27], s80 offen lds\n"
    "s_add_u32 s80, s79, 3072\n"
    "buffer_load_dwordx4 v231, s[24:27], s80 offen lds\n"
    "s_mov_b32 s24, %[wolo]\n"
    "s_and_b32 s25, %[wohi], 65535\n"
    "s_mov_b32 s26, 3670016\n"
    "s_mov_b32 s27, 131072\n"
    "buffer_load_dwordx4 v231, s[24:27], s79 offen lds\n"
    "s_add_u32 s80, s79, 1024\n"
    "buffer_load_dwordx4 v231, s[24:27], s80 offen lds\n"
    "s_add_u32 s80, s79, 2048\n"
    "buffer_load_dwordx4 v231, s[24:27], s80 offen lds\n"
    "s_add_u32 s80, s79, 3072\n"
    "buffer_load_dwordx4 v231, s[24:27], s80 offen lds\n"
    "s_lshl_b32 s79, s65, 14\n"
    "s_lshl_b32 s80, s64, 6\n"
    "s_add_u32 s79, s79, s80\n"
    "s_add_u32 s79, s79, 47185920\n"
    "s_lshl_b32 s80, s66, 8\n"
    "s_add_u32 s36, s79, s80\n"
    "s_lshl_b32 s80, s66, 11\n"
    "s_add_u32 s37, s79, s80\n"
    "v_lshrrev_b32 v241, 4, v231\n"
    "v_lshlrev_b32 v244, 11, v241\n"
    "v_cmp_gt_u32 vcc, 7, v241\n"
    "v_mov_b32 v242, 0x7fff0000\n"
    "s_nop 0\n"
    "v_cndmask_b32 v244, v242, v244, vcc\n"
    "v_cmp_ne_u32 vcc, s66, v241\n"
    "s_nop 1\n"
    "v_cndmask_b32 v244, v242, v244, vcc\n"
    "v_lshrrev_b32 v241, 4, v231\n"
    "v_lshlrev_b32 v245, 8, v241\n"
    "s_cmp_lt_u32 s66, 7\n"
    "s_cselect_b32 s80, 8, 0\n"
    "v_cmp_gt_u32 vcc, s80, v241\n"
    "v_mov_b32 v242, 0x7fff0000\n"
    "s_nop 0\n"
    "v_cndmask_b32 v245, v242, v245, vcc\n"
    "v_cmp_ne_u32 vcc, s66, v241\n"
    "s_nop 1\n"
    "v_cndmask_b32 v245, v242, v245, vcc\n"
    "buffer_load_dword v236, v244, s[48:51], s36 offen sc1\n"
    "buffer_load_dword v237, v245, s[48:51], s37 offen sc1\n"
    "s_sub_u32 s24, %[klo], 96\n"
    "s_subb_u32 s25, %[khi], 0\n"
    "s_and_b32 s25, s25, 65535\n"
    "s_mov_b32 s26, 134217824\n"
    "s_mov_b32 s27, 131072\n"
    "s_sub_u32 s28, %[vlo], 96\n"
    "s_subb_u32 s29, %[vhi], 0\n"
    "s_and_b32 s29, s29, 65535\n"
    "s_mov_b32 s30, 134217824\n"
    "s_mov_b32 s31, 131072\n"
    "s_lshl_b32 s79, s65, 4\n"
    "s_lshl_b32 s80, s66, 1\n"
    "s_add_u32 s79, s79, s80\n"
    "s_lshl_b32 s79, s79, 18\n"
    "s_lshl_b32 s80, s64, 12\n"
    "s_add_u32 s71, s79, s80\n"
    "s_mov_b32 s72, s71\n"
    "s_mov_b32 s67, 0\n"
    "s_mov_b32 s82, 0x3db8aa3b\n"
    "s_mov_b32 s83, 0x3db8aa3b\n"
    "s_mov_b32 s88, 0x41200000\n"
    "LQ:\n"
    "v_add_u32 v226, s33, v252\n"
    "s_waitcnt vmcnt(14)\n"
    "ds_read_b128 a[0:3], v226\n"
    "ds_read_b128 a[4:7], v226 offset:1056\n"
    "ds_read_b128 a[8:11], v226 offset:2112\n"
    "s_waitcnt vmcnt(46)\n"
    "s_barrier\n"
    "v_add_u32 v227, s68, v225\n"
    "s_add_u32 s68, s68, 16896\n"
    "s_cmp_eq_u32 s68, 152064\n"
    "s_cselect_b32 s68, 0, s68\n"
    "ds_read_b64_tr_b16 a[168:169], v227\n"
    "ds_read_b64_tr_b16 a[170:171], v227 offset:512\n"
    "ds_read_b64_tr_b16 a[172:173], v227 offset:32\n"
    "ds_read_b64_tr_b16 a[174:175], v227 offset:544\n"
    "ds_read_b64_tr_b16 a[176:177], v227 offset:64\n"
    "ds_read_b64_tr_b16 a[178:179], v227 offset:576\n"
    "ds_read_b64_tr_b16 a[180:181], v227 offset:96\n"
    "ds_read_b64_tr_b16 a[182:183], v227 offset:608\n"
    "s_waitcnt lgkmcnt(4)\n"
    "v_mfma_f32_16x16x32_bf16 v[0:3], a[168:171], a[0:3], 0\n"
    "ds_read_b64_tr_b16 a[184:185], v227 offset:128\n"
    "v_mfma_f32_16x16x32_bf16 v[8:11], a[168:171], a[4:7], 0\n"
    "ds_read_b64_tr_b16 a[186:187], v227 offset:640\n"
    "v_mfma_f32_16x16x32_bf16 v[16:19], a[168:171], a[8:11], 0\n"
    "ds_read_b128 a[12:15], v226 offset:3168\n"
    "v_add_u32 v226, s54, v252\n"
    "s_waitcnt vmcnt(10)\n"
    "v_mfma_f32_16x16x32_bf16 v[4:7], a[172:175], a[0:3], 0\n"
    "ds_read_b64_tr_b16 a[188:189], v227 offset:160\n"
    "v_mfma_f32_16x16x32_bf16 v[12:15], a[172:175], a[4:7], 0\n"
    "ds_read_b64_tr_b16 a[190:191], v227 offset:672\n"
    "v_mfma_f32_16x16x32_bf16 v[20:23], a[172:175], a[8:11], 0\n"
    "s_waitcnt lgkmcnt(5)\n"
    "ds_read_b128 a[16:19], v226\n"
    "s_waitcnt lgkmcnt(3)\n"
    "v_mfma_f32_16x16x32_bf16 v[24:27], a[176:179], a[0:3], 0\n"
    "ds_read_b64_tr_b16 a[168:169], v227 offset:192\n"
    "s_add_u32 s34, s33, s70\n"
    "v_mfma_f32_16x16x32_bf16 v[32:35], a[176:179], a[4:7], 0\n"
    "ds_read_b64_tr_b16 a[170:171], v227 offset:704\n"
    "s_add_u32 m0, s34, 0\n"
    "v_mfma_f32_16x16x32_bf16 v[40:43], a[176:179], a[8:11], 0\n"
    "ds_read_b128 a[20:23], v226 offset:1056\n"
    "s_add_u32 s35, s77, 320\n"
    "v_mfma_f32_16x16x32_bf16 v[28:31], a[180:183], a[0:3], 0\n"
    "ds_read_b64_tr_b16 a[172:173], v227 offset:224\n"
    "v_mfma_f32_16x16x32_bf16 v[36:39], a[180:183], a[4:7], 0\n"
    "ds_read_b64_tr_b16 a[174:175], v227 offset:736\n"
    "v_mfma_f32_16x16x32_bf16 v[44:47], a[180:183], a[8:11], 0\n"
    "s_waitcnt lgkmcnt(6)\n"
    "ds_read_b128 a[24:27], v226 offset:2112\n"
    "v_mfma_f32_16x16x32_bf16 v[48:51], a[184:187], a[0:3], 0\n"
    "ds_read_b64_tr_b16 a[176:177], v227 offset:256\n"
    "v_mfma_f32_16x16x32_bf16 v[56:59], a[184:187], a[4:7], 0\n"
    "ds_read_b64_tr_b16 a[178:179], v227 offset:768\n"
    "v_mfma_f32_16x16x32_bf16 v[64:67], a[184:187], a[8:11], 0\n"
    "ds_read_b128 a[28:31], v226 offset:3168\n"
    "v_mfma_f32_16x16x32_bf16 v[52:55], a[188:191], a[0:3], 0\n"
    "ds_read_b64_tr_b16 a[180:181], v227 offset:288\n"
    "v_mfma_f32_16x16x32_bf16 v[60:63], a[188:191], a[4:7], 0\n"
    "ds_read_b64_tr_b16 a[182:183], v227 offset:800\n"
    "v_mfma_f32_16x16x32_bf16 v[68:71], a[188:191], a[8:11], 0\n"
    "s_waitcnt lgkmcnt(6)\n"
    "s_waitcnt lgkmcnt(2)\n"
    "buffer_load_dwordx4 v239, s[44:47], s35 offen lds\n"
    "v_mfma_f32_16x16x32_bf16 v[72:75], a[168:171], a[0:3], 0\n"
    "ds_read_b64_tr_b16 a[184:185], v227 offset:320\n"
    "s_add_u32 m0, s34, 1056\n"
    "v_mfma_f32_16x16x32_bf16 v[80:83], a[168:171], a[4:7], 0\n"
    "ds_read_b64_tr_b16 a[186:187], v227 offset:832\n"
    "s_add_u32 s35, s78, 320\n"
    "v_mfma_f32_16x16x32_bf16 v[88:91], a[168:171], a[8:11], 0\n"
    "buffer_load_dwordx4 v239, s[44:47], s35 offen lds\n"
    "s_add_u32 m0, s34, 2112\n"
    "v_mfma_f32_16x16x32_bf16 v[76:79], a[172:175], a[0:3], 0\n"
    "ds_read_b64_tr_b16 a[188:189], v227 offset:352\n"
    "s_add_u32 s35, s76, 384\n"
    "v_mfma_f32_16x16x32_bf16 v[84:87], a[172:175], a[4:7], 0\n"
    "ds_read_b64_tr_b16 a[190:191], v227 offset:864\n"
    "v_mfma_f32_16x16x32_bf16 v[92:95], a[172:175], a[8:11], 0\n"
    "s_waitcnt lgkmcnt(4)\n"
    "buffer_load_dwordx4 v239, s[44:47], s35 offen lds\n"
    "v_mfma_f32_16x16x32_bf16 v[96:99], a[176:179], a[0:3], 0\n"
    "ds_read_b64_tr_b16 a[168:169], v227 offset:384\n"
    "s_add_u32 m0, s34, 3168\n"
    "v_mfma_f32_16x16x32_bf16 v[104:107], a[176:179], a[4:7], 0\n"
    "ds_read_b64_tr_b16 a[170:171], v227 offset:896\n"
    "s_add_u32 s35, s77, 384\n"
    "v_mfma_f32_16x16x32_bf16 v[112:115], a[176:179], a[8:11], 0\n"
    "buffer_load_dwordx4 v239, s[44:47], s35 offen lds\n"
    "s_add_u32 s34, s54, s70\n"
    "v_mfma_f32_16x16x32_bf16 v[100:103], a[180:183], a[0:3], 0\n"
    "ds_read_b64_tr_b16 a[172:173], v227 offset:416\n"
    "s_add_u32 m0, s34, 0\n"
    "v_mfma_f32_16x16x32_bf16 v[108:111], a[180:183], a[4:7], 0\n"
    "ds_read_b64_tr_b16 a[174:175], v227 offset:928\n"
    "s_add_u32 s35, s78, 384\n"
    "v_mfma_f32_16x16x32_bf16 v[116:119], a[180:183], a[8:11], 0\n"
    "s_waitcnt lgkmcnt(4)\n"
    "buffer_load_dwordx4 v239, s[44:47], s35 offen lds\n"
    "v_mfma_f32_16x16x32_bf16 v[120:123], a[184:187], a[0:3], 0\n"
    "ds_read_b64_tr_b16 a[176:177], v227 offset:448\n"
    "s_add_u32 m0, s34, 1056\n"
    "v_mfma_f32_16x16x32_bf16 v[128:131], a[184:187], a[4:7], 0\n"
    "ds_read_b64_tr_b16 a[178:179], v227 offset:960\n"
    "s_add_u32 s35, s76, 448\n"
    "v_mfma_f32_16x16x32_bf16 v[136:139], a[184:187], a[8:11], 0\n"
    "buffer_load_dwordx4 v239, s[44:47], s35 offen lds\n"
    "s_add_u32 m0, s34, 2112\n"
    "v_mfma_f32_16x16x32_bf16 v[124:127], a[188:191], a[0:3], 0\n"
    "ds_read_b64_tr_b16 a[180:181], v227 offset:480\n"
    "s_add_u32 s35, s77, 448\n"
    "v_mfma_f32_16x16x32_bf16 v[132:135], a[188:191], a[4:7], 0\n"
    "ds_read_b64_tr_b16 a[182:183], v227 offset:992\n"
    "v_mfma_f32_16x16x32_bf16 v[140:143], a[188:191], a[8:11], 0\n"
    "s_waitcnt lgkmcnt(4)\n"
    "s_waitcnt vmcnt(44)\n"
    "s_barrier\n"
    "v_add_u32 v227, s68, v225\n"
    "v_mfma_f32_16x16x32_bf16 v[144:147], a[168:171], a[0:3], 0\n"
    "ds_read_b64_tr_b16 a[184:185], v227\n"
    "s_add_u32 s68, s68, 16896\n"
    "v_mfma_f32_16x16x32_bf16 v[152:155], a[168:171], a[4:7], 0\n"
    "ds_read_b64_tr_b16 a[186:187], v227 offset:512\n"
    "v_mfma_f32_16x16x32_bf16 v[160:163], a[168:171], a[8:11], 0\n"
    "s_cmp_eq_u32 s68, 152064\n"
    "s_cselect_b32 s68, 0, s68\n"
    "v_mfma_f32_16x16x32_bf16 v[148:151], a[172:175], a[0:3], 0\n"
    "ds_read_b64_tr_b16 a[188:189], v227 offset:32\n"
    "v_mfma_f32_16x16x32_bf16 v[156:159], a[172:175], a[4:7], 0\n"
    "ds_read_b64_tr_b16 a[190:191], v227 offset:544\n"
    "v_mfma_f32_16x16x32_bf16 v[164:167], a[172:175], a[8:11], 0\n"
    "s_waitcnt lgkmcnt(4)\n"
    "buffer_load_dwordx4 v239, s[44:47], s35 offen lds\n"
    "v_mfma_f32_16x16x32_bf16 v[168:171], a[176:179], a[0:3], 0\n"
    "ds_read_b64_tr_b16 a[168:169], v227 offset:64\n"
    "s_add_u32 m0, s34, 3168\n"
    "v_mfma_f32_16x16x32_bf16 v[176:179], a[176:179], a[4:7], 0\n"
    "ds_read_b64_tr_b16 a[170:171], v227 offset:576\n"
    "s_add_u32 s35, s78, 448\n"
    "v_mfma_f32_16x16x32_bf16 v[184:187], a[176:179], a[8:11], 0\n"
    "buffer_load_dwordx4 v239, s[44:47], s35 offen lds\n"
    "v_mfma_f32_16x16x32_bf16 v[172:175], a[180:183], a[0:3], 0\n"
    "ds_read_b64_tr_b16 a[172:173], v227 offset:96\n"
    "v_mfma_f32_16x16x32_bf16 v[180:183], a[180:183], a[4:7], 0\n"
    "ds_read_b64_tr_b16 a[174:175], v227 offset:608\n"
    "v_mfma_f32_16x16x32_bf16 v[188:191], a[180:183], a[8:11], 0\n"
    "s_waitcnt lgkmcnt(4)\n"
    "v_mfma_f32_16x16x32_bf16 v[0:3], a[184:187], a[12:15], v[0:3]\n"
    "ds_read_b64_tr_b16 a[176:177], v227 offset:128\n"
    "v_mfma_f32_16x16x32_bf16 v[8:11], a[184:187], a[16:19], v[8:11]\n"
    "ds_read_b64_tr_b16 a[178:179], v227 offset:640\n"
    "v_add_u32 v226, s53, v252\n"
    "s_waitcnt vmcnt(14)\n"
    "v_mfma_f32_16x16x32_bf16 v[16:19], a[184:187], a[20:23], v[16:19]\n"
    "ds_read_b128 a[32:35], v226\n"
    "v_mfma_f32_16x16x32_bf16 v[4:7], a[188:191], a[12:15], v[4:7]\n"
    "ds_read_b64_tr_b16 a[180:181], v227 offset:160\n"
    "v_mfma_f32_16x16x32_bf16 v[12:15], a[188:191], a[16:19], v[12:15]\n"
    "ds_read_b64_tr_b16 a[182:183], v227 offset:672\n"
    "v_mfma_f32_16x16x32_bf16 v[20:23], a[188:191], a[20:23], v[20:23]\n"
    "s_waitcnt lgkmcnt(5)\n"
    "ds_read_b128 a[36:39], v226 offset:1056\n"
    "v_mfma_f32_16x16x32_bf16 v[24:27], a[168:171], a[12:15], v[24:27]\n"
    "ds_read_b64_tr_b16 a[184:185], v227 offset:192\n"
    "v_mfma_f32_16x16x32_bf16 v[32:35], a[168:171], a[16:19], v[32:35]\n"
    "ds_read_b64_tr_b16 a[186:187], v227 offset:704\n"
    "v_mfma_f32_16x16x32_bf16 v[40:43], a[168:171], a[20:23], v[40:43]\n"
    "ds_read_b128 a[40:43], v226 offset:2112\n"
    "v_mfma_f32_16x16x32_bf16 v[28:31], a[172:175], a[12:15], v[28:31]\n"
    "ds_read_b64_tr_b16 a[188:189], v227 offset:224\n"
    "v_mfma_f32_16x16x32_bf16 v[36:39], a[172:175], a[16:19], v[36:39]\n"
    "ds_read_b64_tr_b16 a[190:191], v227 offset:736\n"
    "v_mfma_f32_16x16x32_bf16 v[44:47], a[172:175], a[20:23], v[44:47]\n"
    "s_waitcnt lgkmcnt(6)\n"
    "ds_read_b128 a[44:47], v226 offset:3168\n"
    "v_mfma_f32_16x16x32_bf16 v[48:51], a[176:179], a[12:15], v[48:51]\n"
    "ds_read_b64_tr_b16 a[168:169], v227 offset:256\n"
    "v_add_u32 v226, s55, v252\n"
    "s_waitcnt vmcnt(10)\n"
    "v_mfma_f32_16x16x32_bf16 v[56:59], a[176:179], a[16:19], v[56:59]\n"
    "ds_read_b64_tr_b16 a[170:171], v227 offset:768\n"
    "v_mfma_f32_16x16x32_bf16 v[64:67], a[176:179], a[20:23], v[64:67]\n"
    "ds_read_b128 a[48:51], v226\n"
    "s_waitcnt lgkmcnt(3)\n"
    "v_mfma_f32_16x16x32_bf16 v[52:55], a[180:183], a[12:15], v[52:55]\n"
    "ds_read_b64_tr_b16 a[172:173], v227 offset:288\n"
    "s_mov_b32 s35, s73\n"
    "v_mfma_f32_16x16x32_bf16 v[60:63], a[180:183], a[16:19], v[60:63]\n"
    "ds_read_b64_tr_b16 a[174:175], v227 offset:800\n"
    "s_add_u32 m0, s69, s70\n"
    "v_mfma_f32_16x16x32_bf16 v[68:71], a[180:183], a[20:23], v[68:71]\n"
    "ds_read_b128 a[52:55], v226 offset:1056\n"
    "v_mfma_f32_16x16x32_bf16 v[72:75], a[184:187], a[12:15], v[72:75]\n"
    "ds_read_b64_tr_b16 a[176:177], v227 offset:320\n"
    "v_mfma_f32_16x16x32_bf16 v[80:83], a[184:187], a[16:19], v[80:83]\n"
    "ds_read_b64_tr_b16 a[178:179], v227 offset:832\n"
    "v_mfma_f32_16x16x32_bf16 v[88:91], a[184:187], a[20:23], v[88:91]\n"
    "ds_read_b128 a[56:59], v226 offset:2112\n"
    "v_mfma_f32_16x16x32_bf16 v[76:79], a[188:191], a[12:15], v[76:79]\n"
    "ds_read_b64_tr_b16 a[180:181], v227 offset:352\n"
    "v_mfma_f32_16x16x32_bf16 v[84:87], a[188:191], a[16:19], v[84:87]\n"
    "ds_read_b64_tr_b16 a[182:183], v227 offset:864\n"
    "v_mfma_f32_16x16x32_bf16 v[92:95], a[188:191], a[20:23], v[92:95]\n"
    "s_waitcnt lgkmcnt(6)\n"
    "ds_read_b128 a[60:63], v226 offset:3168\n"
    "v_mfma_f32_16x16x32_bf16 v[96:99], a[168:171], a[12:15], v[96:99]\n"
    "ds_read_b64_tr_b16 a[184:185], v227 offset:384\n"
    "v_mfma_f32_16x16x32_bf16 v[104:107], a[168:171], a[16:19], v[104:107]\n"
    "ds_read_b64_tr_b16 a[186:187], v227 offset:896\n"
    "v_mfma_f32_16x16x32_bf16 v[112:115], a[168:171], a[20:23], v[112:115]\n"
    "s_waitcnt lgkmcnt(2)\n"
    "buffer_load_dwordx4 v232, s[56:59], s35 offen lds\n"
    "v_mfma_f32_16x16x32_bf16 v[100:103], a[172:175], a[12:15], v[100:103]\n"
    "ds_read_b64_tr_b16 a[188:189], v227 offset:416\n"
    "v_mfma_f32_16x16x32_bf16 v[108:111], a[172:175], a[16:19], v[108:111]\n"
    "ds_read_b64_tr_b16 a[190:191], v227 offset:928\n"
    "v_mfma_f32_16x16x32_bf16 v[116:119], a[172:175], a[20:23], v[116:119]\n"
    "buffer_load_dwordx4 v233, s[56:59], s35 offen offset:1056 lds\n"
    "v_mfma_f32_16x16x32_bf16 v[120:123], a[176:179], a[12:15], v[120:123]\n"
    "ds_read_b64_tr_b16 a[168:169], v227 offset:448\n"
    "v_mfma_f32_16x16x32_bf16 v[128:131], a[176:179], a[16:19], v[128:131]\n"
    "ds_read_b64_tr_b16 a[170:171], v227 offset:960\n"
    "v_mfma_f32_16x16x32_bf16 v[136:139], a[176:179], a[20:23], v[136:139]\n"
    "buffer_load_dwordx4 v234, s[56:59], s35 offen offset:2112 lds\n"
    "v_mfma_f32_16x16x32_bf16 v[124:127], a[180:183], a[12:15], v[124:127]\n"
    "ds_read_b64_tr_b16 a[172:173], v227 offset:480\n"
    "v_mfma_f32_16x16x32_bf16 v[132:135], a[180:183], a[16:19], v[132:135]\n"
    "ds_read_b64_tr_b16 a[174:175], v227 offset:992\n"
    "v_mfma_f32_16x16x32_bf16 v[140:143], a[180:183], a[20:23], v[140:143]\n"
    "s_waitcnt lgkmcnt(4)\n"
    "s_barrier\n"
    "v_add_u32 v227, s68, v225\n"
    "v_mfma_f32_16x16x32_bf16 v[144:147], a[184:187], a[12:15], v[144:147]\n"
    "ds_read_b64_tr_b16 a[176:177], v227\n"
    "s_add_u32 s68, s68, 16896\n"
    "v_mfma_f32_16x16x32_bf16 v[152:155], a[184:187], a[16:19], v[152:155]\n"
    "ds_read_b64_tr_b16 a[178:179], v227 offset:512\n"
    "v_mfma_f32_16x16x32_bf16 v[160:163], a[184:187], a[20:23], v[160:163]\n"
    "s_cmp_eq_u32 s68, 152064\n"
    "s_cselect_b32 s68, 0, s68\n"
    "v_mfma_f32_16x16x32_bf16 v[148:151], a[188:191], a[12:15], v[148:151]\n"
    "ds_read_b64_tr_b16 a[180:181], v227 offset:32\n"
    "v_mfma_f32_16x16x32_bf16 v[156:159], a[188:191], a[16:19], v[156:159]\n"
    "ds_read_b64_tr_b16 a[182:183], v227 offset:544\n"
    "v_mfma_f32_16x16x32_bf16 v[164:167], a[188:191], a[20:23], v[164:167]\n"
    "s_waitcnt lgkmcnt(4)\n"
    "buffer_load_dwordx4 v235, s[56:59], s35 offen offset:3168 lds\n"
    "v_mfma_f32_16x16x32_bf16 v[168:171], a[168:171], a[12:15], v[168:171]\n"
    "ds_read_b64_tr_b16 a[184:185], v227 offset:64\n"
    "s_add_u32 s69, s69, 16896\n"
    "v_mfma_f32_16x16x32_bf16 v[176:179], a[168:171], a[16:19], v[176:179]\n"
    "ds_read_b64_tr_b16 a[186:187], v227 offset:576\n"
    "v_mfma_f32_16x16x32_bf16 v[184:187], a[168:171], a[20:23], v[184:187]\n"
    "s_cmp_eq_u32 s69, 152064\n"
    "s_cselect_b32 s69, 0, s69\n"
    "v_mfma_f32_16x16x32_bf16 v[172:175], a[172:175], a[12:15], v[172:175]\n"
    "ds_read_b64_tr_b16 a[188:189], v227 offset:96\n"
    "s_add_u32 s34, s55, s70\n"
    "v_mfma_f32_16x16x32_bf16 v[180:183], a[172:175], a[16:19], v[180:183]\n"
    "ds_read_b64_tr_b16 a[190:191], v227 offset:608\n"
    "s_add_u32 m0, s34, 0\n"
    "v_mfma_f32_16x16x32_bf16 v[188:191], a[172:175], a[20:23], v[188:191]\n"
    "s_waitcnt lgkmcnt(4)\n"
    "s_add_u32 s35, s76, 512\n"
    "buffer_load_dwordx4 v239, s[44:47], s35 offen lds\n"
    "s_add_u32 m0, s34, 1056\n"
    "s_add_u32 s35, s77, 512\n"
    "buffer_load_dwordx4 v239, s[44:47], s35 offen lds\n"
    "s_add_u32 m0, s34, 2112\n"
    "s_add_u32 s35, s78, 512\n"
    "buffer_load_dwordx4 v239, s[44:47], s35 offen lds\n"
    "s_add_u32 m0, s34, 3168\n"
    "s_add_u32 s35, s76, 576\n"
    "buffer_load_dwordx4 v239, s[44:47], s35 offen lds\n"
    "v_mfma_f32_16x16x32_bf16 v[0:3], a[176:179], a[24:27], v[0:3]\n"
    "ds_read_b64_tr_b16 a[168:169], v227 offset:128\n"
    "v_mfma_f32_16x16x32_bf16 v[8:11], a[176:179], a[28:31], v[8:11]\n"
    "ds_read_b64_tr_b16 a[170:171], v227 offset:640\n"
    "v_add_u32 v226, s33, v252\n"
    "s_waitcnt vmcnt(12)\n"
    "v_mfma_f32_16x16x32_bf16 v[16:19], a[176:179], a[32:35], v[16:19]\n"
    "ds_read_b128 a[64:67], v226\n"
    "s_add_u32 s35, s73, 524288\n"
    "v_mfma_f32_16x16x32_bf16 v[4:7], a[180:183], a[24:27], v[4:7]\n"
    "ds_read_b64_tr_b16 a[172:173], v227 offset:160\n"
    "s_add_u32 m0, s40, s70\n"
    "v_mfma_f32_16x16x32_bf16 v[12:15], a[180:183], a[28:31], v[12:15]\n"
    "ds_read_b64_tr_b16 a[174:175], v227 offset:672\n"
    "v_mfma_f32_16x16x32_bf16 v[20:23], a[180:183], a[32:35], v[20:23]\n"
    "s_waitcnt lgkmcnt(5)\n"
    "ds_read_b128 a[68:71], v226 offset:1056\n"
    "v_mfma_f32_16x16x32_bf16 v[24:27], a[184:187], a[24:27], v[24:27]\n"
    "ds_read_b64_tr_b16 a[176:177], v227 offset:192\n"
    "v_mfma_f32_16x16x32_bf16 v[32:35], a[184:187], a[28:31], v[32:35]\n"
    "ds_read_b64_tr_b16 a[178:179], v227 offset:704\n"
    "v_mfma_f32_16x16x32_bf16 v[40:43], a[184:187], a[32:35], v[40:43]\n"
    "ds_read_b128 a[72:75], v226 offset:2112\n"
    "v_mfma_f32_16x16x32_bf16 v[28:31], a[188:191], a[24:27], v[28:31]\n"
    "ds_read_b64_tr_b16 a[180:181], v227 offset:224\n"
    "v_mfma_f32_16x16x32_bf16 v[36:39], a[188:191], a[28:31], v[36:39]\n"
    "ds_read_b64_tr_b16 a[182:183], v227 offset:736\n"
    "v_mfma_f32_16x16x32_bf16 v[44:47], a[188:191], a[32:35], v[44:47]\n"
    "s_waitcnt lgkmcnt(6)\n"
    "ds_read_b128 a[76:79], v226 offset:3168\n"
    "v_mfma_f32_16x16x32_bf16 v[48:51], a[168:171], a[24:27], v[48:51]\n"
    "ds_read_b64_tr_b16 a[184:185], v227 offset:256\n"
    "v_mfma_f32_16x16x32_bf16 v[56:59], a[168:171], a[28:31], v[56:59]\n"
    "ds_read_b64_tr_b16 a[186:187], v227 offset:768\n"
    "v_mfma_f32_16x16x32_bf16 v[64:67], a[168:171], a[32:35], v[64:67]\n"
    "s_waitcnt lgkmcnt(2)\n"
    "buffer_load_dwordx4 v232, s[56:59], s35 offen lds\n"
    "v_mfma_f32_16x16x32_bf16 v[52:55], a[172:175], a[24:27], v[52:55]\n"
    "ds_read_b64_tr_b16 a[188:189], v227 offset:288\n"
    "v_mfma_f32_16x16x32_bf16 v[60:63], a[172:175], a[28:31], v[60:63]\n"
    "ds_read_b64_tr_b16 a[190:191], v227 offset:800\n"
    "v_mfma_f32_16x16x32_bf16 v[68:71], a[172:175], a[32:35], v[68:71]\n"
    "buffer_load_dwordx4 v233, s[56:59], s35 offen offset:1056 lds\n"
    "v_mfma_f32_16x16x32_bf16 v[72:75], a[176:179], a[24:27], v[72:75]\n"
    "ds_read_b64_tr_b16 a[168:169], v227 offset:320\n"
    "v_mfma_f32_16x16x32_bf16 v[80:83], a[176:179], a[28:31], v[80:83]\n"
    "ds_read_b64_tr_b16 a[170:171], v227 offset:832\n"
    "v_mfma_f32_16x16x32_bf16 v[88:91], a[176:179], a[32:35], v[88:91]\n"
    "buffer_load_dwordx4 v234, s[56:59], s35 offen offset:2112 lds\n"
    "v_mfma_f32_16x16x32_bf16 v[76:79], a[180:183], a[24:27], v[76:79]\n"
    "ds_read_b64_tr_b16 a[172:173], v227 offset:352\n"
    "v_mfma_f32_16x16x32_bf16 v[84:87], a[180:183], a[28:31], v[84:87]\n"
    "ds_read_b64_tr_b16 a[174:175], v227 offset:864\n"
    "v_mfma_f32_16x16x32_bf16 v[92:95], a[180:183], a[32:35], v[92:95]\n"
    "s_waitcnt lgkmcnt(4)\n"
    "buffer_load_dwordx4 v235, s[56:59], s35 offen offset:3168 lds\n"
    "v_mfma_f32_16x16x32_bf16 v[96:99], a[184:187], a[24:27], v[96:99]\n"
    "ds_read_b64_tr_b16 a[176:177], v227 offset:384\n"
    "s_add_u32 s34, s33, s70\n"
    "v_mfma_f32_16x16x32_bf16 v[104:107], a[184:187], a[28:31], v[104:107]\n"
    "ds_read_b64_tr_b16 a[178:179], v227 offset:896\n"
    "s_add_u32 m0, s34, 0\n"
    "v_mfma_f32_16x16x32_bf16 v[112:115], a[184:187], a[32:35], v[112:115]\n"
    "s_add_u32 s35, s77, 576\n"
    "v_mfma_f32_16x16x32_bf16 v[100:103], a[188:191], a[24:27], v[100:103]\n"
    "ds_read_b64_tr_b16 a[180:181], v227 offset:416\n"
    "v_mfma_f32_16x16x32_bf16 v[108:111], a[188:191], a[28:31], v[108:111]\n"
    "ds_read_b64_tr_b16 a[182:183], v227 offset:928\n"
    "v_mfma_f32_16x16x32_bf16 v[116:119], a[188:191], a[32:35], v[116:119]\n"
    "s_waitcnt lgkmcnt(4)\n"
    "buffer_load_dwordx4 v239, s[44:47], s35 offen lds\n"
    "v_mfma_f32_16x16x32_bf16 v[120:123], a[168:171], a[24:27], v[120:123]\n"
    "ds_read_b64_tr_b16 a[184:185], v227 offset:448\n"
    "s_add_u32 m0, s34, 1056\n"
    "v_mfma_f32_16x16x32_bf16 v[128:131], a[168:171], a[28:31], v[128:131]\n"
    "ds_read_b64_tr_b16 a[186:187], v227 offset:960\n"
    "s_add_u32 s35, s78, 576\n"
    "v_mfma_f32_16x16x32_bf16 v[136:139], a[168:171], a[32:35], v[136:139]\n"
    "buffer_load_dwordx4 v239, s[44:47], s35 offen lds\n"
    "s_add_u32 m0, s34, 2112\n"
    "v_mfma_f32_16x16x32_bf16 v[124:127], a[172:175], a[24:27], v[124:127]\n"
    "ds_read_b64_tr_b16 a[188:189], v227 offset:480\n"
    "s_add_u32 s35, s76, 640\n"
    "v_mfma_f32_16x16x32_bf16 v[132:135], a[172:175], a[28:31], v[132:135]\n"
    "ds_read_b64_tr_b16 a[190:191], v227 offset:992\n"
    "v_mfma_f32_16x16x32_bf16 v[140:143], a[172:175], a[32:35], v[140:143]\n"
    "s_waitcnt lgkmcnt(4)\n"
    "s_barrier\n"
    "v_add_u32 v227, s68, v225\n"
    "v_mfma_f32_16x16x32_bf16 v[144:147], a[176:179], a[24:27], v[144:147]\n"
    "ds_read_b64_tr_b16 a[168:169], v227\n"
    "s_add_u32 s68, s68, 16896\n"
    "v_mfma_f32_16x16x32_bf16 v[152:155], a[176:179], a[28:31], v[152:155]\n"
    "ds_read_b64_tr_b16 a[170:171], v227 offset:512\n"
    "v_mfma_f32_16x16x32_bf16 v[160:163], a[176:179], a[32:35], v[160:163]\n"
    "s_cmp_eq_u32 s68, 152064\n"
    "s_cselect_b32 s68, 0, s68\n"
    "v_mfma_f32_16x16x32_bf16 v[148:151], a[180:183], a[24:27], v[148:151]\n"
    "ds_read_b64_tr_b16 a[172:173], v227 offset:32\n"
    "v_mfma_f32_16x16x32_bf16 v[156:159], a[180:183], a[28:31], v[156:159]\n"
    "ds_read_b64_tr_b16 a[174:175], v227 offset:544\n"
    "v_mfma_f32_16x16x32_bf16 v[164:167], a[180:183], a[32:35], v[164:167]\n"
    "s_waitcnt lgkmcnt(4)\n"
    "buffer_load_dwordx4 v239, s[44:47], s35 offen lds\n"
    "v_mfma_f32_16x16x32_bf16 v[168:171], a[184:187], a[24:27], v[168:171]\n"
    "ds_read_b64_tr_b16 a[176:177], v227 offset:64\n"
    "s_add_u32 m0, s34, 3168\n"
    "v_mfma_f32_16x16x32_bf16 v[176:179], a[184:187], a[28:31], v[176:179]\n"
    "ds_read_b64_tr_b16 a[178:179], v227 offset:576\n"
    "s_add_u32 s35, s77, 640\n"
    "v_mfma_f32_16x16x32_bf16 v[184:187], a[184:187], a[32:35], v[184:187]\n"
    "buffer_load_dwordx4 v239, s[44:47], s35 offen lds\n"
    "v_mfma_f32_16x16x32_bf16 v[172:175], a[188:191], a[24:27], v[172:175]\n"
    "ds_read_b64_tr_b16 a[180:181], v227 offset:96\n"
    "v_mfma_f32_16x16x32_bf16 v[180:183], a[188:191], a[28:31], v[180:183]\n"
    "ds_read_b64_tr_b16 a[182:183], v227 offset:608\n"
    "v_mfma_f32_16x16x32_bf16 v[188:191], a[188:191], a[32:35], v[188:191]\n"
    "s_waitcnt lgkmcnt(4)\n"
    "v_mfma_f32_16x16x32_bf16 v[0:3], a[168:171], a[36:39], v[0:3]\n"
    "ds_read_b64_tr_b16 a[184:185], v227 offset:128\n"
    "v_mfma_f32_16x16x32_bf16 v[8:11], a[168:171], a[40:43], v[8:11]\n"
    "ds_read_b64_tr_b16 a[186:187], v227 offset:640\n"
    "v_add_u32 v226, s54, v252\n"
    "s_waitcnt vmcnt(16)\n"
    "v_mfma_f32_16x16x32_bf16 v[16:19], a[168:171], a[44:47], v[16:19]\n"
    "ds_read_b128 a[80:83], v226\n"
    "s_add_u32 s35, s73, 786432\n"
    "v_mfma_f32_16x16x32_bf16 v[4:7], a[172:175], a[36:39], v[4:7]\n"
    "ds_read_b64_tr_b16 a[188:189], v227 offset:160\n"
    "s_add_u32 m0, s41, s70\n"
    "v_mfma_f32_16x16x32_bf16 v[12:15], a[172:175], a[40:43], v[12:15]\n"
    "ds_read_b64_tr_b16 a[190:191], v227 offset:672\n"
    "v_mfma_f32_16x16x32_bf16 v[20:23], a[172:175], a[44:47], v[20:23]\n"
    "s_waitcnt lgkmcnt(5)\n"
    "ds_read_b128 a[84:87], v226 offset:1056\n"
    "v_mfma_f32_16x16x32_bf16 v[24:27], a[176:179], a[36:39], v[24:27]\n"
    "ds_read_b64_tr_b16 a[168:169], v227 offset:192\n"
    "v_mfma_f32_16x16x32_bf16 v[32:35], a[176:179], a[40:43], v[32:35]\n"
    "ds_read_b64_tr_b16 a[170:171], v227 offset:704\n"
    "v_mfma_f32_16x16x32_bf16 v[40:43], a[176:179], a[44:47], v[40:43]\n"
    "ds_read_b128 a[88:91], v226 offset:2112\n"
    "v_mfma_f32_16x16x32_bf16 v[28:31], a[180:183], a[36:39], v[28:31]\n"
    "ds_read_b64_tr_b16 a[172:173], v227 offset:224\n"
    "v_mfma_f32_16x16x32_bf16 v[36:39], a[180:183], a[40:43], v[36:39]\n"
    "ds_read_b64_tr_b16 a[174:175], v227 offset:736\n"
    "v_mfma_f32_16x16x32_bf16 v[44:47], a[180:183], a[44:47], v[44:47]\n"
    "s_waitcnt lgkmcnt(6)\n"
    "ds_read_b128 a[92:95], v226 offset:3168\n"
    "v_mfma_f32_16x16x32_bf16 v[48:51], a[184:187], a[36:39], v[48:51]\n"
    "ds_read_b64_tr_b16 a[176:177], v227 offset:256\n"
    "v_add_u32 v226, s55, v252\n"
    "s_waitcnt vmcnt(8)\n"
    "v_mfma_f32_16x16x32_bf16 v[56:59], a[184:187], a[40:43], v[56:59]\n"
    "ds_read_b64_tr_b16 a[178:179], v227 offset:768\n"
    "v_mfma_f32_16x16x32_bf16 v[64:67], a[184:187], a[44:47], v[64:67]\n"
    "ds_read_b128 a[96:99], v226\n"
    "s_waitcnt lgkmcnt(3)\n"
    "v_mfma_f32_16x16x32_bf16 v[52:55], a[188:191], a[36:39], v[52:55]\n"
    "ds_read_b64_tr_b16 a[180:181], v227 offset:288\n"
    "v_mfma_f32_16x16x32_bf16 v[60:63], a[188:191], a[40:43], v[60:63]\n"
    "ds_read_b64_tr_b16 a[182:183], v227 offset:800\n"
    "v_mfma_f32_16x16x32_bf16 v[68:71], a[188:191], a[44:47], v[68:71]\n"
    "ds_read_b128 a[100:103], v226 offset:1056\n"
    "v_mfma_f32_16x16x32_bf16 v[72:75], a[168:171], a[36:39], v[72:75]\n"
    "ds_read_b64_tr_b16 a[184:185], v227 offset:320\n"
    "v_mfma_f32_16x16x32_bf16 v[80:83], a[168:171], a[40:43], v[80:83]\n"
    "ds_read_b64_tr_b16 a[186:187], v227 offset:832\n"
    "v_mfma_f32_16x16x32_bf16 v[88:91], a[168:171], a[44:47], v[88:91]\n"
    "ds_read_b128 a[104:107], v226 offset:2112\n"
    "v_mfma_f32_16x16x32_bf16 v[76:79], a[172:175], a[36:39], v[76:79]\n"
    "ds_read_b64_tr_b16 a[188:189], v227 offset:352\n"
    "v_mfma_f32_16x16x32_bf16 v[84:87], a[172:175], a[40:43], v[84:87]\n"
    "ds_read_b64_tr_b16 a[190:191], v227 offset:864\n"
    "v_mfma_f32_16x16x32_bf16 v[92:95], a[172:175], a[44:47], v[92:95]\n"
    "s_waitcnt lgkmcnt(6)\n"
    "ds_read_b128 a[108:111], v226 offset:3168\n"
    "v_mfma_f32_16x16x32_bf16 v[96:99], a[176:179], a[36:39], v[96:99]\n"
    "ds_read_b64_tr_b16 a[168:169], v227 offset:384\n"
    "v_mfma_f32_16x16x32_bf16 v[104:107], a[176:179], a[40:43], v[104:107]\n"
    "ds_read_b64_tr_b16 a[170:171], v227 offset:896\n"
    "v_mfma_f32_16x16x32_bf16 v[112:115], a[176:179], a[44:47], v[112:115]\n"
    "s_waitcnt lgkmcnt(2)\n"
    "buffer_load_dwordx4 v232, s[56:59], s35 offen lds\n"
    "v_mfma_f32_16x16x32_bf16 v[100:103], a[180:183], a[36:39], v[100:103]\n"
    "ds_read_b64_tr_b16 a[172:173], v227 offset:416\n"
    "v_mfma_f32_16x16x32_bf16 v[108:111], a[180:183], a[40:43], v[108:111]\n"
    "ds_read_b64_tr_b16 a[174:175], v227 offset:928\n"
    "v_mfma_f32_16x16x32_bf16 v[116:119], a[180:183], a[44:47], v[116:119]\n"
    "buffer_load_dwordx4 v233, s[56:59], s35 offen offset:1056 lds\n"
    "v_mfma_f32_16x16x32_bf16 v[120:123], a[184:187], a[36:39], v[120:123]\n"
    "ds_read_b64_tr_b16 a[176:177], v227 offset:448\n"
    "v_mfma_f32_16x16x32_bf16 v[128:131], a[184:187], a[40:43], v[128:131]\n"
    "ds_read_b64_tr_b16 a[178:179], v227 offset:960\n"
    "v_mfma_f32_16x16x32_bf16 v[136:139], a[184:187], a[44:47], v[136:139]\n"
    "buffer_load_dwordx4 v234, s[56:59], s35 offen offset:2112 lds\n"
    "v_mfma_f32_16x16x32_bf16 v[124:127], a[188:191], a[36:39], v[124:127]\n"
    "ds_read_b64_tr_b16 a[180:181], v227 offset:480\n"
    "v_mfma_f32_16x16x32_bf16 v[132:135], a[188:191], a[40:43], v[132:135]\n"
    "ds_read_b64_tr_b16 a[182:183], v227 offset:992\n"
    "v_mfma_f32_16x16x32_bf16 v[140:143], a[188:191], a[44:47], v[140:143]\n"
    "s_waitcnt lgkmcnt(4)\n"
    "s_barrier\n"
    "v_add_u32 v227, s68, v225\n"
    "v_mfma_f32_16x16x32_bf16 v[144:147], a[168:171], a[36:39], v[144:147]\n"
    "ds_read_b64_tr_b16 a[184:185], v227\n"
    "s_add_u32 s68, s68, 16896\n"
    "v_mfma_f32_16x16x32_bf16 v[152:155], a[168:171], a[40:43], v[152:155]\n"
    "ds_read_b64_tr_b16 a[186:187], v227 offset:512\n"
    "v_mfma_f32_16x16x32_bf16 v[160:163], a[168:171], a[44:47], v[160:163]\n"
    "s_cmp_eq_u32 s68, 152064\n"
    "s_cselect_b32 s68, 0, s68\n"
    "v_mfma_f32_16x16x32_bf16 v[148:151], a[172:175], a[36:39], v[148:151]\n"
    "ds_read_b64_tr_b16 a[188:189], v227 offset:32\n"
    "v_mfma_f32_16x16x32_bf16 v[156:159], a[172:175], a[40:43], v[156:159]\n"
    "ds_read_b64_tr_b16 a[190:191], v227 offset:544\n"
    "v_mfma_f32_16x16x32_bf16 v[164:167], a[172:175], a[44:47], v[164:167]\n"
    "s_waitcnt lgkmcnt(4)\n"
    "buffer_load_dwordx4 v235, s[56:59], s35 offen offset:3168 lds\n"
    "v_mfma_f32_16x16x32_bf16 v[168:171], a[176:179], a[36:39], v[168:171]\n"
    "ds_read_b64_tr_b16 a[168:169], v227 offset:64\n"
    "s_add_u32 s35, s73, 262144\n"
    "v_mfma_f32_16x16x32_bf16 v[176:179], a[176:179], a[40:43], v[176:179]\n"
    "ds_read_b64_tr_b16 a[170:171], v227 offset:576\n"
    "s_add_u32 m0, s69, s70\n"
    "v_mfma_f32_16x16x32_bf16 v[184:187], a[176:179], a[44:47], v[184:187]\n"
    "buffer_load_dwordx4 v232, s[56:59], s35 offen lds\n"
    "v_mfma_f32_16x16x32_bf16 v[172:175], a[180:183], a[36:39], v[172:175]\n"
    "ds_read_b64_tr_b16 a[172:173], v227 offset:96\n"
    "v_mfma_f32_16x16x32_bf16 v[180:183], a[180:183], a[40:43], v[180:183]\n"
    "ds_read_b64_tr_b16 a[174:175], v227 offset:608\n"
    "v_mfma_f32_16x16x32_bf16 v[188:191], a[180:183], a[44:47], v[188:191]\n"
    "s_waitcnt lgkmcnt(4)\n"
    "buffer_load_dwordx4 v233, s[56:59], s35 offen offset:1056 lds\n"
    "buffer_load_dwordx4 v234, s[56:59], s35 offen offset:2112 lds\n"
    "buffer_load_dwordx4 v235, s[56:59], s35 offen offset:3168 lds\n"
    "s_add_u32 s69, s69, 16896\n"
    "s_cmp_eq_u32 s69, 152064\n"
    "s_cselect_b32 s69, 0, s69\n"
    "s_add_u32 s34, s55, s70\n"
    "s_add_u32 m0, s34, 0\n"
    "s_add_u32 s35, s78, 640\n"
    "buffer_load_dwordx4 v239, s[44:47], s35 offen lds\n"
    "s_add_u32 m0, s34, 1056\n"
    "s_add_u32 s35, s76, 704\n"
    "buffer_load_dwordx4 v239, s[44:47], s35 offen lds\n"
    "s_add_u32 m0, s34, 2112\n"
    "s_add_u32 s35, s77, 704\n"
    "buffer_load_dwordx4 v239, s[44:47], s35 offen lds\n"
    "s_add_u32 m0, s34, 3168\n"
    "s_add_u32 s35, s78, 704\n"
    "buffer_load_dwordx4 v239, s[44:47], s35 offen lds\n"
    "v_mfma_f32_16x16x32_bf16 v[0:3], a[184:187], a[48:51], v[0:3]\n"
    "ds_read_b64_tr_b16 a[176:177], v227 offset:128\n"
    "v_mfma_f32_16x16x32_bf16 v[8:11], a[184:187], a[52:55], v[8:11]\n"
    "ds_read_b64_tr_b16 a[178:179], v227 offset:640\n"
    "v_add_u32 v226, s33, v252\n"
    "s_waitcnt vmcnt(12)\n"
    "v_mfma_f32_16x16x32_bf16 v[16:19], a[184:187], a[56:59], v[16:19]\n"
    "ds_read_b128 a[112:115], v226\n"
    "s_add_u32 s35, s73, 1048576\n"
    "v_mfma_f32_16x16x32_bf16 v[4:7], a[188:191], a[48:51], v[4:7]\n"
    "ds_read_b64_tr_b16 a[180:181], v227 offset:160\n"
    "s_add_u32 m0, s42, s70\n"
    "v_mfma_f32_16x16x32_bf16 v[12:15], a[188:191], a[52:55], v[12:15]\n"
    "ds_read_b64_tr_b16 a[182:183], v227 offset:672\n"
    "v_mfma_f32_16x16x32_bf16 v[20:23], a[188:191], a[56:59], v[20:23]\n"
    "s_waitcnt lgkmcnt(5)\n"
    "ds_read_b128 a[116:119], v226 offset:1056\n"
    "v_mfma_f32_16x16x32_bf16 v[24:27], a[168:171], a[48:51], v[24:27]\n"
    "ds_read_b64_tr_b16 a[184:185], v227 offset:192\n"
    "v_mfma_f32_16x16x32_bf16 v[32:35], a[168:171], a[52:55], v[32:35]\n"
    "ds_read_b64_tr_b16 a[186:187], v227 offset:704\n"
    "v_mfma_f32_16x16x32_bf16 v[40:43], a[168:171], a[56:59], v[40:43]\n"
    "ds_read_b128 a[120:123], v226 offset:2112\n"
    "v_mfma_f32_16x16x32_bf16 v[28:31], a[172:175], a[48:51], v[28:31]\n"
    "ds_read_b64_tr_b16 a[188:189], v227 offset:224\n"
    "v_mfma_f32_16x16x32_bf16 v[36:39], a[172:175], a[52:55], v[36:39]\n"
    "ds_read_b64_tr_b16 a[190:191], v227 offset:736\n"
    "v_mfma_f32_16x16x32_bf16 v[44:47], a[172:175], a[56:59], v[44:47]\n"
    "s_waitcnt lgkmcnt(6)\n"
    "ds_read_b128 a[124:127], v226 offset:3168\n"
    "v_mfma_f32_16x16x32_bf16 v[48:51], a[176:179], a[48:51], v[48:51]\n"
    "ds_read_b64_tr_b16 a[168:169], v227 offset:256\n"
    "v_mfma_f32_16x16x32_bf16 v[56:59], a[176:179], a[52:55], v[56:59]\n"
    "ds_read_b64_tr_b16 a[170:171], v227 offset:768\n"
    "v_mfma_f32_16x16x32_bf16 v[64:67], a[176:179], a[56:59], v[64:67]\n"
    "s_waitcnt lgkmcnt(2)\n"
    "buffer_load_dwordx4 v232, s[56:59], s35 offen lds\n"
    "v_mfma_f32_16x16x32_bf16 v[52:55], a[180:183], a[48:51], v[52:55]\n"
    "ds_read_b64_tr_b16 a[172:173], v227 offset:288\n"
    "v_mfma_f32_16x16x32_bf16 v[60:63], a[180:183], a[52:55], v[60:63]\n"
    "ds_read_b64_tr_b16 a[174:175], v227 offset:800\n"
    "v_mfma_f32_16x16x32_bf16 v[68:71], a[180:183], a[56:59], v[68:71]\n"
    "buffer_load_dwordx4 v233, s[56:59], s35 offen offset:1056 lds\n"
    "v_mfma_f32_16x16x32_bf16 v[72:75], a[184:187], a[48:51], v[72:75]\n"
    "ds_read_b64_tr_b16 a[176:177], v227 offset:320\n"
    "v_mfma_f32_16x16x32_bf16 v[80:83], a[184:187], a[52:55], v[80:83]\n"
    "ds_read_b64_tr_b16 a[178:179], v227 offset:832\n"
    "v_mfma_f32_16x16x32_bf16 v[88:91], a[184:187], a[56:59], v[88:91]\n"
    "buffer_load_dwordx4 v234, s[56:59], s35 offen offset:2112 lds\n"
    "v_mfma_f32_16x16x32_bf16 v[76:79], a[188:191], a[48:51], v[76:79]\n"
    "ds_read_b64_tr_b16 a[180:181], v227 offset:352\n"
    "v_mfma_f32_16x16x32_bf16 v[84:87], a[188:191], a[52:55], v[84:87]\n"
    "ds_read_b64_tr_b16 a[182:183], v227 offset:864\n"
    "v_mfma_f32_16x16x32_bf16 v[92:95], a[188:191], a[56:59], v[92:95]\n"
    "s_waitcnt lgkmcnt(4)\n"
    "buffer_load_dwordx4 v235, s[56:59], s35 offen offset:3168 lds\n"
    "v_mfma_f32_16x16x32_bf16 v[96:99], a[168:171], a[48:51], v[96:99]\n"
    "ds_read_b64_tr_b16 a[184:185], v227 offset:384\n"
    "s_add_u32 s34, s33, s70\n"
    "v_mfma_f32_16x16x32_bf16 v[104:107], a[168:171], a[52:55], v[104:107]\n"
    "ds_read_b64_tr_b16 a[186:187], v227 offset:896\n"
    "s_add_u32 m0, s34, 0\n"
    "v_mfma_f32_16x16x32_bf16 v[112:115], a[168:171], a[56:59], v[112:115]\n"
    "s_add_u32 s35, s76, 768\n"
    "v_mfma_f32_16x16x32_bf16 v[100:103], a[172:175], a[48:51], v[100:103]\n"
    "ds_read_b64_tr_b16 a[188:189], v227 offset:416\n"
    "v_mfma_f32_16x16x32_bf16 v[108:111], a[172:175], a[52:55], v[108:111]\n"
    "ds_read_b64_tr_b16 a[190:191], v227 offset:928\n"
    "v_mfma_f32_16x16x32_bf16 v[116:119], a[172:175], a[56:59], v[116:119]\n"
    "s_waitcnt lgkmcnt(4)\n"
    "buffer_load_dwordx4 v239, s[44:47], s35 offen lds\n"
    "v_mfma_f32_16x16x32_bf16 v[120:123], a[176:179], a[48:51], v[120:123]\n"
    "ds_read_b64_tr_b16 a[168:169], v227 offset:448\n"
    "s_add_u32 m0, s34, 1056\n"
    "v_mfma_f32_16x16x32_bf16 v[128:131], a[176:179], a[52:55], v[128:131]\n"
    "ds_read_b64_tr_b16 a[170:171], v227 offset:960\n"
    "s_add_u32 s35, s77, 768\n"
    "v_mfma_f32_16x16x32_bf16 v[136:139], a[176:179], a[56:59], v[136:139]\n"
    "buffer_load_dwordx4 v239, s[44:47], s35 offen lds\n"
    "s_add_u32 m0, s34, 2112\n"
    "v_mfma_f32_16x16x32_bf16 v[124:127], a[180:183], a[48:51], v[124:127]\n"
    "ds_read_b64_tr_b16 a[172:173], v227 offset:480\n"
    "s_add_u32 s35, s78, 768\n"
    "v_mfma_f32_16x16x32_bf16 v[132:135], a[180:183], a[52:55], v[132:135]\n"
    "ds_read_b64_tr_b16 a[174:175], v227 offset:992\n"
    "v_mfma_f32_16x16x32_bf16 v[140:143], a[180:183], a[56:59], v[140:143]\n"
    "s_waitcnt lgkmcnt(4)\n"
    "s_barrier\n"
    "v_add_u32 v227, s68, v225\n"
    "v_mfma_f32_16x16x32_bf16 v[144:147], a[184:187], a[48:51], v[144:147]\n"
    "ds_read_b64_tr_b16 a[176:177], v227\n"
    "s_add_u32 s68, s68, 16896\n"
    "v_mfma_f32_16x16x32_bf16 v[152:155], a[184:187], a[52:55], v[152:155]\n"
    "ds_read_b64_tr_b16 a[178:179], v227 offset:512\n"
    "v_mfma_f32_16x16x32_bf16 v[160:163], a[184:187], a[56:59], v[160:163]\n"
    "s_cmp_eq_u32 s68, 152064\n"
    "s_cselect_b32 s68, 0, s68\n"
    "v_mfma_f32_16x16x32_bf16 v[148:151], a[188:191], a[48:51], v[148:151]\n"
    "ds_read_b64_tr_b16 a[180:181], v227 offset:32\n"
    "v_mfma_f32_16x16x32_bf16 v[156:159], a[188:191], a[52:55], v[156:159]\n"
    "ds_read_b64_tr_b16 a[182:183], v227 offset:544\n"
    "v_mfma_f32_16x16x32_bf16 v[164:167], a[188:191], a[56:59], v[164:167]\n"
    "s_waitcnt lgkmcnt(4)\n"
    "buffer_load_dwordx4 v239, s[44:47], s35 offen lds\n"
    "v_mfma_f32_16x16x32_bf16 v[168:171], a[168:171], a[48:51], v[168:171]\n"
    "ds_read_b64_tr_b16 a[184:185], v227 offset:64\n"
    "s_add_u32 m0, s34, 3168\n"
    "v_mfma_f32_16x16x32_bf16 v[176:179], a[168:171], a[52:55], v[176:179]\n"
    "ds_read_b64_tr_b16 a[186:187], v227 offset:576\n"
    "s_add_u32 s35, s76, 832\n"
    "v_mfma_f32_16x16x32_bf16 v[184:187], a[168:171], a[56:59], v[184:187]\n"
    "buffer_load_dwordx4 v239, s[44:47], s35 offen lds\n"
    "v_mfma_f32_16x16x32_bf16 v[172:175], a[172:175], a[48:51], v[172:175]\n"
    "ds_read_b64_tr_b16 a[188:189], v227 offset:96\n"
    "v_mfma_f32_16x16x32_bf16 v[180:183], a[172:175], a[52:55], v[180:183]\n"
    "ds_read_b64_tr_b16 a[190:191], v227 offset:608\n"
    "v_mfma_f32_16x16x32_bf16 v[188:191], a[172:175], a[56:59], v[188:191]\n"
    "s_waitcnt lgkmcnt(4)\n"
    "v_mfma_f32_16x16x32_bf16 v[0:3], a[176:179], a[60:63], v[0:3]\n"
    "ds_read_b64_tr_b16 a[168:169], v227 offset:128\n"
    "v_mfma_f32_16x16x32_bf16 v[8:11], a[176:179], a[64:67], v[8:11]\n"
    "ds_read_b64_tr_b16 a[170:171], v227 offset:640\n"
    "v_add_u32 v226, s55, v252\n"
    "s_waitcnt vmcnt(8)\n"
    "v_mfma_f32_16x16x32_bf16 v[16:19], a[176:179], a[68:71], v[16:19]\n"
    "ds_read_b128 a[128:131], v226\n"
    "s_add_u32 s35, s73, 1310720\n"
    "v_mfma_f32_16x16x32_bf16 v[4:7], a[180:183], a[60:63], v[4:7]\n"
    "ds_read_b64_tr_b16 a[172:173], v227 offset:160\n"
    "s_add_u32 m0, s43, s70\n"
    "v_mfma_f32_16x16x32_bf16 v[12:15], a[180:183], a[64:67], v[12:15]\n"
    "ds_read_b64_tr_b16 a[174:175], v227 offset:672\n"
    "v_mfma_f32_16x16x32_bf16 v[20:23], a[180:183], a[68:71], v[20:23]\n"
    "s_waitcnt lgkmcnt(5)\n"
    "ds_read_b128 a[132:135], v226 offset:1056\n"
    "v_mfma_f32_16x16x32_bf16 v[24:27], a[184:187], a[60:63], v[24:27]\n"
    "ds_read_b64_tr_b16 a[176:177], v227 offset:192\n"
    "v_mfma_f32_16x16x32_bf16 v[32:35], a[184:187], a[64:67], v[32:35]\n"
    "ds_read_b64_tr_b16 a[178:179], v227 offset:704\n"
    "v_mfma_f32_16x16x32_bf16 v[40:43], a[184:187], a[68:71], v[40:43]\n"
    "ds_read_b128 a[136:139], v226 offset:2112\n"
    "v_mfma_f32_16x16x32_bf16 v[28:31], a[188:191], a[60:63], v[28:31]\n"
    "ds_read_b64_tr_b16 a[180:181], v227 offset:224\n"
    "v_mfma_f32_16x16x32_bf16 v[36:39], a[188:191], a[64:67], v[36:39]\n"
    "ds_read_b64_tr_b16 a[182:183], v227 offset:736\n"
    "v_mfma_f32_16x16x32_bf16 v[44:47], a[188:191], a[68:71], v[44:47]\n"
    "s_waitcnt lgkmcnt(6)\n"
    "ds_read_b128 a[140:143], v226 offset:3168\n"
    "v_mfma_f32_16x16x32_bf16 v[48:51], a[168:171], a[60:63], v[48:51]\n"
    "ds_read_b64_tr_b16 a[184:185], v227 offset:256\n"
    "v_mfma_f32_16x16x32_bf16 v[56:59], a[168:171], a[64:67], v[56:59]\n"
    "ds_read_b64_tr_b16 a[186:187], v227 offset:768\n"
    "v_mfma_f32_16x16x32_bf16 v[64:67], a[168:171], a[68:71], v[64:67]\n"
    "s_waitcnt lgkmcnt(2)\n"
    "buffer_load_dwordx4 v232, s[56:59], s35 offen lds\n"
    "v_mfma_f32_16x16x32_bf16 v[52:55], a[172:175], a[60:63], v[52:55]\n"
    "ds_read_b64_tr_b16 a[188:189], v227 offset:288\n"
    "v_mfma_f32_16x16x32_bf16 v[60:63], a[172:175], a[64:67], v[60:63]\n"
    "ds_read_b64_tr_b16 a[190:191], v227 offset:800\n"
    "v_mfma_f32_16x16x32_bf16 v[68:71], a[172:175], a[68:71], v[68:71]\n"
    "buffer_load_dwordx4 v233, s[56:59], s35 offen offset:1056 lds\n"
    "v_mfma_f32_16x16x32_bf16 v[72:75], a[176:179], a[60:63], v[72:75]\n"
    "ds_read_b64_tr_b16 a[168:169], v227 offset:320\n"
    "v_mfma_f32_16x16x32_bf16 v[80:83], a[176:179], a[64:67], v[80:83]\n"
    "ds_read_b64_tr_b16 a[170:171], v227 offset:832\n"
    "v_mfma_f32_16x16x32_bf16 v[88:91], a[176:179], a[68:71], v[88:91]\n"
    "buffer_load_dwordx4 v234, s[56:59], s35 offen offset:2112 lds\n"
    "v_mfma_f32_16x16x32_bf16 v[76:79], a[180:183], a[60:63], v[76:79]\n"
    "ds_read_b64_tr_b16 a[172:173], v227 offset:352\n"
    "v_mfma_f32_16x16x32_bf16 v[84:87], a[180:183], a[64:67], v[84:87]\n"
    "ds_read_b64_tr_b16 a[174:175], v227 offset:864\n"
    "v_mfma_f32_16x16x32_bf16 v[92:95], a[180:183], a[68:71], v[92:95]\n"
    "s_waitcnt lgkmcnt(4)\n"
    "buffer_load_dwordx4 v235, s[56:59], s35 offen offset:3168 lds\n"
    "v_mfma_f32_16x16x32_bf16 v[96:99], a[184:187], a[60:63], v[96:99]\n"
    "ds_read_b64_tr_b16 a[176:177], v227 offset:384\n"
    "s_add_u32 m0, s69, s70\n"
    "v_mfma_f32_16x16x32_bf16 v[104:107], a[184:187], a[64:67], v[104:107]\n"
    "ds_read_b64_tr_b16 a[178:179], v227 offset:896\n"
    "v_mfma_f32_16x16x32_bf16 v[112:115], a[184:187], a[68:71], v[112:115]\n"
    "buffer_load_dwordx4 v228, s[24:27], s71 offen nt lds\n"
    "v_mfma_f32_16x16x32_bf16 v[100:103], a[188:191], a[60:63], v[100:103]\n"
    "ds_read_b64_tr_b16 a[180:181], v227 offset:416\n"
    "v_mfma_f32_16x16x32_bf16 v[108:111], a[188:191], a[64:67], v[108:111]\n"
    "ds_read_b64_tr_b16 a[182:183], v227 offset:928\n"
    "v_mfma_f32_16x16x32_bf16 v[116:119], a[188:191], a[68:71], v[116:119]\n"
    "s_waitcnt lgkmcnt(4)\n"
    "buffer_load_dwordx4 v229, s[24:27], s71 offen offset:1056 nt lds\n"
    "v_mfma_f32_16x16x32_bf16 v[120:123], a[168:171], a[60:63], v[120:123]\n"
    "ds_read_b64_tr_b16 a[184:185], v227 offset:448\n"
    "v_mfma_f32_16x16x32_bf16 v[128:131], a[168:171], a[64:67], v[128:131]\n"
    "ds_read_b64_tr_b16 a[186:187], v227 offset:960\n"
    "v_mfma_f32_16x16x32_bf16 v[136:139], a[168:171], a[68:71], v[136:139]\n"
    "buffer_load_dwordx4 v230, s[24:27], s71 offen offset:2112 nt lds\n"
    "v_mfma_f32_16x16x32_bf16 v[124:127], a[172:175], a[60:63], v[124:127]\n"
    "ds_read_b64_tr_b16 a[188:189], v227 offset:480\n"
    "v_mfma_f32_16x16x32_bf16 v[132:135], a[172:175], a[64:67], v[132:135]\n"
    "ds_read_b64_tr_b16 a[190:191], v227 offset:992\n"
    "v_mfma_f32_16x16x32_bf16 v[140:143], a[172:175], a[68:71], v[140:143]\n"
    "s_waitcnt lgkmcnt(4)\n"
    "s_barrier\n"
    "v_add_u32 v227, s68, v225\n"
    "v_mfma_f32_16x16x32_bf16 v[144:147], a[176:179], a[60:63], v[144:147]\n"
    "ds_read_b64_tr_b16 a[168:169], v227\n"
    "s_add_u32 s68, s68, 16896\n"
    "v_mfma_f32_16x16x32_bf16 v[152:155], a[176:179], a[64:67], v[152:155]\n"
    "ds_read_b64_tr_b16 a[170:171], v227 offset:512\n"
    "v_mfma_f32_16x16x32_bf16 v[160:163], a[176:179], a[68:71], v[160:163]\n"
    "s_cmp_eq_u32 s68, 152064\n"
    "s_cselect_b32 s68, 0, s68\n"
    "v_mfma_f32_16x16x32_bf16 v[148:151], a[180:183], a[60:63], v[148:151]\n"
    "ds_read_b64_tr_b16 a[172:173], v227 offset:32\n"
    "v_mfma_f32_16x16x32_bf16 v[156:159], a[180:183], a[64:67], v[156:159]\n"
    "ds_read_b64_tr_b16 a[174:175], v227 offset:544\n"
    "v_mfma_f32_16x16x32_bf16 v[164:167], a[180:183], a[68:71], v[164:167]\n"
    "s_waitcnt lgkmcnt(4)\n"
    "buffer_load_dwordx4 v231, s[24:27], s71 offen offset:3168 nt lds\n"
    "v_mfma_f32_16x16x32_bf16 v[168:171], a[184:187], a[60:63], v[168:171]\n"
    "ds_read_b64_tr_b16 a[176:177], v227 offset:64\n"
    "s_add_u32 s69, s69, 16896\n"
    "v_mfma_f32_16x16x32_bf16 v[176:179], a[184:187], a[64:67], v[176:179]\n"
    "ds_read_b64_tr_b16 a[178:179], v227 offset:576\n"
    "v_mfma_f32_16x16x32_bf16 v[184:187], a[184:187], a[68:71], v[184:187]\n"
    "s_cmp_eq_u32 s69, 152064\n"
    "s_cselect_b32 s69, 0, s69\n"
    "v_mfma_f32_16x16x32_bf16 v[172:175], a[188:191], a[60:63], v[172:175]\n"
    "ds_read_b64_tr_b16 a[180:181], v227 offset:96\n"
    "s_add_u32 s71, s71, 16384\n"
    "v_mfma_f32_16x16x32_bf16 v[180:183], a[188:191], a[64:67], v[180:183]\n"
    "ds_read_b64_tr_b16 a[182:183], v227 offset:608\n"
    "v_mfma_f32_16x16x32_bf16 v[188:191], a[188:191], a[68:71], v[188:191]\n"
    "s_waitcnt lgkmcnt(4)\n"
    "v_mfma_f32_16x16x32_bf16 v[0:3], a[168:171], a[72:75], v[0:3]\n"
    "ds_read_b64_tr_b16 a[184:185], v227 offset:128\n"
    "v_mfma_f32_16x16x32_bf16 v[8:11], a[168:171], a[76:79], v[8:11]\n"
    "ds_read_b64_tr_b16 a[186:187], v227 offset:640\n"
    "v_add_u32 v226, s33, v252\n"
    "s_waitcnt vmcnt(8)\n"
    "v_mfma_f32_16x16x32_bf16 v[16:19], a[168:171], a[80:83], v[16:19]\n"
    "ds_read_b128 a[144:147], v226\n"
    "s_add_u32 s35, s73, 1572864\n"
    "v_mfma_f32_16x16x32_bf16 v[4:7], a[172:175], a[72:75], v[4:7]\n"
    "ds_read_b64_tr_b16 a[188:189], v227 offset:160\n"
    "s_add_u32 m0, s52, s70\n"
    "v_mfma_f32_16x16x32_bf16 v[12:15], a[172:175], a[76:79], v[12:15]\n"
    "ds_read_b64_tr_b16 a[190:191], v227 offset:672\n"
    "v_mfma_f32_16x16x32_bf16 v[20:23], a[172:175], a[80:83], v[20:23]\n"
    "s_waitcnt lgkmcnt(5)\n"
    "ds_read_b128 a[148:151], v226 offset:1056\n"
    "v_mfma_f32_16x16x32_bf16 v[24:27], a[176:179], a[72:75], v[24:27]\n"
    "ds_read_b64_tr_b16 a[168:169], v227 offset:192\n"
    "v_mfma_f32_16x16x32_bf16 v[32:35], a[176:179], a[76:79], v[32:35]\n"
    "ds_read_b64_tr_b16 a[170:171], v227 offset:704\n"
    "v_mfma_f32_16x16x32_bf16 v[40:43], a[176:179], a[80:83], v[40:43]\n"
    "ds_read_b128 a[152:155], v226 offset:2112\n"
    "v_mfma_f32_16x16x32_bf16 v[28:31], a[180:183], a[72:75], v[28:31]\n"
    "ds_read_b64_tr_b16 a[172:173], v227 offset:224\n"
    "v_mfma_f32_16x16x32_bf16 v[36:39], a[180:183], a[76:79], v[36:39]\n"
    "ds_read_b64_tr_b16 a[174:175], v227 offset:736\n"
    "v_mfma_f32_16x16x32_bf16 v[44:47], a[180:183], a[80:83], v[44:47]\n"
    "s_waitcnt lgkmcnt(6)\n"
    "ds_read_b128 a[156:159], v226 offset:3168\n"
    "v_mfma_f32_16x16x32_bf16 v[48:51], a[184:187], a[72:75], v[48:51]\n"
    "ds_read_b64_tr_b16 a[176:177], v227 offset:256\n"
    "s_lshl_b32 s34, s64, 11\n"
    "v_mfma_f32_16x16x32_bf16 v[56:59], a[184:187], a[76:79], v[56:59]\n"
    "ds_read_b64_tr_b16 a[178:179], v227 offset:768\n"
    "s_add_u32 s34, s34, 153088\n"
    "v_mfma_f32_16x16x32_bf16 v[64:67], a[184:187], a[80:83], v[64:67]\n"
    "s_sub_u32 s34, s34, s70\n"
    "s_add_u32 s34, s34, %[ldsb]\n"
    "s_waitcnt lgkmcnt(2)\n"
    "v_mfma_f32_16x16x32_bf16 v[52:55], a[188:191], a[72:75], v[52:55]\n"
    "ds_read_b64_tr_b16 a[180:181], v227 offset:288\n"
    "v_add_u32 v226, s34, v252\n"
    "v_mfma_f32_16x16x32_bf16 v[60:63], a[188:191], a[76:79], v[60:63]\n"
    "ds_read_b64_tr_b16 a[182:183], v227 offset:800\n"
    "v_mfma_f32_16x16x32_bf16 v[68:71], a[188:191], a[80:83], v[68:71]\n"
    "ds_read_b128 a[160:163], v226\n"
    "v_mfma_f32_16x16x32_bf16 v[72:75], a[168:171], a[72:75], v[72:75]\n"
    "ds_read_b64_tr_b16 a[184:185], v227 offset:320\n"
    "v_mfma_f32_16x16x32_bf16 v[80:83], a[168:171], a[76:79], v[80:83]\n"
    "ds_read_b64_tr_b16 a[186:187], v227 offset:832\n"
    "v_mfma_f32_16x16x32_bf16 v[88:91], a[168:171], a[80:83], v[88:91]\n"
    "ds_read_b128 a[164:167], v226 offset:1024\n"
    "v_mfma_f32_16x16x32_bf16 v[76:79], a[172:175], a[72:75], v[76:79]\n"
    "ds_read_b64_tr_b16 a[188:189], v227 offset:352\n"
    "v_mfma_f32_16x16x32_bf16 v[84:87], a[172:175], a[76:79], v[84:87]\n"
    "ds_read_b64_tr_b16 a[190:191], v227 offset:864\n"
    "v_mfma_f32_16x16x32_bf16 v[92:95], a[172:175], a[80:83], v[92:95]\n"
    "s_waitcnt lgkmcnt(6)\n"
    "buffer_load_dwordx4 v232, s[56:59], s35 offen lds\n"
    "v_mfma_f32_16x16x32_bf16 v[96:99], a[176:179], a[72:75], v[96:99]\n"
    "ds_read_b64_tr_b16 a[168:169], v227 offset:384\n"
    "v_mfma_f32_16x16x32_bf16 v[104:107], a[176:179], a[76:79], v[104:107]\n"
    "ds_read_b64_tr_b16 a[170:171], v227 offset:896\n"
    "v_mfma_f32_16x16x32_bf16 v[112:115], a[176:179], a[80:83], v[112:115]\n"
    "buffer_load_dwordx4 v233, s[56:59], s35 offen offset:1056 lds\n"
    "v_mfma_f32_16x16x32_bf16 v[100:103], a[180:183], a[72:75], v[100:103]\n"
    "ds_read_b64_tr_b16 a[172:173], v227 offset:416\n"
    "v_mfma_f32_16x16x32_bf16 v[108:111], a[180:183], a[76:79], v[108:111]\n"
    "ds_read_b64_tr_b16 a[174:175], v227 offset:928\n"
    "v_mfma_f32_16x16x32_bf16 v[116:119], a[180:183], a[80:83], v[116:119]\n"
    "s_waitcnt lgkmcnt(4)\n"
    "buffer_load_dwordx4 v234, s[56:59], s35 offen offset:2112 lds\n"
    "v_mfma_f32_16x16x32_bf16 v[120:123], a[184:187], a[72:75], v[120:123]\n"
    "ds_read_b64_tr_b16 a[176:177], v227 offset:448\n"
    "v_mfma_f32_16x16x32_bf16 v[128:131], a[184:187], a[76:79], v[128:131]\n"
    "ds_read_b64_tr_b16 a[178:179], v227 offset:960\n"
    "v_mfma_f32_16x16x32_bf16 v[136:139], a[184:187], a[80:83], v[136:139]\n"
    "buffer_load_dwordx4 v235, s[56:59], s35 offen offset:3168 lds\n"
    "s_add_u32 m0, s69, s70\n"
    "v_mfma_f32_16x16x32_bf16 v[124:127], a[188:191], a[72:75], v[124:127]\n"
    "ds_read_b64_tr_b16 a[180:181], v227 offset:480\n"
    "v_mfma_f32_16x16x32_bf16 v[132:135], a[188:191], a[76:79], v[132:135]\n"
    "ds_read_b64_tr_b16 a[182:183], v227 offset:992\n"
    "v_mfma_f32_16x16x32_bf16 v[140:143], a[188:191], a[80:83], v[140:143]\n"
    "s_waitcnt lgkmcnt(4)\n"
    "s_barrier\n"
    "v_add_u32 v227, s40, v225\n"
    "v_mfma_f32_16x16x32_bf16 v[144:147], a[168:171], a[72:75], v[144:147]\n"
    "ds_read_b64_tr_b16 a[184:185], v227\n"
    "v_mfma_f32_16x16x32_bf16 v[152:155], a[168:171], a[76:79], v[152:155]\n"
    "ds_read_b64_tr_b16 a[186:187], v227 offset:512\n"
    "v_mfma_f32_16x16x32_bf16 v[160:163], a[168:171], a[80:83], v[160:163]\n"
    "buffer_load_dwordx4 v228, s[24:27], s71 offen nt lds\n"
    "v_mfma_f32_16x16x32_bf16 v[148:151], a[172:175], a[72:75], v[148:151]\n"
    "ds_read_b64_tr_b16 a[188:189], v227 offset:32\n"
    "v_mfma_f32_16x16x32_bf16 v[156:159], a[172:175], a[76:79], v[156:159]\n"
    "ds_read_b64_tr_b16 a[190:191], v227 offset:544\n"
    "v_mfma_f32_16x16x32_bf16 v[164:167], a[172:175], a[80:83], v[164:167]\n"
    "s_waitcnt lgkmcnt(4)\n"
    "buffer_load_dwordx4 v229, s[24:27], s71 offen offset:1056 nt lds\n"
    "v_mfma_f32_16x16x32_bf16 v[168:171], a[176:179], a[72:75], v[168:171]\n"
    "ds_read_b64_tr_b16 a[168:169], v227 offset:64\n"
    "v_mfma_f32_16x16x32_bf16 v[176:179], a[176:179], a[76:79], v[176:179]\n"
    "ds_read_b64_tr_b16 a[170:171], v227 offset:576\n"
    "v_mfma_f32_16x16x32_bf16 v[184:187], a[176:179], a[80:83], v[184:187]\n"
    "buffer_load_dwordx4 v230, s[24:27], s71 offen offset:2112 nt lds\n"
    "v_mfma_f32_16x16x32_bf16 v[172:175], a[180:183], a[72:75], v[172:175]\n"
    "ds_read_b64_tr_b16 a[172:173], v227 offset:96\n"
    "v_mfma_f32_16x16x32_bf16 v[180:183], a[180:183], a[76:79], v[180:183]\n"
    "ds_read_b64_tr_b16 a[174:175], v227 offset:608\n"
    "v_mfma_f32_16x16x32_bf16 v[188:191], a[180:183], a[80:83], v[188:191]\n"
    "s_waitcnt lgkmcnt(4)\n"
    "buffer_load_dwordx4 v231, s[24:27], s71 offen offset:3168 nt lds\n"
    "s_add_u32 s69, s69, 16896\n"
    "s_cmp_eq_u32 s69, 152064\n"
    "s_cselect_b32 s69, 0, s69\n"
    "s_add_u32 s71, s71, 16384\n"
    "v_mfma_f32_16x16x32_bf16 v[0:3], a[184:187], a[84:87], v[0:3]\n"
    "ds_read_b64_tr_b16 a[176:177], v227 offset:128\n"
    "v_mfma_f32_16x16x32_bf16 v[8:11], a[184:187], a[88:91], v[8:11]\n"
    "ds_read_b64_tr_b16 a[178:179], v227 offset:640\n"
    "s_add_u32 s35, s73, 1835008\n"
    "v_mfma_f32_16x16x32_bf16 v[16:19], a[184:187], a[92:95], v[16:19]\n"
    "s_add_u32 m0, s53, s70\n"
    "v_mfma_f32_16x16x32_bf16 v[4:7], a[188:191], a[84:87], v[4:7]\n"
    "ds_read_b64_tr_b16 a[180:181], v227 offset:160\n"
    "v_mfma_f32_16x16x32_bf16 v[12:15], a[188:191], a[88:91], v[12:15]\n"
    "ds_read_b64_tr_b16 a[182:183], v227 offset:672\n"
    "v_mfma_f32_16x16x32_bf16 v[20:23], a[188:191], a[92:95], v[20:23]\n"
    "s_waitcnt lgkmcnt(4)\n"
    "buffer_load_dwordx4 v232, s[56:59], s35 offen lds\n"
    "v_mfma_f32_16x16x32_bf16 v[24:27], a[168:171], a[84:87], v[24:27]\n"
    "ds_read_b64_tr_b16 a[184:185], v227 offset:192\n"
    "v_mfma_f32_16x16x32_bf16 v[32:35], a[168:171], a[88:91], v[32:35]\n"
    "ds_read_b64_tr_b16 a[186:187], v227 offset:704\n"
    "v_mfma_f32_16x16x32_bf16 v[40:43], a[168:171], a[92:95], v[40:43]\n"
    "buffer_load_dwordx4 v233, s[56:59], s35 offen offset:1056 lds\n"
    "v_mfma_f32_16x16x32_bf16 v[28:31], a[172:175], a[84:87], v[28:31]\n"
    "ds_read_b64_tr_b16 a[188:189], v227 offset:224\n"
    "v_mfma_f32_16x16x32_bf16 v[36:39], a[172:175], a[88:91], v[36:39]\n"
    "ds_read_b64_tr_b16 a[190:191], v227 offset:736\n"
    "v_mfma_f32_16x16x32_bf16 v[44:47], a[172:175], a[92:95], v[44:47]\n"
    "s_waitcnt lgkmcnt(4)\n"
    "buffer_load_dwordx4 v234, s[56:59], s35 offen offset:2112 lds\n"
    "v_mfma_f32_16x16x32_bf16 v[48:51], a[176:179], a[84:87], v[48:51]\n"
    "ds_read_b64_tr_b16 a[168:169], v227 offset:256\n"
    "v_mfma_f32_16x16x32_bf16 v[56:59], a[176:179], a[88:91], v[56:59]\n"
    "ds_read_b64_tr_b16 a[170:171], v227 offset:768\n"
    "v_mfma_f32_16x16x32_bf16 v[64:67], a[176:179], a[92:95], v[64:67]\n"
    "buffer_load_dwordx4 v235, s[56:59], s35 offen offset:3168 lds\n"
    "v_mfma_f32_16x16x32_bf16 v[52:55], a[180:183], a[84:87], v[52:55]\n"
    "ds_read_b64_tr_b16 a[172:173], v227 offset:288\n"
    "v_mfma_f32_16x16x32_bf16 v[60:63], a[180:183], a[88:91], v[60:63]\n"
    "ds_read_b64_tr_b16 a[174:175], v227 offset:800\n"
    "v_mfma_f32_16x16x32_bf16 v[68:71], a[180:183], a[92:95], v[68:71]\n"
    "s_waitcnt lgkmcnt(4)\n"
    "v_mfma_f32_16x16x32_bf16 v[72:75], a[184:187], a[84:87], v[72:75]\n"
    "ds_read_b64_tr_b16 a[176:177], v227 offset:320\n"
    "v_mfma_f32_16x16x32_bf16 v[80:83], a[184:187], a[88:91], v[80:83]\n"
    "ds_read_b64_tr_b16 a[178:179], v227 offset:832\n"
    "v_mfma_f32_16x16x32_bf16 v[88:91], a[184:187], a[92:95], v[88:91]\n"
    "v_mfma_f32_16x16x32_bf16 v[76:79], a[188:191], a[84:87], v[76:79]\n"
    "ds_read_b64_tr_b16 a[180:181], v227 offset:352\n"
    "v_mfma_f32_16x16x32_bf16 v[84:87], a[188:191], a[88:91], v[84:87]\n"
    "ds_read_b64_tr_b16 a[182:183], v227 offset:864\n"
    "v_mfma_f32_16x16x32_bf16 v[92:95], a[188:191], a[92:95], v[92:95]\n"
    "s_waitcnt lgkmcnt(4)\n"
    "v_mfma_f32_16x16x32_bf16 v[96:99], a[168:171], a[84:87], v[96:99]\n"
    "ds_read_b64_tr_b16 a[184:185], v227 offset:384\n"
    "v_mfma_f32_16x16x32_bf16 v[104:107], a[168:171], a[88:91], v[104:107]\n"
    "ds_read_b64_tr_b16 a[186:187], v227 offset:896\n"
    "v_mfma_f32_16x16x32_bf16 v[112:115], a[168:171], a[92:95], v[112:115]\n"
    "v_mfma_f32_16x16x32_bf16 v[100:103], a[172:175], a[84:87], v[100:103]\n"
    "ds_read_b64_tr_b16 a[188:189], v227 offset:416\n"
    "v_mfma_f32_16x16x32_bf16 v[108:111], a[172:175], a[88:91], v[108:111]\n"
    "ds_read_b64_tr_b16 a[190:191], v227 offset:928\n"
    "v_mfma_f32_16x16x32_bf16 v[116:119], a[172:175], a[92:95], v[116:119]\n"
    "s_waitcnt lgkmcnt(4)\n"
    "v_mfma_f32_16x16x32_bf16 v[120:123], a[176:179], a[84:87], v[120:123]\n"
    "ds_read_b64_tr_b16 a[168:169], v227 offset:448\n"
    "v_mfma_f32_16x16x32_bf16 v[128:131], a[176:179], a[88:91], v[128:131]\n"
    "ds_read_b64_tr_b16 a[170:171], v227 offset:960\n"
    "v_mfma_f32_16x16x32_bf16 v[136:139], a[176:179], a[92:95], v[136:139]\n"
    "v_mfma_f32_16x16x32_bf16 v[124:127], a[180:183], a[84:87], v[124:127]\n"
    "ds_read_b64_tr_b16 a[172:173], v227 offset:480\n"
    "v_mfma_f32_16x16x32_bf16 v[132:135], a[180:183], a[88:91], v[132:135]\n"
    "ds_read_b64_tr_b16 a[174:175], v227 offset:992\n"
    "v_mfma_f32_16x16x32_bf16 v[140:143], a[180:183], a[92:95], v[140:143]\n"
    "s_waitcnt lgkmcnt(4)\n"
    "s_barrier\n"
    "v_add_u32 v227, s41, v225\n"
    "v_mfma_f32_16x16x32_bf16 v[144:147], a[184:187], a[84:87], v[144:147]\n"
    "ds_read_b64_tr_b16 a[176:177], v227\n"
    "v_mfma_f32_16x16x32_bf16 v[152:155], a[184:187], a[88:91], v[152:155]\n"
    "ds_read_b64_tr_b16 a[178:179], v227 offset:512\n"
    "v_mfma_f32_16x16x32_bf16 v[160:163], a[184:187], a[92:95], v[160:163]\n"
    "v_mfma_f32_16x16x32_bf16 v[148:151], a[188:191], a[84:87], v[148:151]\n"
    "ds_read_b64_tr_b16 a[180:181], v227 offset:32\n"
    "v_mfma_f32_16x16x32_bf16 v[156:159], a[188:191], a[88:91], v[156:159]\n"
    "ds_read_b64_tr_b16 a[182:183], v227 offset:544\n"
    "v_mfma_f32_16x16x32_bf16 v[164:167], a[188:191], a[92:95], v[164:167]\n"
    "s_waitcnt lgkmcnt(4)\n"
    "v_mfma_f32_16x16x32_bf16 v[168:171], a[168:171], a[84:87], v[168:171]\n"
    "ds_read_b64_tr_b16 a[184:185], v227 offset:64\n"
    "v_mfma_f32_16x16x32_bf16 v[176:179], a[168:171], a[88:91], v[176:179]\n"
    "ds_read_b64_tr_b16 a[186:187], v227 offset:576\n"
    "v_mfma_f32_16x16x32_bf16 v[184:187], a[168:171], a[92:95], v[184:187]\n"
    "v_mfma_f32_16x16x32_bf16 v[172:175], a[172:175], a[84:87], v[172:175]\n"
    "ds_read_b64_tr_b16 a[188:189], v227 offset:96\n"
    "v_mfma_f32_16x16x32_bf16 v[180:183], a[172:175], a[88:91], v[180:183]\n"
    "ds_read_b64_tr_b16 a[190:191], v227 offset:608\n"
    "v_mfma_f32_16x16x32_bf16 v[188:191], a[172:175], a[92:95], v[188:191]\n"
    "s_waitcnt lgkmcnt(4)\n"
    "v_mfma_f32_16x16x32_bf16 v[0:3], a[176:179], a[96:99], v[0:3]\n"
    "ds_read_b64_tr_b16 a[168:169], v227 offset:128\n"
    "v_mfma_f32_16x16x32_bf16 v[8:11], a[176:179], a[100:103], v[8:11]\n"
    "ds_read_b64_tr_b16 a[170:171], v227 offset:640\n"
    "s_add_u32 s35, s73, 2097152\n"
    "v_mfma_f32_16x16x32_bf16 v[16:19], a[176:179], a[104:107], v[16:19]\n"
    "s_add_u32 m0, s54, s70\n"
    "v_mfma_f32_16x16x32_bf16 v[4:7], a[180:183], a[96:99], v[4:7]\n"
    "ds_read_b64_tr_b16 a[172:173], v227 offset:160\n"
    "v_mfma_f32_16x16x32_bf16 v[12:15], a[180:183], a[100:103], v[12:15]\n"
    "ds_read_b64_tr_b16 a[174:175], v227 offset:672\n"
    "v_mfma_f32_16x16x32_bf16 v[20:23], a[180:183], a[104:107], v[20:23]\n"
    "s_waitcnt lgkmcnt(4)\n"
    "buffer_load_dwordx4 v232, s[56:59], s35 offen lds\n"
    "v_mfma_f32_16x16x32_bf16 v[24:27], a[184:187], a[96:99], v[24:27]\n"
    "ds_read_b64_tr_b16 a[176:177], v227 offset:192\n"
    "v_mfma_f32_16x16x32_bf16 v[32:35], a[184:187], a[100:103], v[32:35]\n"
    "ds_read_b64_tr_b16 a[178:179], v227 offset:704\n"
    "v_mfma_f32_16x16x32_bf16 v[40:43], a[184:187], a[104:107], v[40:43]\n"
    "buffer_load_dwordx4 v233, s[56:59], s35 offen offset:1056 lds\n"
    "v_mfma_f32_16x16x32_bf16 v[28:31], a[188:191], a[96:99], v[28:31]\n"
    "ds_read_b64_tr_b16 a[180:181], v227 offset:224\n"
    "v_mfma_f32_16x16x32_bf16 v[36:39], a[188:191], a[100:103], v[36:39]\n"
    "ds_read_b64_tr_b16 a[182:183], v227 offset:736\n"
    "v_mfma_f32_16x16x32_bf16 v[44:47], a[188:191], a[104:107], v[44:47]\n"
    "s_waitcnt lgkmcnt(4)\n"
    "buffer_load_dwordx4 v234, s[56:59], s35 offen offset:2112 lds\n"
    "v_mfma_f32_16x16x32_bf16 v[48:51], a[168:171], a[96:99], v[48:51]\n"
    "ds_read_b64_tr_b16 a[184:185], v227 offset:256\n"
    "v_mfma_f32_16x16x32_bf16 v[56:59], a[168:171], a[100:103], v[56:59]\n"
    "ds_read_b64_tr_b16 a[186:187], v227 offset:768\n"
    "v_mfma_f32_16x16x32_bf16 v[64:67], a[168:171], a[104:107], v[64:67]\n"
    "buffer_load_dwordx4 v235, s[56:59], s35 offen offset:3168 lds\n"
    "v_mfma_f32_16x16x32_bf16 v[52:55], a[172:175], a[96:99], v[52:55]\n"
    "ds_read_b64_tr_b16 a[188:189], v227 offset:288\n"
    "v_mfma_f32_16x16x32_bf16 v[60:63], a[172:175], a[100:103], v[60:63]\n"
    "ds_read_b64_tr_b16 a[190:191], v227 offset:800\n"
    "v_mfma_f32_16x16x32_bf16 v[68:71], a[172:175], a[104:107], v[68:71]\n"
    "s_waitcnt lgkmcnt(4)\n"
    "v_mfma_f32_16x16x32_bf16 v[72:75], a[176:179], a[96:99], v[72:75]\n"
    "ds_read_b64_tr_b16 a[168:169], v227 offset:320\n"
    "v_mfma_f32_16x16x32_bf16 v[80:83], a[176:179], a[100:103], v[80:83]\n"
    "ds_read_b64_tr_b16 a[170:171], v227 offset:832\n"
    "v_mfma_f32_16x16x32_bf16 v[88:91], a[176:179], a[104:107], v[88:91]\n"
    "v_mfma_f32_16x16x32_bf16 v[76:79], a[180:183], a[96:99], v[76:79]\n"
    "ds_read_b64_tr_b16 a[172:173], v227 offset:352\n"
    "v_mfma_f32_16x16x32_bf16 v[84:87], a[180:183], a[100:103], v[84:87]\n"
    "ds_read_b64_tr_b16 a[174:175], v227 offset:864\n"
    "v_mfma_f32_16x16x32_bf16 v[92:95], a[180:183], a[104:107], v[92:95]\n"
    "s_waitcnt lgkmcnt(4)\n"
    "v_mfma_f32_16x16x32_bf16 v[96:99], a[184:187], a[96:99], v[96:99]\n"
    "ds_read_b64_tr_b16 a[176:177], v227 offset:384\n"
    "v_mfma_f32_16x16x32_bf16 v[104:107], a[184:187], a[100:103], v[104:107]\n"
    "ds_read_b64_tr_b16 a[178:179], v227 offset:896\n"
    "v_mfma_f32_16x16x32_bf16 v[112:115], a[184:187], a[104:107], v[112:115]\n"
    "v_mfma_f32_16x16x32_bf16 v[100:103], a[188:191], a[96:99], v[100:103]\n"
    "ds_read_b64_tr_b16 a[180:181], v227 offset:416\n"
    "v_mfma_f32_16x16x32_bf16 v[108:111], a[188:191], a[100:103], v[108:111]\n"
    "ds_read_b64_tr_b16 a[182:183], v227 offset:928\n"
    "v_mfma_f32_16x16x32_bf16 v[116:119], a[188:191], a[104:107], v[116:119]\n"
    "s_waitcnt lgkmcnt(4)\n"
    "v_mfma_f32_16x16x32_bf16 v[120:123], a[168:171], a[96:99], v[120:123]\n"
    "ds_read_b64_tr_b16 a[184:185], v227 offset:448\n"
    "v_mfma_f32_16x16x32_bf16 v[128:131], a[168:171], a[100:103], v[128:131]\n"
    "ds_read_b64_tr_b16 a[186:187], v227 offset:960\n"
    "v_mfma_f32_16x16x32_bf16 v[136:139], a[168:171], a[104:107], v[136:139]\n"
    "v_mfma_f32_16x16x32_bf16 v[124:127], a[172:175], a[96:99], v[124:127]\n"
    "ds_read_b64_tr_b16 a[188:189], v227 offset:480\n"
    "v_mfma_f32_16x16x32_bf16 v[132:135], a[172:175], a[100:103], v[132:135]\n"
    "ds_read_b64_tr_b16 a[190:191], v227 offset:992\n"
    "v_mfma_f32_16x16x32_bf16 v[140:143], a[172:175], a[104:107], v[140:143]\n"
    "s_waitcnt lgkmcnt(4)\n"
    "s_barrier\n"
    "v_add_u32 v227, s42, v225\n"
    "v_mfma_f32_16x16x32_bf16 v[144:147], a[176:179], a[96:99], v[144:147]\n"
    "ds_read_b64_tr_b16 a[168:169], v227\n"
    "v_mfma_f32_16x16x32_bf16 v[152:155], a[176:179], a[100:103], v[152:155]\n"
    "ds_read_b64_tr_b16 a[170:171], v227 offset:512\n"
    "v_mfma_f32_16x16x32_bf16 v[160:163], a[176:179], a[104:107], v[160:163]\n"
    "v_mfma_f32_16x16x32_bf16 v[148:151], a[180:183], a[96:99], v[148:151]\n"
    "ds_read_b64_tr_b16 a[172:173], v227 offset:32\n"
    "v_mfma_f32_16x16x32_bf16 v[156:159], a[180:183], a[100:103], v[156:159]\n"
    "ds_read_b64_tr_b16 a[174:175], v227 offset:544\n"
    "v_mfma_f32_16x16x32_bf16 v[164:167], a[180:183], a[104:107], v[164:167]\n"
    "s_waitcnt lgkmcnt(4)\n"
    "v_mfma_f32_16x16x32_bf16 v[168:171], a[184:187], a[96:99], v[168:171]\n"
    "ds_read_b64_tr_b16 a[176:177], v227 offset:64\n"
    "v_mfma_f32_16x16x32_bf16 v[176:179], a[184:187], a[100:103], v[176:179]\n"
    "ds_read_b64_tr_b16 a[178:179], v227 offset:576\n"
    "v_mfma_f32_16x16x32_bf16 v[184:187], a[184:187], a[104:107], v[184:187]\n"
    "v_mfma_f32_16x16x32_bf16 v[172:175], a[188:191], a[96:99], v[172:175]\n"
    "ds_read_b64_tr_b16 a[180:181], v227 offset:96\n"
    "v_mfma_f32_16x16x32_bf16 v[180:183], a[188:191], a[100:103], v[180:183]\n"
    "ds_read_b64_tr_b16 a[182:183], v227 offset:608\n"
    "v_mfma_f32_16x16x32_bf16 v[188:191], a[188:191], a[104:107], v[188:191]\n"
    "s_waitcnt lgkmcnt(4)\n"
    "v_mfma_f32_16x16x32_bf16 v[0:3], a[168:171], a[108:111], v[0:3]\n"
    "ds_read_b64_tr_b16 a[184:185], v227 offset:128\n"
    "v_mfma_f32_16x16x32_bf16 v[8:11], a[168:171], a[112:115], v[8:11]\n"
    "ds_read_b64_tr_b16 a[186:187], v227 offset:640\n"
    "s_add_u32 m0, s69, s70\n"
    "v_mfma_f32_16x16x32_bf16 v[16:19], a[168:171], a[116:119], v[16:19]\n"
    "buffer_load_dwordx4 v228, s[28:31], s72 offen nt lds\n"
    "v_mfma_f32_16x16x32_bf16 v[4:7], a[172:175], a[108:111], v[4:7]\n"
    "ds_read_b64_tr_b16 a[188:189], v227 offset:160\n"
    "v_mfma_f32_16x16x32_bf16 v[12:15], a[172:175], a[112:115], v[12:15]\n"
    "ds_read_b64_tr_b16 a[190:191], v227 offset:672\n"
    "v_mfma_f32_16x16x32_bf16 v[20:23], a[172:175], a[116:119], v[20:23]\n"
    "s_waitcnt lgkmcnt(4)\n"
    "buffer_load_dwordx4 v229, s[28:31], s72 offen offset:1056 nt lds\n"
    "v_mfma_f32_16x16x32_bf16 v[24:27], a[176:179], a[108:111], v[24:27]\n"
    "ds_read_b64_tr_b16 a[168:169], v227 offset:192\n"
    "v_mfma_f32_16x16x32_bf16 v[32:35], a[176:179], a[112:115], v[32:35]\n"
    "ds_read_b64_tr_b16 a[170:171], v227 offset:704\n"
    "v_mfma_f32_16x16x32_bf16 v[40:43], a[176:179], a[116:119], v[40:43]\n"
    "buffer_load_dwordx4 v230, s[28:31], s72 offen offset:2112 nt lds\n"
    "v_mfma_f32_16x16x32_bf16 v[28:31], a[180:183], a[108:111], v[28:31]\n"
    "ds_read_b64_tr_b16 a[172:173], v227 offset:224\n"
    "v_mfma_f32_16x16x32_bf16 v[36:39], a[180:183], a[112:115], v[36:39]\n"
    "ds_read_b64_tr_b16 a[174:175], v227 offset:736\n"
    "v_mfma_f32_16x16x32_bf16 v[44:47], a[180:183], a[116:119], v[44:47]\n"
    "s_waitcnt lgkmcnt(4)\n"
    "buffer_load_dwordx4 v231, s[28:31], s72 offen offset:3168 nt lds\n"
    "v_mfma_f32_16x16x32_bf16 v[48:51], a[184:187], a[108:111], v[48:51]\n"
    "ds_read_b64_tr_b16 a[176:177], v227 offset:256\n"
    "s_add_u32 s69, s69, 16896\n"
    "v_mfma_f32_16x16x32_bf16 v[56:59], a[184:187], a[112:115], v[56:59]\n"
    "ds_read_b64_tr_b16 a[178:179], v227 offset:768\n"
    "v_mfma_f32_16x16x32_bf16 v[64:67], a[184:187], a[116:119], v[64:67]\n"
    "s_cmp_eq_u32 s69, 152064\n"
    "s_cselect_b32 s69, 0, s69\n"
    "v_mfma_f32_16x16x32_bf16 v[52:55], a[188:191], a[108:111], v[52:55]\n"
    "ds_read_b64_tr_b16 a[180:181], v227 offset:288\n"
    "s_add_u32 s72, s72, 16384\n"
    "v_mfma_f32_16x16x32_bf16 v[60:63], a[188:191], a[112:115], v[60:63]\n"
    "ds_read_b64_tr_b16 a[182:183], v227 offset:800\n"
    "v_mfma_f32_16x16x32_bf16 v[68:71], a[188:191], a[116:119], v[68:71]\n"
    "s_waitcnt lgkmcnt(4)\n"
    "v_mfma_f32_16x16x32_bf16 v[72:75], a[168:171], a[108:111], v[72:75]\n"
    "ds_read_b64_tr_b16 a[184:185], v227 offset:320\n"
    "v_mfma_f32_16x16x32_bf16 v[80:83], a[168:171], a[112:115], v[80:83]\n"
    "ds_read_b64_tr_b16 a[186:187], v227 offset:832\n"
    "v_mfma_f32_16x16x32_bf16 v[88:91], a[168:171], a[116:119], v[88:91]\n"
    "v_mfma_f32_16x16x32_bf16 v[76:79], a[172:175], a[108:111], v[76:79]\n"
    "ds_read_b64_tr_b16 a[188:189], v227 offset:352\n"
    "v_mfma_f32_16x16x32_bf16 v[84:87], a[172:175], a[112:115], v[84:87]\n"
    "ds_read_b64_tr_b16 a[190:191], v227 offset:864\n"
    "v_mfma_f32_16x16x32_bf16 v[92:95], a[172:175], a[116:119], v[92:95]\n"
    "s_waitcnt lgkmcnt(4)\n"
    "v_mfma_f32_16x16x32_bf16 v[96:99], a[176:179], a[108:111], v[96:99]\n"
    "ds_read_b64_tr_b16 a[168:169], v227 offset:384\n"
    "v_mfma_f32_16x16x32_bf16 v[104:107], a[176:179], a[112:115], v[104:107]\n"
    "ds_read_b64_tr_b16 a[170:171], v227 offset:896\n"
    "v_mfma_f32_16x16x32_bf16 v[112:115], a[176:179], a[116:119], v[112:115]\n"
    "v_mfma_f32_16x16x32_bf16 v[100:103], a[180:183], a[108:111], v[100:103]\n"
    "ds_read_b64_tr_b16 a[172:173], v227 offset:416\n"
    "v_mfma_f32_16x16x32_bf16 v[108:111], a[180:183], a[112:115], v[108:111]\n"
    "ds_read_b64_tr_b16 a[174:175], v227 offset:928\n"
    "v_mfma_f32_16x16x32_bf16 v[116:119], a[180:183], a[116:119], v[116:119]\n"
    "s_waitcnt lgkmcnt(4)\n"
    "v_mfma_f32_16x16x32_bf16 v[120:123], a[184:187], a[108:111], v[120:123]\n"
    "ds_read_b64_tr_b16 a[176:177], v227 offset:448\n"
    "v_mfma_f32_16x16x32_bf16 v[128:131], a[184:187], a[112:115], v[128:131]\n"
    "ds_read_b64_tr_b16 a[178:179], v227 offset:960\n"
    "v_mfma_f32_16x16x32_bf16 v[136:139], a[184:187], a[116:119], v[136:139]\n"
    "v_mfma_f32_16x16x32_bf16 v[124:127], a[188:191], a[108:111], v[124:127]\n"
    "ds_read_b64_tr_b16 a[180:181], v227 offset:480\n"
    "v_mfma_f32_16x16x32_bf16 v[132:135], a[188:191], a[112:115], v[132:135]\n"
    "ds_read_b64_tr_b16 a[182:183], v227 offset:992\n"
    "v_mfma_f32_16x16x32_bf16 v[140:143], a[188:191], a[116:119], v[140:143]\n"
    "s_waitcnt lgkmcnt(4)\n"
    "s_waitcnt vmcnt(24)\n"
    "s_barrier\n"
    "v_add_u32 v227, s43, v225\n"
    "v_mfma_f32_16x16x32_bf16 v[144:147], a[168:171], a[108:111], v[144:147]\n"
    "ds_read_b64_tr_b16 a[184:185], v227\n"
    "v_mfma_f32_16x16x32_bf16 v[152:155], a[168:171], a[112:115], v[152:155]\n"
    "ds_read_b64_tr_b16 a[186:187], v227 offset:512\n"
    "v_mfma_f32_16x16x32_bf16 v[160:163], a[168:171], a[116:119], v[160:163]\n"
    "v_mfma_f32_16x16x32_bf16 v[148:151], a[172:175], a[108:111], v[148:151]\n"
    "ds_read_b64_tr_b16 a[188:189], v227 offset:32\n"
    "v_mfma_f32_16x16x32_bf16 v[156:159], a[172:175], a[112:115], v[156:159]\n"
    "ds_read_b64_tr_b16 a[190:191], v227 offset:544\n"
    "v_mfma_f32_16x16x32_bf16 v[164:167], a[172:175], a[116:119], v[164:167]\n"
    "s_waitcnt lgkmcnt(4)\n"
    "v_mfma_f32_16x16x32_bf16 v[168:171], a[176:179], a[108:111], v[168:171]\n"
    "ds_read_b64_tr_b16 a[168:169], v227 offset:64\n"
    "v_mfma_f32_16x16x32_bf16 v[176:179], a[176:179], a[112:115], v[176:179]\n"
    "ds_read_b64_tr_b16 a[170:171], v227 offset:576\n"
    "v_mfma_f32_16x16x32_bf16 v[184:187], a[176:179], a[116:119], v[184:187]\n"
    "v_mfma_f32_16x16x32_bf16 v[172:175], a[180:183], a[108:111], v[172:175]\n"
    "ds_read_b64_tr_b16 a[172:173], v227 offset:96\n"
    "v_mfma_f32_16x16x32_bf16 v[180:183], a[180:183], a[112:115], v[180:183]\n"
    "ds_read_b64_tr_b16 a[174:175], v227 offset:608\n"
    "v_mfma_f32_16x16x32_bf16 v[188:191], a[180:183], a[116:119], v[188:191]\n"
    "s_waitcnt lgkmcnt(4)\n"
    "v_mfma_f32_16x16x32_bf16 v[0:3], a[184:187], a[120:123], v[0:3]\n"
    "ds_read_b64_tr_b16 a[176:177], v227 offset:128\n"
    "v_mfma_f32_16x16x32_bf16 v[8:11], a[184:187], a[124:127], v[8:11]\n"
    "ds_read_b64_tr_b16 a[178:179], v227 offset:640\n"
    "s_add_u32 m0, s69, s70\n"
    "v_mfma_f32_16x16x32_bf16 v[16:19], a[184:187], a[128:131], v[16:19]\n"
    "buffer_load_dwordx4 v228, s[24:27], s71 offen nt lds\n"
    "v_mfma_f32_16x16x32_bf16 v[4:7], a[188:191], a[120:123], v[4:7]\n"
    "ds_read_b64_tr_b16 a[180:181], v227 offset:160\n"
    "v_mfma_f32_16x16x32_bf16 v[12:15], a[188:191], a[124:127], v[12:15]\n"
    "ds_read_b64_tr_b16 a[182:183], v227 offset:672\n"
    "v_mfma_f32_16x16x32_bf16 v[20:23], a[188:191], a[128:131], v[20:23]\n"
    "s_waitcnt lgkmcnt(4)\n"
    "buffer_load_dwordx4 v229, s[24:27], s71 offen offset:1056 nt lds\n"
    "v_mfma_f32_16x16x32_bf16 v[24:27], a[168:171], a[120:123], v[24:27]\n"
    "ds_read_b64_tr_b16 a[184:185], v227 offset:192\n"
    "v_mfma_f32_16x16x32_bf16 v[32:35], a[168:171], a[124:127], v[32:35]\n"
    "ds_read_b64_tr_b16 a[186:187], v227 offset:704\n"
    "v_mfma_f32_16x16x32_bf16 v[40:43], a[168:171], a[128:131], v[40:43]\n"
    "buffer_load_dwordx4 v230, s[24:27], s71 offen offset:2112 nt lds\n"
    "v_mfma_f32_16x16x32_bf16 v[28:31], a[172:175], a[120:123], v[28:31]\n"
    "ds_read_b64_tr_b16 a[188:189], v227 offset:224\n"
    "v_mfma_f32_16x16x32_bf16 v[36:39], a[172:175], a[124:127], v[36:39]\n"
    "ds_read_b64_tr_b16 a[190:191], v227 offset:736\n"
    "v_mfma_f32_16x16x32_bf16 v[44:47], a[172:175], a[128:131], v[44:47]\n"
    "s_waitcnt lgkmcnt(4)\n"
    "buffer_load_dwordx4 v231, s[24:27], s71 offen offset:3168 nt lds\n"
    "v_mfma_f32_16x16x32_bf16 v[48:51], a[176:179], a[120:123], v[48:51]\n"
    "ds_read_b64_tr_b16 a[168:169], v227 offset:256\n"
    "s_add_u32 s69, s69, 16896\n"
    "v_mfma_f32_16x16x32_bf16 v[56:59], a[176:179], a[124:127], v[56:59]\n"
    "ds_read_b64_tr_b16 a[170:171], v227 offset:768\n"
    "v_mfma_f32_16x16x32_bf16 v[64:67], a[176:179], a[128:131], v[64:67]\n"
    "s_cmp_eq_u32 s69, 152064\n"
    "s_cselect_b32 s69, 0, s69\n"
    "v_mfma_f32_16x16x32_bf16 v[52:55], a[180:183], a[120:123], v[52:55]\n"
    "ds_read_b64_tr_b16 a[172:173], v227 offset:288\n"
    "s_add_u32 s71, s71, 16384\n"
    "v_mfma_f32_16x16x32_bf16 v[60:63], a[180:183], a[124:127], v[60:63]\n"
    "ds_read_b64_tr_b16 a[174:175], v227 offset:800\n"
    "v_mfma_f32_16x16x32_bf16 v[68:71], a[180:183], a[128:131], v[68:71]\n"
    "s_waitcnt lgkmcnt(4)\n"
    "v_mfma_f32_16x16x32_bf16 v[72:75], a[184:187], a[120:123], v[72:75]\n"
    "ds_read_b64_tr_b16 a[176:177], v227 offset:320\n"
    "v_mfma_f32_16x16x32_bf16 v[80:83], a[184:187], a[124:127], v[80:83]\n"
    "ds_read_b64_tr_b16 a[178:179], v227 offset:832\n"
    "v_mfma_f32_16x16x32_bf16 v[88:91], a[184:187], a[128:131], v[88:91]\n"
    "v_mfma_f32_16x16x32_bf16 v[76:79], a[188:191], a[120:123], v[76:79]\n"
    "ds_read_b64_tr_b16 a[180:181], v227 offset:352\n"
    "v_mfma_f32_16x16x32_bf16 v[84:87], a[188:191], a[124:127], v[84:87]\n"
    "ds_read_b64_tr_b16 a[182:183], v227 offset:864\n"
    "v_mfma_f32_16x16x32_bf16 v[92:95], a[188:191], a[128:131], v[92:95]\n"
    "s_waitcnt lgkmcnt(4)\n"
    "v_mfma_f32_16x16x32_bf16 v[96:99], a[168:171], a[120:123], v[96:99]\n"
    "ds_read_b64_tr_b16 a[184:185], v227 offset:384\n"
    "v_mfma_f32_16x16x32_bf16 v[104:107], a[168:171], a[124:127], v[104:107]\n"
    "ds_read_b64_tr_b16 a[186:187], v227 offset:896\n"
    "v_mfma_f32_16x16x32_bf16 v[112:115], a[168:171], a[128:131], v[112:115]\n"
    "v_mfma_f32_16x16x32_bf16 v[100:103], a[172:175], a[120:123], v[100:103]\n"
    "ds_read_b64_tr_b16 a[188:189], v227 offset:416\n"
    "v_mfma_f32_16x16x32_bf16 v[108:111], a[172:175], a[124:127], v[108:111]\n"
    "ds_read_b64_tr_b16 a[190:191], v227 offset:928\n"
    "v_mfma_f32_16x16x32_bf16 v[116:119], a[172:175], a[128:131], v[116:119]\n"
    "s_waitcnt lgkmcnt(4)\n"
    "v_mfma_f32_16x16x32_bf16 v[120:123], a[176:179], a[120:123], v[120:123]\n"
    "ds_read_b64_tr_b16 a[168:169], v227 offset:448\n"
    "v_mfma_f32_16x16x32_bf16 v[128:131], a[176:179], a[124:127], v[128:131]\n"
    "ds_read_b64_tr_b16 a[170:171], v227 offset:960\n"
    "v_mfma_f32_16x16x32_bf16 v[136:139], a[176:179], a[128:131], v[136:139]\n"
    "v_mfma_f32_16x16x32_bf16 v[124:127], a[180:183], a[120:123], v[124:127]\n"
    "ds_read_b64_tr_b16 a[172:173], v227 offset:480\n"
    "v_mfma_f32_16x16x32_bf16 v[132:135], a[180:183], a[124:127], v[132:135]\n"
    "ds_read_b64_tr_b16 a[174:175], v227 offset:992\n"
    "v_mfma_f32_16x16x32_bf16 v[140:143], a[180:183], a[128:131], v[140:143]\n"
    "s_waitcnt lgkmcnt(4)\n"
    "s_waitcnt vmcnt(20)\n"
    "s_barrier\n"
    "v_add_u32 v227, s52, v225\n"
    "v_mfma_f32_16x16x32_bf16 v[144:147], a[184:187], a[120:123], v[144:147]\n"
    "ds_read_b64_tr_b16 a[176:177], v227\n"
    "v_mfma_f32_16x16x32_bf16 v[152:155], a[184:187], a[124:127], v[152:155]\n"
    "ds_read_b64_tr_b16 a[178:179], v227 offset:512\n"
    "v_mfma_f32_16x16x32_bf16 v[160:163], a[184:187], a[128:131], v[160:163]\n"
    "v_mfma_f32_16x16x32_bf16 v[148:151], a[188:191], a[120:123], v[148:151]\n"
    "ds_read_b64_tr_b16 a[180:181], v227 offset:32\n"
    "v_mfma_f32_16x16x32_bf16 v[156:159], a[188:191], a[124:127], v[156:159]\n"
    "ds_read_b64_tr_b16 a[182:183], v227 offset:544\n"
    "v_mfma_f32_16x16x32_bf16 v[164:167], a[188:191], a[128:131], v[164:167]\n"
    "s_waitcnt lgkmcnt(4)\n"
    "v_mfma_f32_16x16x32_bf16 v[168:171], a[168:171], a[120:123], v[168:171]\n"
    "ds_read_b64_tr_b16 a[184:185], v227 offset:64\n"
    "v_mfma_f32_16x16x32_bf16 v[176:179], a[168:171], a[124:127], v[176:179]\n"
    "ds_read_b64_tr_b16 a[186:187], v227 offset:576\n"
    "v_mfma_f32_16x16x32_bf16 v[184:187], a[168:171], a[128:131], v[184:187]\n"
    "v_mfma_f32_16x16x32_bf16 v[172:175], a[172:175], a[120:123], v[172:175]\n"
    "ds_read_b64_tr_b16 a[188:189], v227 offset:96\n"
    "v_mfma_f32_16x16x32_bf16 v[180:183], a[172:175], a[124:127], v[180:183]\n"
    "ds_read_b64_tr_b16 a[190:191], v227 offset:608\n"
    "v_mfma_f32_16x16x32_bf16 v[188:191], a[172:175], a[128:131], v[188:191]\n"
    "s_waitcnt lgkmcnt(4)\n"
    "v_mfma_f32_16x16x32_bf16 v[0:3], a[176:179], a[132:135], v[0:3]\n"
    "ds_read_b64_tr_b16 a[168:169], v227 offset:128\n"
    "v_mfma_f32_16x16x32_bf16 v[8:11], a[176:179], a[136:139], v[8:11]\n"
    "ds_read_b64_tr_b16 a[170:171], v227 offset:640\n"
    "s_add_u32 m0, s69, s70\n"
    "v_mfma_f32_16x16x32_bf16 v[16:19], a[176:179], a[140:143], v[16:19]\n"
    "buffer_load_dwordx4 v228, s[28:31], s72 offen nt lds\n"
    "v_mfma_f32_16x16x32_bf16 v[4:7], a[180:183], a[132:135], v[4:7]\n"
    "ds_read_b64_tr_b16 a[172:173], v227 offset:160\n"
    "v_mfma_f32_16x16x32_bf16 v[12:15], a[180:183], a[136:139], v[12:15]\n"
    "ds_read_b64_tr_b16 a[174:175], v227 offset:672\n"
    "v_mfma_f32_16x16x32_bf16 v[20:23], a[180:183], a[140:143], v[20:23]\n"
    "s_waitcnt lgkmcnt(4)\n"
    "buffer_load_dwordx4 v229, s[28:31], s72 offen offset:1056 nt lds\n"
    "v_mfma_f32_16x16x32_bf16 v[24:27], a[184:187], a[132:135], v[24:27]\n"
    "ds_read_b64_tr_b16 a[176:177], v227 offset:192\n"
    "v_mfma_f32_16x16x32_bf16 v[32:35], a[184:187], a[136:139], v[32:35]\n"
    "ds_read_b64_tr_b16 a[178:179], v227 offset:704\n"
    "v_mfma_f32_16x16x32_bf16 v[40:43], a[184:187], a[140:143], v[40:43]\n"
    "buffer_load_dwordx4 v230, s[28:31], s72 offen offset:2112 nt lds\n"
    "v_mfma_f32_16x16x32_bf16 v[28:31], a[188:191], a[132:135], v[28:31]\n"
    "ds_read_b64_tr_b16 a[180:181], v227 offset:224\n"
    "v_mfma_f32_16x16x32_bf16 v[36:39], a[188:191], a[136:139], v[36:39]\n"
    "ds_read_b64_tr_b16 a[182:183], v227 offset:736\n"
    "v_mfma_f32_16x16x32_bf16 v[44:47], a[188:191], a[140:143], v[44:47]\n"
    "s_waitcnt lgkmcnt(4)\n"
    "buffer_load_dwordx4 v231, s[28:31], s72 offen offset:3168 nt lds\n"
    "v_mfma_f32_16x16x32_bf16 v[48:51], a[168:171], a[132:135], v[48:51]\n"
    "ds_read_b64_tr_b16 a[184:185], v227 offset:256\n"
    "s_add_u32 s69, s69, 16896\n"
    "v_mfma_f32_16x16x32_bf16 v[56:59], a[168:171], a[136:139], v[56:59]\n"
    "ds_read_b64_tr_b16 a[186:187], v227 offset:768\n"
    "v_mfma_f32_16x16x32_bf16 v[64:67], a[168:171], a[140:143], v[64:67]\n"
    "s_cmp_eq_u32 s69, 152064\n"
    "s_cselect_b32 s69, 0, s69\n"
    "v_mfma_f32_16x16x32_bf16 v[52:55], a[172:175], a[132:135], v[52:55]\n"
    "ds_read_b64_tr_b16 a[188:189], v227 offset:288\n"
    "s_add_u32 s72, s72, 16384\n"
    "v_mfma_f32_16x16x32_bf16 v[60:63], a[172:175], a[136:139], v[60:63]\n"
    "ds_read_b64_tr_b16 a[190:191], v227 offset:800\n"
    "v_mfma_f32_16x16x32_bf16 v[68:71], a[172:175], a[140:143], v[68:71]\n"
    "s_waitcnt lgkmcnt(4)\n"
    "v_mfma_f32_16x16x32_bf16 v[72:75], a[176:179], a[132:135], v[72:75]\n"
    "ds_read_b64_tr_b16 a[168:169], v227 offset:320\n"
    "v_mfma_f32_16x16x32_bf16 v[80:83], a[176:179], a[136:139], v[80:83]\n"
    "ds_read_b64_tr_b16 a[170:171], v227 offset:832\n"
    "v_mfma_f32_16x16x32_bf16 v[88:91], a[176:179], a[140:143], v[88:91]\n"
    "v_mfma_f32_16x16x32_bf16 v[76:79], a[180:183], a[132:135], v[76:79]\n"
    "ds_read_b64_tr_b16 a[172:173], v227 offset:352\n"
    "v_mfma_f32_16x16x32_bf16 v[84:87], a[180:183], a[136:139], v[84:87]\n"
    "ds_read_b64_tr_b16 a[174:175], v227 offset:864\n"
    "v_mfma_f32_16x16x32_bf16 v[92:95], a[180:183], a[140:143], v[92:95]\n"
    "s_waitcnt lgkmcnt(4)\n"
    "v_mfma_f32_16x16x32_bf16 v[96:99], a[184:187], a[132:135], v[96:99]\n"
    "ds_read_b64_tr_b16 a[176:177], v227 offset:384\n"
    "v_mfma_f32_16x16x32_bf16 v[104:107], a[184:187], a[136:139], v[104:107]\n"
    "ds_read_b64_tr_b16 a[178:179], v227 offset:896\n"
    "v_mfma_f32_16x16x32_bf16 v[112:115], a[184:187], a[140:143], v[112:115]\n"
    "v_mfma_f32_16x16x32_bf16 v[100:103], a[188:191], a[132:135], v[100:103]\n"
    "ds_read_b64_tr_b16 a[180:181], v227 offset:416\n"
    "v_mfma_f32_16x16x32_bf16 v[108:111], a[188:191], a[136:139], v[108:111]\n"
    "ds_read_b64_tr_b16 a[182:183], v227 offset:928\n"
    "v_mfma_f32_16x16x32_bf16 v[116:119], a[188:191], a[140:143], v[116:119]\n"
    "s_waitcnt lgkmcnt(4)\n"
    "v_mfma_f32_16x16x32_bf16 v[120:123], a[168:171], a[132:135], v[120:123]\n"
    "ds_read_b64_tr_b16 a[184:185], v227 offset:448\n"
    "v_mfma_f32_16x16x32_bf16 v[128:131], a[168:171], a[136:139], v[128:131]\n"
    "ds_read_b64_tr_b16 a[186:187], v227 offset:960\n"
    "v_mfma_f32_16x16x32_bf16 v[136:139], a[168:171], a[140:143], v[136:139]\n"
    "v_mfma_f32_16x16x32_bf16 v[124:127], a[172:175], a[132:135], v[124:127]\n"
    "ds_read_b64_tr_b16 a[188:189], v227 offset:480\n"
    "v_mfma_f32_16x16x32_bf16 v[132:135], a[172:175], a[136:139], v[132:135]\n"
    "ds_read_b64_tr_b16 a[190:191], v227 offset:992\n"
    "v_mfma_f32_16x16x32_bf16 v[140:143], a[172:175], a[140:143], v[140:143]\n"
    "s_waitcnt lgkmcnt(4)\n"
    "s_waitcnt vmcnt(16)\n"
    "s_barrier\n"
    "v_add_u32 v227, s53, v225\n"
    "v_mfma_f32_16x16x32_bf16 v[144:147], a[176:179], a[132:135], v[144:147]\n"
    "ds_read_b64_tr_b16 a[168:169], v227\n"
    "v_mfma_f32_16x16x32_bf16 v[152:155], a[176:179], a[136:139], v[152:155]\n"
    "ds_read_b64_tr_b16 a[170:171], v227 offset:512\n"
    "v_mfma_f32_16x16x32_bf16 v[160:163], a[176:179], a[140:143], v[160:163]\n"
    "v_mfma_f32_16x16x32_bf16 v[148:151], a[180:183], a[132:135], v[148:151]\n"
    "ds_read_b64_tr_b16 a[172:173], v227 offset:32\n"
    "v_mfma_f32_16x16x32_bf16 v[156:159], a[180:183], a[136:139], v[156:159]\n"
    "ds_read_b64_tr_b16 a[174:175], v227 offset:544\n"
    "v_mfma_f32_16x16x32_bf16 v[164:167], a[180:183], a[140:143], v[164:167]\n"
    "s_waitcnt lgkmcnt(4)\n"
    "v_mfma_f32_16x16x32_bf16 v[168:171], a[184:187], a[132:135], v[168:171]\n"
    "ds_read_b64_tr_b16 a[176:177], v227 offset:64\n"
    "v_mfma_f32_16x16x32_bf16 v[176:179], a[184:187], a[136:139], v[176:179]\n"
    "ds_read_b64_tr_b16 a[178:179], v227 offset:576\n"
    "v_mfma_f32_16x16x32_bf16 v[184:187], a[184:187], a[140:143], v[184:187]\n"
    "v_mfma_f32_16x16x32_bf16 v[172:175], a[188:191], a[132:135], v[172:175]\n"
    "ds_read_b64_tr_b16 a[180:181], v227 offset:96\n"
    "v_mfma_f32_16x16x32_bf16 v[180:183], a[188:191], a[136:139], v[180:183]\n"
    "ds_read_b64_tr_b16 a[182:183], v227 offset:608\n"
    "v_mfma_f32_16x16x32_bf16 v[188:191], a[188:191], a[140:143], v[188:191]\n"
    "s_waitcnt lgkmcnt(4)\n"
    "v_mfma_f32_16x16x32_bf16 v[0:3], a[168:171], a[144:147], v[0:3]\n"
    "ds_read_b64_tr_b16 a[184:185], v227 offset:128\n"
    "v_mfma_f32_16x16x32_bf16 v[8:11], a[168:171], a[148:151], v[8:11]\n"
    "ds_read_b64_tr_b16 a[186:187], v227 offset:640\n"
    "s_add_u32 m0, s69, s70\n"
    "v_mfma_f32_16x16x32_bf16 v[16:19], a[168:171], a[152:155], v[16:19]\n"
    "buffer_load_dwordx4 v228, s[24:27], s71 offen nt lds\n"
    "v_mfma_f32_16x16x32_bf16 v[4:7], a[172:175], a[144:147], v[4:7]\n"
    "ds_read_b64_tr_b16 a[188:189], v227 offset:160\n"
    "v_mfma_f32_16x16x32_bf16 v[12:15], a[172:175], a[148:151], v[12:15]\n"
    "ds_read_b64_tr_b16 a[190:191], v227 offset:672\n"
    "v_mfma_f32_16x16x32_bf16 v[20:23], a[172:175], a[152:155], v[20:23]\n"
    "s_waitcnt lgkmcnt(4)\n"
    "buffer_load_dwordx4 v229, s[24:27], s71 offen offset:1056 nt lds\n"
    "v_mfma_f32_16x16x32_bf16 v[24:27], a[176:179], a[144:147], v[24:27]\n"
    "ds_read_b64_tr_b16 a[168:169], v227 offset:192\n"
    "v_mfma_f32_16x16x32_bf16 v[32:35], a[176:179], a[148:151], v[32:35]\n"
    "ds_read_b64_tr_b16 a[170:171], v227 offset:704\n"
    "v_mfma_f32_16x16x32_bf16 v[40:43], a[176:179], a[152:155], v[40:43]\n"
    "buffer_load_dwordx4 v230, s[24:27], s71 offen offset:2112 nt lds\n"
    "v_mfma_f32_16x16x32_bf16 v[28:31], a[180:183], a[144:147], v[28:31]\n"
    "ds_read_b64_tr_b16 a[172:173], v227 offset:224\n"
    "v_mfma_f32_16x16x32_bf16 v[36:39], a[180:183], a[148:151], v[36:39]\n"
    "ds_read_b64_tr_b16 a[174:175], v227 offset:736\n"
    "v_mfma_f32_16x16x32_bf16 v[44:47], a[180:183], a[152:155], v[44:47]\n"
    "s_waitcnt lgkmcnt(4)\n"
    "buffer_load_dwordx4 v231, s[24:27], s71 offen offset:3168 nt lds\n"
    "v_mfma_f32_16x16x32_bf16 v[48:51], a[184:187], a[144:147], v[48:51]\n"
    "ds_read_b64_tr_b16 a[176:177], v227 offset:256\n"
    "s_add_u32 s69, s69, 16896\n"
    "v_mfma_f32_16x16x32_bf16 v[56:59], a[184:187], a[148:151], v[56:59]\n"
    "ds_read_b64_tr_b16 a[178:179], v227 offset:768\n"
    "v_mfma_f32_16x16x32_bf16 v[64:67], a[184:187], a[152:155], v[64:67]\n"
    "s_cmp_eq_u32 s69, 152064\n"
    "s_cselect_b32 s69, 0, s69\n"
    "v_mfma_f32_16x16x32_bf16 v[52:55], a[188:191], a[144:147], v[52:55]\n"
    "ds_read_b64_tr_b16 a[180:181], v227 offset:288\n"
    "s_add_u32 s71, s71, 16384\n"
    "v_mfma_f32_16x16x32_bf16 v[60:63], a[188:191], a[148:151], v[60:63]\n"
    "ds_read_b64_tr_b16 a[182:183], v227 offset:800\n"
    "v_mfma_f32_16x16x32_bf16 v[68:71], a[188:191], a[152:155], v[68:71]\n"
    "s_waitcnt lgkmcnt(4)\n"
    "v_mfma_f32_16x16x32_bf16 v[72:75], a[168:171], a[144:147], v[72:75]\n"
    "ds_read_b64_tr_b16 a[184:185], v227 offset:320\n"
    "v_mfma_f32_16x16x32_bf16 v[80:83], a[168:171], a[148:151], v[80:83]\n"
    "ds_read_b64_tr_b16 a[186:187], v227 offset:832\n"
    "v_mfma_f32_16x16x32_bf16 v[88:91], a[168:171], a[152:155], v[88:91]\n"
    "v_mfma_f32_16x16x32_bf16 v[76:79], a[172:175], a[144:147], v[76:79]\n"
    "ds_read_b64_tr_b16 a[188:189], v227 offset:352\n"
    "v_mfma_f32_16x16x32_bf16 v[84:87], a[172:175], a[148:151], v[84:87]\n"
    "ds_read_b64_tr_b16 a[190:191], v227 offset:864\n"
    "v_mfma_f32_16x16x32_bf16 v[92:95], a[172:175], a[152:155], v[92:95]\n"
    "s_waitcnt lgkmcnt(4)\n"
    "v_mfma_f32_16x16x32_bf16 v[96:99], a[176:179], a[144:147], v[96:99]\n"
    "ds_read_b64_tr_b16 a[168:169], v227 offset:384\n"
    "v_mfma_f32_16x16x32_bf16 v[104:107], a[176:179], a[148:151], v[104:107]\n"
    "ds_read_b64_tr_b16 a[170:171], v227 offset:896\n"
    "v_mfma_f32_16x16x32_bf16 v[112:115], a[176:179], a[152:155], v[112:115]\n"
    "v_mfma_f32_16x16x32_bf16 v[100:103], a[180:183], a[144:147], v[100:103]\n"
    "ds_read_b64_tr_b16 a[172:173], v227 offset:416\n"
    "v_mfma_f32_16x16x32_bf16 v[108:111], a[180:183], a[148:151], v[108:111]\n"
    "ds_read_b64_tr_b16 a[174:175], v227 offset:928\n"
    "v_mfma_f32_16x16x32_bf16 v[116:119], a[180:183], a[152:155], v[116:119]\n"
    "s_waitcnt lgkmcnt(4)\n"
    "v_mfma_f32_16x16x32_bf16 v[120:123], a[184:187], a[144:147], v[120:123]\n"
    "ds_read_b64_tr_b16 a[176:177], v227 offset:448\n"
    "v_mfma_f32_16x16x32_bf16 v[128:131], a[184:187], a[148:151], v[128:131]\n"
    "ds_read_b64_tr_b16 a[178:179], v227 offset:960\n"
    "v_mfma_f32_16x16x32_bf16 v[136:139], a[184:187], a[152:155], v[136:139]\n"
    "v_mfma_f32_16x16x32_bf16 v[124:127], a[188:191], a[144:147], v[124:127]\n"
    "ds_read_b64_tr_b16 a[180:181], v227 offset:480\n"
    "v_mfma_f32_16x16x32_bf16 v[132:135], a[188:191], a[148:151], v[132:135]\n"
    "ds_read_b64_tr_b16 a[182:183], v227 offset:992\n"
    "v_mfma_f32_16x16x32_bf16 v[140:143], a[188:191], a[152:155], v[140:143]\n"
    "s_waitcnt lgkmcnt(4)\n"
    "s_waitcnt vmcnt(16)\n"
    "s_barrier\n"
    "v_add_u32 v227, s54, v225\n"
    "v_mfma_f32_16x16x32_bf16 v[144:147], a[168:171], a[144:147], v[144:147]\n"
    "ds_read_b64_tr_b16 a[184:185], v227\n"
    "v_mfma_f32_16x16x32_bf16 v[152:155], a[168:171], a[148:151], v[152:155]\n"
    "ds_read_b64_tr_b16 a[186:187], v227 offset:512\n"
    "v_mfma_f32_16x16x32_bf16 v[160:163], a[168:171], a[152:155], v[160:163]\n"
    "v_mfma_f32_16x16x32_bf16 v[148:151], a[172:175], a[144:147], v[148:151]\n"
    "ds_read_b64_tr_b16 a[188:189], v227 offset:32\n"
    "v_mfma_f32_16x16x32_bf16 v[156:159], a[172:175], a[148:151], v[156:159]\n"
    "ds_read_b64_tr_b16 a[190:191], v227 offset:544\n"
    "v_mfma_f32_16x16x32_bf16 v[164:167], a[172:175], a[152:155], v[164:167]\n"
    "s_waitcnt lgkmcnt(4)\n"
    "v_mfma_f32_16x16x32_bf16 v[168:171], a[176:179], a[144:147], v[168:171]\n"
    "ds_read_b64_tr_b16 a[168:169], v227 offset:64\n"
    "v_mfma_f32_16x16x32_bf16 v[176:179], a[176:179], a[148:151], v[176:179]\n"
    "ds_read_b64_tr_b16 a[170:171], v227 offset:576\n"
    "v_mfma_f32_16x16x32_bf16 v[184:187], a[176:179], a[152:155], v[184:187]\n"
    "v_mfma_f32_16x16x32_bf16 v[172:175], a[180:183], a[144:147], v[172:175]\n"
    "ds_read_b64_tr_b16 a[172:173], v227 offset:96\n"
    "v_mfma_f32_16x16x32_bf16 v[180:183], a[180:183], a[148:151], v[180:183]\n"
    "ds_read_b64_tr_b16 a[174:175], v227 offset:608\n"
    "v_mfma_f32_16x16x32_bf16 v[188:191], a[180:183], a[152:155], v[188:191]\n"
    "s_waitcnt lgkmcnt(4)\n"
    "v_mfma_f32_16x16x32_bf16 v[0:3], a[184:187], a[156:159], v[0:3]\n"
    "ds_read_b64_tr_b16 a[176:177], v227 offset:128\n"
    "v_mfma_f32_16x16x32_bf16 v[8:11], a[184:187], a[160:163], v[8:11]\n"
    "ds_read_b64_tr_b16 a[178:179], v227 offset:640\n"
    "s_add_u32 m0, s69, s70\n"
    "v_mfma_f32_16x16x32_bf16 v[16:19], a[184:187], a[164:167], v[16:19]\n"
    "buffer_load_dwordx4 v228, s[28:31], s72 offen nt lds\n"
    "v_mfma_f32_16x16x32_bf16 v[4:7], a[188:191], a[156:159], v[4:7]\n"
    "ds_read_b64_tr_b16 a[180:181], v227 offset:160\n"
    "v_mfma_f32_16x16x32_bf16 v[12:15], a[188:191], a[160:163], v[12:15]\n"
    "ds_read_b64_tr_b16 a[182:183], v227 offset:672\n"
    "v_mfma_f32_16x16x32_bf16 v[20:23], a[188:191], a[164:167], v[20:23]\n"
    "s_waitcnt lgkmcnt(4)\n"
    "buffer_load_dwordx4 v229, s[28:31], s72 offen offset:1056 nt lds\n"
    "v_mfma_f32_16x16x32_bf16 v[24:27], a[168:171], a[156:159], v[24:27]\n"
    "ds_read_b64_tr_b16 a[184:185], v227 offset:192\n"
    "v_mfma_f32_16x16x32_bf16 v[32:35], a[168:171], a[160:163], v[32:35]\n"
    "ds_read_b64_tr_b16 a[186:187], v227 offset:704\n"
    "v_mfma_f32_16x16x32_bf16 v[40:43], a[168:171], a[164:167], v[40:43]\n"
    "buffer_load_dwordx4 v230, s[28:31], s72 offen offset:2112 nt lds\n"
    "v_mfma_f32_16x16x32_bf16 v[28:31], a[172:175], a[156:159], v[28:31]\n"
    "ds_read_b64_tr_b16 a[188:189], v227 offset:224\n"
    "v_mfma_f32_16x16x32_bf16 v[36:39], a[172:175], a[160:163], v[36:39]\n"
    "ds_read_b64_tr_b16 a[190:191], v227 offset:736\n"
    "v_mfma_f32_16x16x32_bf16 v[44:47], a[172:175], a[164:167], v[44:47]\n"
    "s_waitcnt lgkmcnt(4)\n"
    "buffer_load_dwordx4 v231, s[28:31], s72 offen offset:3168 nt lds\n"
    "v_mfma_f32_16x16x32_bf16 v[48:51], a[176:179], a[156:159], v[48:51]\n"
    "ds_read_b64_tr_b16 a[168:169], v227 offset:256\n"
    "s_add_u32 s69, s69, 16896\n"
    "v_mfma_f32_16x16x32_bf16 v[56:59], a[176:179], a[160:163], v[56:59]\n"
    "ds_read_b64_tr_b16 a[170:171], v227 offset:768\n"
    "v_mfma_f32_16x16x32_bf16 v[64:67], a[176:179], a[164:167], v[64:67]\n"
    "s_cmp_eq_u32 s69, 152064\n"
    "s_cselect_b32 s69, 0, s69\n"
    "v_mfma_f32_16x16x32_bf16 v[52:55], a[180:183], a[156:159], v[52:55]\n"
    "ds_read_b64_tr_b16 a[172:173], v227 offset:288\n"
    "s_add_u32 s72, s72, 16384\n"
    "v_mfma_f32_16x16x32_bf16 v[60:63], a[180:183], a[160:163], v[60:63]\n"
    "ds_read_b64_tr_b16 a[174:175], v227 offset:800\n"
    "v_mfma_f32_16x16x32_bf16 v[68:71], a[180:183], a[164:167], v[68:71]\n"
    "s_waitcnt lgkmcnt(4)\n"
    "v_mfma_f32_16x16x32_bf16 v[72:75], a[184:187], a[156:159], v[72:75]\n"
    "ds_read_b64_tr_b16 a[176:177], v227 offset:320\n"
    "v_mfma_f32_16x16x32_bf16 v[80:83], a[184:187], a[160:163], v[80:83]\n"
    "ds_read_b64_tr_b16 a[178:179], v227 offset:832\n"
    "v_mfma_f32_16x16x32_bf16 v[88:91], a[184:187], a[164:167], v[88:91]\n"
    "v_mfma_f32_16x16x32_bf16 v[76:79], a[188:191], a[156:159], v[76:79]\n"
    "ds_read_b64_tr_b16 a[180:181], v227 offset:352\n"
    "v_mfma_f32_16x16x32_bf16 v[84:87], a[188:191], a[160:163], v[84:87]\n"
    "ds_read_b64_tr_b16 a[182:183], v227 offset:864\n"
    "v_mfma_f32_16x16x32_bf16 v[92:95], a[188:191], a[164:167], v[92:95]\n"
    "s_waitcnt lgkmcnt(4)\n"
    "v_mfma_f32_16x16x32_bf16 v[96:99], a[168:171], a[156:159], v[96:99]\n"
    "ds_read_b64_tr_b16 a[184:185], v227 offset:384\n"
    "v_mfma_f32_16x16x32_bf16 v[104:107], a[168:171], a[160:163], v[104:107]\n"
    "ds_read_b64_tr_b16 a[186:187], v227 offset:896\n"
    "v_mfma_f32_16x16x32_bf16 v[112:115], a[168:171], a[164:167], v[112:115]\n"
    "v_mfma_f32_16x16x32_bf16 v[100:103], a[172:175], a[156:159], v[100:103]\n"
    "ds_read_b64_tr_b16 a[188:189], v227 offset:416\n"
    "v_mfma_f32_16x16x32_bf16 v[108:111], a[172:175], a[160:163], v[108:111]\n"
    "ds_read_b64_tr_b16 a[190:191], v227 offset:928\n"
    "v_mfma_f32_16x16x32_bf16 v[116:119], a[172:175], a[164:167], v[116:119]\n"
    "s_waitcnt lgkmcnt(4)\n"
    "v_mfma_f32_16x16x32_bf16 v[120:123], a[176:179], a[156:159], v[120:123]\n"
    "ds_read_b64_tr_b16 a[168:169], v227 offset:448\n"
    "v_mfma_f32_16x16x32_bf16 v[128:131], a[176:179], a[160:163], v[128:131]\n"
    "ds_read_b64_tr_b16 a[170:171], v227 offset:960\n"
    "v_mfma_f32_16x16x32_bf16 v[136:139], a[176:179], a[164:167], v[136:139]\n"
    "v_mfma_f32_16x16x32_bf16 v[124:127], a[180:183], a[156:159], v[124:127]\n"
    "ds_read_b64_tr_b16 a[172:173], v227 offset:480\n"
    "v_mfma_f32_16x16x32_bf16 v[132:135], a[180:183], a[160:163], v[132:135]\n"
    "ds_read_b64_tr_b16 a[174:175], v227 offset:992\n"
    "v_mfma_f32_16x16x32_bf16 v[140:143], a[180:183], a[164:167], v[140:143]\n"
    "s_waitcnt lgkmcnt(4)\n"
    "v_mfma_f32_16x16x32_bf16 v[144:147], a[184:187], a[156:159], v[144:147]\n"
    "v_mfma_f32_16x16x32_bf16 v[152:155], a[184:187], a[160:163], v[152:155]\n"
    "v_mfma_f32_16x16x32_bf16 v[160:163], a[184:187], a[164:167], v[160:163]\n"
    "v_mfma_f32_16x16x32_bf16 v[148:151], a[188:191], a[156:159], v[148:151]\n"
    "v_mfma_f32_16x16x32_bf16 v[156:159], a[188:191], a[160:163], v[156:159]\n"
    "v_mfma_f32_16x16x32_bf16 v[164:167], a[188:191], a[164:167], v[164:167]\n"
    "s_waitcnt lgkmcnt(0)\n"
    "v_mfma_f32_16x16x32_bf16 v[168:171], a[168:171], a[156:159], v[168:171]\n"
    "v_mfma_f32_16x16x32_bf16 v[176:179], a[168:171], a[160:163], v[176:179]\n"
    "v_mfma_f32_16x16x32_bf16 v[184:187], a[168:171], a[164:167], v[184:187]\n"
    "v_mfma_f32_16x16x32_bf16 v[172:175], a[172:175], a[156:159], v[172:175]\n"
    "v_mfma_f32_16x16x32_bf16 v[180:183], a[172:175], a[160:163], v[180:183]\n"
    "v_mfma_f32_16x16x32_bf16 v[188:191], a[172:175], a[164:167], v[188:191]\n"
    "s_cmp_eq_u32 s67, 0\n"
    "s_cbranch_scc0 LCQ1\n"
    "s_lshl_b32 s73, s64, 16\n"
    "s_lshl_b32 s79, s66, 10\n"
    "s_add_u32 s73, s73, s79\n"
    "s_add_u32 s73, s73, 512\n"
    "v_lshrrev_b32 v241, 4, v231\n"
    "v_lshlrev_b32 v244, 11, v241\n"
    "v_cmp_gt_u32 vcc, 7, v241\n"
    "v_mov_b32 v242, 0x7fff0000\n"
    "s_nop 0\n"
    "v_cndmask_b32 v244, v242, v244, vcc\n"
    "v_cmp_ne_u32 vcc, s66, v241\n"
    "s_nop 1\n"
    "v_cndmask_b32 v244, v242, v244, vcc\n"
    "v_lshrrev_b32 v241, 4, v231\n"
    "v_lshlrev_b32 v245, 8, v241\n"
    "s_cmp_lt_u32 s66, 7\n"
    "s_cselect_b32 s80, 8, 0\n"
    "v_cmp_gt_u32 vcc, s80, v241\n"
    "v_mov_b32 v242, 0x7fff0000\n"
    "s_nop 0\n"
    "v_cndmask_b32 v245, v242, v245, vcc\n"
    "v_cmp_ne_u32 vcc, s66, v241\n"
    "s_nop 1\n"
    "v_cndmask_b32 v245, v242, v245, vcc\n"
    "v_add_u32 v236, 1, v236\n"
    "v_cmp_gt_u32 vcc, 0x7fff0000, v244\n"
    "s_nop 1\n"
    "v_cndmask_b32 v236, 0, v236, vcc\n"
    "v_add_u32 v237, 1, v237\n"
    "v_cmp_gt_u32 vcc, 0x7fff0000, v245\n"
    "s_nop 1\n"
    "v_cndmask_b32 v237, 0, v237, vcc\n"
    "s_add_u32 s79, s37, 524288\n"
    "buffer_store_dword v237, v245, s[48:51], s79 offen sc1\n"
    "s_branch LCQ2\n"
    "LCQ1:\n"
    "s_sub_u32 s56, %[wolo], 480\n"
    "s_subb_u32 s57, %[wohi], 0\n"
    "s_and_b32 s57, s57, 65535\n"
    "s_mov_b32 s58, 3670496\n"
    "s_mov_b32 s59, 131072\n"
    "v_and_b32 v241, 112, v231\n"
    "v_lshrrev_b32 v242, 7, v231\n"
    "v_mul_u32_u24 v242, 14336, v242\n"
    "v_add_u32 v241, v241, v242\n"
    "v_add_u32 v232, 480, v241\n"
    "v_add_u32 v233, 320, v241\n"
    "v_add_u32 v234, 160, v241\n"
    "v_add_u32 v235, 0, v241\n"
    "v_lshrrev_b32 v241, 4, v231\n"
    "v_lshlrev_b32 v244, 11, v241\n"
    "v_cmp_gt_u32 vcc, 7, v241\n"
    "v_mov_b32 v242, 0x7fff0000\n"
    "s_nop 0\n"
    "v_cndmask_b32 v244, v242, v244, vcc\n"
    "v_cmp_ne_u32 vcc, s66, v241\n"
    "s_nop 1\n"
    "v_cndmask_b32 v244, v242, v244, vcc\n"
    "s_add_u32 s79, s36, 524288\n"
    "buffer_load_dword v238, v244, s[48:51], s79 offen sc1\n"
    "s_add_u32 s91, s66, 1\n"
    "s_cmp_ge_u32 s91, 7\n"
    "s_cselect_b32 s79, 7, 0\n"
    "s_sub_u32 s91, s91, s79\n"
    "s_mov_b32 s93, s91\n"
    "s_lshl_b32 s79, s66, 9\n"
    "s_lshl_b32 s80, s64, 2\n"
    "s_add_u32 s79, s79, s80\n"
    "s_mul_i32 s92, s79, 896\n"
    "s_lshl_b32 s79, s91, 7\n"
    "s_add_u32 s73, s92, s79\n"
    "s_mov_b32 s74, 114688\n"
    "s_add_u32 s91, s91, 1\n"
    "s_cmp_eq_u32 s91, 7\n"
    "s_cselect_b32 s91, 0, s91\n"
    "s_lshl_b32 s80, s91, 7\n"
    "s_sub_u32 s81, s80, s79\n"
    "s_sub_u32 s81, s81, 344064\n"
    "LCQ2:\n"
    "s_barrier\n"
    "s_nop 3\n"
    "v_pk_mul_f32 v[0:1], v[0:1], s[82:83]\n"
    "v_pk_mul_f32 v[2:3], v[2:3], s[82:83]\n"
    "v_pk_mul_f32 v[4:5], v[4:5], s[82:83]\n"
    "v_pk_mul_f32 v[6:7], v[6:7], s[82:83]\n"
    "v_cvt_pk_bf16_f32 v0, v0, v1\n"
    "v_cvt_pk_bf16_f32 v1, v2, v3\n"
    "v_cvt_pk_bf16_f32 v2, v4, v5\n"
    "v_cvt_pk_bf16_f32 v3, v6, v7\n"
    "s_add_u32 m0, s69, s70\n"
    "s_nop 0\n"
    "buffer_load_dwordx4 v228, s[24:27], s71 offen nt lds\n"
    "v_pk_mul_f32 v[8:9], v[8:9], s[82:83]\n"
    "v_pk_mul_f32 v[10:11], v[10:11], s[82:83]\n"
    "v_pk_mul_f32 v[12:13], v[12:13], s[82:83]\n"
    "v_pk_mul_f32 v[14:15], v[14:15], s[82:83]\n"
    "v_cvt_pk_bf16_f32 v4, v8, v9\n"
    "v_cvt_pk_bf16_f32 v5, v10, v11\n"
    "v_cvt_pk_bf16_f32 v6, v12, v13\n"
    "v_cvt_pk_bf16_f32 v7, v14, v15\n"
    "v_permlane32_swap_b32 v0, v2\n"
    "v_permlane32_swap_b32 v1, v3\n"
    "s_nop 0\n"
    "v_permlane16_swap_b32 v0, v2\n"
    "v_permlane16_swap_b32 v1, v3\n"
    "v_pk_mul_f32 v[16:17], v[16:17], s[82:83]\n"
    "v_pk_mul_f32 v[18:19], v[18:19], s[82:83]\n"
    "v_pk_mul_f32 v[20:21], v[20:21], s[82:83]\n"
    "v_pk_mul_f32 v[22:23], v[22:23], s[82:83]\n"
    "v_cvt_pk_bf16_f32 v8, v16, v17\n"
    "v_cvt_pk_bf16_f32 v9, v18, v19\n"
    "v_cvt_pk_bf16_f32 v10, v20, v21\n"
    "v_cvt_pk_bf16_f32 v11, v22, v23\n"
    "v_permlane32_swap_b32 v4, v6\n"
    "v_permlane32_swap_b32 v5, v7\n"
    "s_nop 0\n"
    "v_permlane16_swap_b32 v4, v6\n"
    "v_permlane16_swap_b32 v5, v7\n"
    "buffer_load_dwordx4 v229, s[24:27], s71 offen offset:1056 nt lds\n"
    "v_pk_mul_f32 v[24:25], v[24:25], s[82:83]\n"
    "v_pk_mul_f32 v[26:27], v[26:27], s[82:83]\n"
    "v_pk_mul_f32 v[28:29], v[28:29], s[82:83]\n"
    "v_pk_mul_f32 v[30:31], v[30:31], s[82:83]\n"
    "v_cvt_pk_bf16_f32 v12, v24, v25\n"
    "v_cvt_pk_bf16_f32 v13, v26, v27\n"
    "v_cvt_pk_bf16_f32 v14, v28, v29\n"
    "v_cvt_pk_bf16_f32 v15, v30, v31\n"
    "v_permlane32_swap_b32 v8, v10\n"
    "v_permlane32_swap_b32 v9, v11\n"
    "s_nop 0\n"
    "v_permlane16_swap_b32 v8, v10\n"
    "v_permlane16_swap_b32 v9, v11\n"
    "v_pk_mul_f32 v[32:33], v[32:33], s[82:83]\n"
    "v_pk_mul_f32 v[34:35], v[34:35], s[82:83]\n"
    "v_pk_mul_f32 v[36:37], v[36:37], s[82:83]\n"
    "v_pk_mul_f32 v[38:39], v[38:39], s[82:83]\n"
    "v_cvt_pk_bf16_f32 v16, v32, v33\n"
    "v_cvt_pk_bf16_f32 v17, v34, v35\n"
    "v_cvt_pk_bf16_f32 v18, v36, v37\n"
    "v_cvt_pk_bf16_f32 v19, v38, v39\n"
    "v_permlane32_swap_b32 v12, v14\n"
    "v_permlane32_swap_b32 v13, v15\n"
    "s_nop 0\n"
    "v_permlane16_swap_b32 v12, v14\n"
    "v_permlane16_swap_b32 v13, v15\n"
    "buffer_load_dwordx4 v230, s[24:27], s71 offen offset:2112 nt lds\n"
    "v_pk_mul_f32 v[40:41], v[40:41], s[82:83]\n"
    "v_pk_mul_f32 v[42:43], v[42:43], s[82:83]\n"
    "v_pk_mul_f32 v[44:45], v[44:45], s[82:83]\n"
    "v_pk_mul_f32 v[46:47], v[46:47], s[82:83]\n"
    "v_cvt_pk_bf16_f32 v20, v40, v41\n"
    "v_cvt_pk_bf16_f32 v21, v42, v43\n"
    "v_cvt_pk_bf16_f32 v22, v44, v45\n"
    "v_cvt_pk_bf16_f32 v23, v46, v47\n"
    "v_permlane32_swap_b32 v16, v18\n"
    "v_permlane32_swap_b32 v17, v19\n"
    "s_nop 0\n"
    "v_permlane16_swap_b32 v16, v18\n"
    "v_permlane16_swap_b32 v17, v19\n"
    "v_pk_mul_f32 v[48:49], v[48:49], s[82:83]\n"
    "v_pk_mul_f32 v[50:51], v[50:51], s[82:83]\n"
    "v_pk_mul_f32 v[52:53], v[52:53], s[82:83]\n"
    "v_pk_mul_f32 v[54:55], v[54:55], s[82:83]\n"
    "v_cvt_pk_bf16_f32 v24, v48, v49\n"
    "v_cvt_pk_bf16_f32 v25, v50, v51\n"
    "v_cvt_pk_bf16_f32 v26, v52, v53\n"
    "v_cvt_pk_bf16_f32 v27, v54, v55\n"
    "v_permlane32_swap_b32 v20, v22\n"
    "v_permlane32_swap_b32 v21, v23\n"
    "s_nop 0\n"
    "v_permlane16_swap_b32 v20, v22\n"
    "v_permlane16_swap_b32 v21, v23\n"
    "buffer_load_dwordx4 v231, s[24:27], s71 offen offset:3168 nt lds\n"
    "v_pk_mul_f32 v[56:57], v[56:57], s[82:83]\n"
    "v_pk_mul_f32 v[58:59], v[58:59], s[82:83]\n"
    "v_pk_mul_f32 v[60:61], v[60:61], s[82:83]\n"
    "v_pk_mul_f32 v[62:63], v[62:63], s[82:83]\n"
    "v_cvt_pk_bf16_f32 v28, v56, v57\n"
    "v_cvt_pk_bf16_f32 v29, v58, v59\n"
    "v_cvt_pk_bf16_f32 v30, v60, v61\n"
    "v_cvt_pk_bf16_f32 v31, v62, v63\n"
    "v_permlane32_swap_b32 v24, v26\n"
    "v_permlane32_swap_b32 v25, v27\n"
    "s_nop 0\n"
    "v_permlane16_swap_b32 v24, v26\n"
    "v_permlane16_swap_b32 v25, v27\n"
    "v_pk_mul_f32 v[64:65], v[64:65], s[82:83]\n"
    "v_pk_mul_f32 v[66:67], v[66:67], s[82:83]\n"
    "v_pk_mul_f32 v[68:69], v[68:69], s[82:83]\n"
    "v_pk_mul_f32 v[70:71], v[70:71], s[82:83]\n"
    "v_cvt_pk_bf16_f32 v32, v64, v65\n"
    "v_cvt_pk_bf16_f32 v33, v66, v67\n"
    "v_cvt_pk_bf16_f32 v34, v68, v69\n"
    "v_cvt_pk_bf16_f32 v35, v70, v71\n"
    "v_permlane32_swap_b32 v28, v30\n"
    "v_permlane32_swap_b32 v29, v31\n"
    "s_nop 0\n"
    "v_permlane16_swap_b32 v28, v30\n"
    "v_permlane16_swap_b32 v29, v31\n"
    "s_add_u32 s69, s69, 16896\n"
    "s_cmp_eq_u32 s69, 152064\n"
    "s_cselect_b32 s69, 0, s69\n"
    "s_add_u32 s71, s71, 16384\n"
    "s_add_u32 m0, s69, s70\n"
    "s_nop 0\n"
    "buffer_load_dwordx4 v228, s[28:31], s72 offen nt lds\n"
    "v_pk_mul_f32 v[72:73], v[72:73], s[82:83]\n"
    "v_pk_mul_f32 v[74:75], v[74:75], s[82:83]\n"
    "v_pk_mul_f32 v[76:77], v[76:77], s[82:83]\n"
    "v_pk_mul_f32 v[78:79], v[78:79], s[82:83]\n"
    "v_cvt_pk_bf16_f32 v36, v72, v73\n"
    "v_cvt_pk_bf16_f32 v37, v74, v75\n"
    "v_cvt_pk_bf16_f32 v38, v76, v77\n"
    "v_cvt_pk_bf16_f32 v39, v78, v79\n"
    "v_permlane32_swap_b32 v32, v34\n"
    "v_permlane32_swap_b32 v33, v35\n"
    "s_nop 0\n"
    "v_permlane16_swap_b32 v32, v34\n"
    "v_permlane16_swap_b32 v33, v35\n"
    "v_pk_mul_f32 v[80:81], v[80:81], s[82:83]\n"
    "v_pk_mul_f32 v[82:83], v[82:83], s[82:83]\n"
    "v_pk_mul_f32 v[84:85], v[84:85], s[82:83]\n"
    "v_pk_mul_f32 v[86:87], v[86:87], s[82:83]\n"
    "v_cvt_pk_bf16_f32 v40, v80, v81\n"
    "v_cvt_pk_bf16_f32 v41, v82, v83\n"
    "v_cvt_pk_bf16_f32 v42, v84, v85\n"
    "v_cvt_pk_bf16_f32 v43, v86, v87\n"
    "v_permlane32_swap_b32 v36, v38\n"
    "v_permlane32_swap_b32 v37, v39\n"
    "s_nop 0\n"
    "v_permlane16_swap_b32 v36, v38\n"
    "v_permlane16_swap_b32 v37, v39\n"
    "buffer_load_dwordx4 v229, s[28:31], s72 offen offset:1056 nt lds\n"
    "v_pk_mul_f32 v[88:89], v[88:89], s[82:83]\n"
    "v_pk_mul_f32 v[90:91], v[90:91], s[82:83]\n"
    "v_pk_mul_f32 v[92:93], v[92:93], s[82:83]\n"
    "v_pk_mul_f32 v[94:95], v[94:95], s[82:83]\n"
    "v_cvt_pk_bf16_f32 v44, v88, v89\n"
    "v_cvt_pk_bf16_f32 v45, v90, v91\n"
    "v_cvt_pk_bf16_f32 v46, v92, v93\n"
    "v_cvt_pk_bf16_f32 v47, v94, v95\n"
    "v_permlane32_swap_b32 v40, v42\n"
    "v_permlane32_swap_b32 v41, v43\n"
    "s_nop 0\n"
    "v_permlane16_swap_b32 v40, v42\n"
    "v_permlane16_swap_b32 v41, v43\n"
    "v_pk_mul_f32 v[96:97], v[96:97], s[82:83]\n"
    "v_pk_mul_f32 v[98:99], v[98:99], s[82:83]\n"
    "v_pk_mul_f32 v[100:101], v[100:101], s[82:83]\n"
    "v_pk_mul_f32 v[102:103], v[102:103], s[82:83]\n"
    "v_cvt_pk_bf16_f32 v48, v96, v97\n"
    "v_cvt_pk_bf16_f32 v49, v98, v99\n"
    "v_cvt_pk_bf16_f32 v50, v100, v101\n"
    "v_cvt_pk_bf16_f32 v51, v102, v103\n"
    "v_permlane32_swap_b32 v44, v46\n"
    "v_permlane32_swap_b32 v45, v47\n"
    "s_nop 0\n"
    "v_permlane16_swap_b32 v44, v46\n"
    "v_permlane16_swap_b32 v45, v47\n"
    "buffer_load_dwordx4 v230, s[28:31], s72 offen offset:2112 nt lds\n"
    "v_pk_mul_f32 v[104:105], v[104:105], s[82:83]\n"
    "v_pk_mul_f32 v[106:107], v[106:107], s[82:83]\n"
    "v_pk_mul_f32 v[108:109], v[108:109], s[82:83]\n"
    "v_pk_mul_f32 v[110:111], v[110:111], s[82:83]\n"
    "v_cvt_pk_bf16_f32 v52, v104, v105\n"
    "v_cvt_pk_bf16_f32 v53, v106, v107\n"
    "v_cvt_pk_bf16_f32 v54, v108, v109\n"
    "v_cvt_pk_bf16_f32 v55, v110, v111\n"
    "v_permlane32_swap_b32 v48, v50\n"
    "v_permlane32_swap_b32 v49, v51\n"
    "s_nop 0\n"
    "v_permlane16_swap_b32 v48, v50\n"
    "v_permlane16_swap_b32 v49, v51\n"
    "v_pk_mul_f32 v[112:113], v[112:113], s[82:83]\n"
    "v_pk_mul_f32 v[114:115], v[114:115], s[82:83]\n"
    "v_pk_mul_f32 v[116:117], v[116:117], s[82:83]\n"
    "v_pk_mul_f32 v[118:119], v[118:119], s[82:83]\n"
    "v_cvt_pk_bf16_f32 v56, v112, v113\n"
    "v_cvt_pk_bf16_f32 v57, v114, v115\n"
    "v_cvt_pk_bf16_f32 v58, v116, v117\n"
    "v_cvt_pk_bf16_f32 v59, v118, v119\n"
    "v_permlane32_swap_b32 v52, v54\n"
    "v_permlane32_swap_b32 v53, v55\n"
    "s_nop 0\n"
    "v_permlane16_swap_b32 v52, v54\n"
    "v_permlane16_swap_b32 v53, v55\n"
    "buffer_load_dwordx4 v231, s[28:31], s72 offen offset:3168 nt lds\n"
    "v_pk_mul_f32 v[120:121], v[120:121], s[82:83]\n"
    "v_pk_mul_f32 v[122:123], v[122:123], s[82:83]\n"
    "v_pk_mul_f32 v[124:125], v[124:125], s[82:83]\n"
    "v_pk_mul_f32 v[126:127], v[126:127], s[82:83]\n"
    "v_cvt_pk_bf16_f32 v60, v120, v121\n"
    "v_cvt_pk_bf16_f32 v61, v122, v123\n"
    "v_cvt_pk_bf16_f32 v62, v124, v125\n"
    "v_cvt_pk_bf16_f32 v63, v126, v127\n"
    "v_permlane32_swap_b32 v56, v58\n"
    "v_permlane32_swap_b32 v57, v59\n"
    "s_nop 0\n"
    "v_permlane16_swap_b32 v56, v58\n"
    "v_permlane16_swap_b32 v57, v59\n"
    "v_pk_mul_f32 v[128:129], v[128:129], s[82:83]\n"
    "v_pk_mul_f32 v[130:131], v[130:131], s[82:83]\n"
    "v_pk_mul_f32 v[132:133], v[132:133], s[82:83]\n"
    "v_pk_mul_f32 v[134:135], v[134:135], s[82:83]\n"
    "v_cvt_pk_bf16_f32 v64, v128, v129\n"
    "v_cvt_pk_bf16_f32 v65, v130, v131\n"
    "v_cvt_pk_bf16_f32 v66, v132, v133\n"
    "v_cvt_pk_bf16_f32 v67, v134, v135\n"
    "v_permlane32_swap_b32 v60, v62\n"
    "v_permlane32_swap_b32 v61, v63\n"
    "s_nop 0\n"
    "v_permlane16_swap_b32 v60, v62\n"
    "v_permlane16_swap_b32 v61, v63\n"
    "s_add_u32 s69, s69, 16896\n"
    "s_cmp_eq_u32 s69, 152064\n"
    "s_cselect_b32 s69, 0, s69\n"
    "s_add_u32 s72, s72, 16384\n"
    "v_pk_mul_f32 v[136:137], v[136:137], s[82:83]\n"
    "v_pk_mul_f32 v[138:139], v[138:139], s[82:83]\n"
    "v_pk_mul_f32 v[140:141], v[140:141], s[82:83]\n"
    "v_pk_mul_f32 v[142:143], v[142:143], s[82:83]\n"
    "v_cvt_pk_bf16_f32 v68, v136, v137\n"
    "v_cvt_pk_bf16_f32 v69, v138, v139\n"
    "v_cvt_pk_bf16_f32 v70, v140, v141\n"
    "v_cvt_pk_bf16_f32 v71, v142, v143\n"
    "v_permlane32_swap_b32 v64, v66\n"
    "v_permlane32_swap_b32 v65, v67\n"
    "s_nop 0\n"
    "v_permlane16_swap_b32 v64, v66\n"
    "v_permlane16_swap_b32 v65, v67\n"
    "v_pk_mul_f32 v[144:145], v[144:145], s[82:83]\n"
    "v_pk_mul_f32 v[146:147], v[146:147], s[82:83]\n"
    "v_pk_mul_f32 v[148:149], v[148:149], s[82:83]\n"
    "v_pk_mul_f32 v[150:151], v[150:151], s[82:83]\n"
    "v_cvt_pk_bf16_f32 v72, v144, v145\n"
    "v_cvt_pk_bf16_f32 v73, v146, v147\n"
    "v_cvt_pk_bf16_f32 v74, v148, v149\n"
    "v_cvt_pk_bf16_f32 v75, v150, v151\n"
    "v_permlane32_swap_b32 v68, v70\n"
    "v_permlane32_swap_b32 v69, v71\n"
    "s_nop 0\n"
    "v_permlane16_swap_b32 v68, v70\n"
    "v_permlane16_swap_b32 v69, v71\n"
    "v_pk_mul_f32 v[152:153], v[152:153], s[82:83]\n"
    "v_pk_mul_f32 v[154:155], v[154:155], s[82:83]\n"
    "v_pk_mul_f32 v[156:157], v[156:157], s[82:83]\n"
    "v_pk_mul_f32 v[158:159], v[158:159], s[82:83]\n"
    "v_cvt_pk_bf16_f32 v76, v152, v153\n"
    "v_cvt_pk_bf16_f32 v77, v154, v155\n"
    "v_cvt_pk_bf16_f32 v78, v156, v157\n"
    "v_cvt_pk_bf16_f32 v79, v158, v159\n"
    "v_permlane32_swap_b32 v72, v74\n"
    "v_permlane32_swap_b32 v73, v75\n"
    "s_nop 0\n"
    "v_permlane16_swap_b32 v72, v74\n"
    "v_permlane16_swap_b32 v73, v75\n"
    "v_pk_mul_f32 v[160:161], v[160:161], s[82:83]\n"
    "v_pk_mul_f32 v[162:163], v[162:163], s[82:83]\n"
    "v_pk_mul_f32 v[164:165], v[164:165], s[82:83]\n"
    "v_pk_mul_f32 v[166:167], v[166:167], s[82:83]\n"
    "v_cvt_pk_bf16_f32 v80, v160, v161\n"
    "v_cvt_pk_bf16_f32 v81, v162, v163\n"
    "v_cvt_pk_bf16_f32 v82, v164, v165\n"
    "v_cvt_pk_bf16_f32 v83, v166, v167\n"
    "v_permlane32_swap_b32 v76, v78\n"
    "v_permlane32_swap_b32 v77, v79\n"
    "s_nop 0\n"
    "v_permlane16_swap_b32 v76, v78\n"
    "v_permlane16_swap_b32 v77, v79\n"
    "v_pk_mul_f32 v[168:169], v[168:169], s[82:83]\n"
    "v_pk_mul_f32 v[170:171], v[170:171], s[82:83]\n"
    "v_pk_mul_f32 v[172:173], v[172:173], s[82:83]\n"
    "v_pk_mul_f32 v[174:175], v[174:175], s[82:83]\n"
    "v_cvt_pk_bf16_f32 v84, v168, v169\n"
    "v_cvt_pk_bf16_f32 v85, v170, v171\n"
    "v_cvt_pk_bf16_f32 v86, v172, v173\n"
    "v_cvt_pk_bf16_f32 v87, v174, v175\n"
    "v_permlane32_swap_b32 v80, v82\n"
    "v_permlane32_swap_b32 v81, v83\n"
    "s_nop 0\n"
    "v_permlane16_swap_b32 v80, v82\n"
    "v_permlane16_swap_b32 v81, v83\n"
    "v_pk_mul_f32 v[176:177], v[176:177], s[82:83]\n"
    "v_pk_mul_f32 v[178:179], v[178:179], s[82:83]\n"
    "v_pk_mul_f32 v[180:181], v[180:181], s[82:83]\n"
    "v_pk_mul_f32 v[182:183], v[182:183], s[82:83]\n"
    "v_cvt_pk_bf16_f32 v88, v176, v177\n"
    "v_cvt_pk_bf16_f32 v89, v178, v179\n"
    "v_cvt_pk_bf16_f32 v90, v180, v181\n"
    "v_cvt_pk_bf16_f32 v91, v182, v183\n"
    "v_permlane32_swap_b32 v84, v86\n"
    "v_permlane32_swap_b32 v85, v87\n"
    "s_nop 0\n"
    "v_permlane16_swap_b32 v84, v86\n"
    "v_permlane16_swap_b32 v85, v87\n"
    "v_pk_mul_f32 v[184:185], v[184:185], s[82:83]\n"
    "v_pk_mul_f32 v[186:187], v[186:187], s[82:83]\n"
    "v_pk_mul_f32 v[188:189], v[188:189], s[82:83]\n"
    "v_pk_mul_f32 v[190:191], v[190:191], s[82:83]\n"
    "v_cvt_pk_bf16_f32 v92, v184, v185\n"
    "v_cvt_pk_bf16_f32 v93, v186, v187\n"
    "v_cvt_pk_bf16_f32 v94, v188, v189\n"
    "v_cvt_pk_bf16_f32 v95, v190, v191\n"
    "v_permlane32_swap_b32 v88, v90\n"
    "v_permlane32_swap_b32 v89, v91\n"
    "s_nop 0\n"
    "v_permlane16_swap_b32 v88, v90\n"
    "v_permlane16_swap_b32 v89, v91\n"
    "v_permlane32_swap_b32 v92, v94\n"
    "v_permlane32_swap_b32 v93, v95\n"
    "s_nop 0\n"
    "v_permlane16_swap_b32 v92, v94\n"
    "v_permlane16_swap_b32 v93, v95\n"
    "v_add_u32 v226, s68, v224\n"
    "s_add_u32 s68, s68, 16896\n"
    "s_cmp_eq_u32 s68, 152064\n"
    "s_cselect_b32 s68, 0, s68\n"
    "ds_read_b128 v[156:159], v226\n"
    "ds_read_b128 v[160:163], v226 offset:64\n"
    "ds_read_b128 v[164:167], v226 offset:128\n"
    "ds_read_b128 v[168:171], v226 offset:192\n"
    "ds_read_b128 v[172:175], v226 offset:256\n"
    "ds_read_b128 v[176:179], v226 offset:320\n"
    "v_mov_b32 v188, 1065369472\n"
    "v_mov_b32 v189, 1065369472\n"
    "v_mov_b32 v190, 1065369472\n"
    "v_mov_b32 v191, 1065369472\n"
    "s_waitcnt lgkmcnt(2)\n"
    "v_mfma_f32_16x16x32_bf16 v[96:99], v[156:159], v[0:3], 0\n"
    "ds_read_b128 v[180:183], v226 offset:384\n"
    "v_mfma_f32_16x16x32_bf16 v[100:103], v[156:159], v[4:7], 0\n"
    "v_mfma_f32_16x16x32_bf16 v[104:107], v[156:159], v[8:11], 0\n"
    "v_mfma_f32_16x16x32_bf16 v[96:99], v[160:163], v[12:15], v[96:99]\n"
    "ds_read_b128 v[184:187], v226 offset:448\n"
    "v_mfma_f32_16x16x32_bf16 v[100:103], v[160:163], v[16:19], v[100:103]\n"
    "v_mfma_f32_16x16x32_bf16 v[104:107], v[160:163], v[20:23], v[104:107]\n"
    "v_mfma_f32_16x16x32_bf16 v[96:99], v[164:167], v[24:27], v[96:99]\n"
    "ds_read_b128 v[156:159], v226 offset:512\n"
    "v_mfma_f32_16x16x32_bf16 v[100:103], v[164:167], v[28:31], v[100:103]\n"
    "v_mfma_f32_16x16x32_bf16 v[104:107], v[164:167], v[32:35], v[104:107]\n"
    "v_mfma_f32_16x16x32_bf16 v[96:99], v[168:171], v[36:39], v[96:99]\n"
    "ds_read_b128 v[160:163], v226 offset:576\n"
    "v_mfma_f32_16x16x32_bf16 v[100:103], v[168:171], v[40:43], v[100:103]\n"
    "v_mfma_f32_16x16x32_bf16 v[104:107], v[168:171], v[44:47], v[104:107]\n"
    "s_waitcnt lgkmcnt(2)\n"
    "v_mfma_f32_16x16x32_bf16 v[96:99], v[172:175], v[48:51], v[96:99]\n"
    "ds_read_b128 v[164:167], v226 offset:640\n"
    "v_mfma_f32_16x16x32_bf16 v[100:103], v[172:175], v[52:55], v[100:103]\n"
    "v_mfma_f32_16x16x32_bf16 v[104:107], v[172:175], v[56:59], v[104:107]\n"
    "v_mfma_f32_16x16x32_bf16 v[96:99], v[176:179], v[60:63], v[96:99]\n"
    "ds_read_b128 v[168:171], v226 offset:704\n"
    "v_mfma_f32_16x16x32_bf16 v[100:103], v[176:179], v[64:67], v[100:103]\n"
    "v_mfma_f32_16x16x32_bf16 v[104:107], v[176:179], v[68:71], v[104:107]\n"
    "v_mfma_f32_16x16x32_bf16 v[96:99], v[180:183], v[72:75], v[96:99]\n"
    "ds_read_b128 v[172:175], v226 offset:768\n"
    "v_mfma_f32_16x16x32_bf16 v[100:103], v[180:183], v[76:79], v[100:103]\n"
    "v_mfma_f32_16x16x32_bf16 v[104:107], v[180:183], v[80:83], v[104:107]\n"
    "v_mfma_f32_16x16x32_bf16 v[96:99], v[184:187], v[84:87], v[96:99]\n"
    "ds_read_b128 v[176:179], v226 offset:832\n"
    "v_mfma_f32_16x16x32_bf16 v[100:103], v[184:187], v[88:91], v[100:103]\n"
    "v_mfma_f32_16x16x32_bf16 v[104:107], v[184:187], v[92:95], v[104:107]\n"
    "s_waitcnt lgkmcnt(2)\n"
    "s_nop 3\n"
    "v_max3_f32 v108, v96, v97, v98\n"
    "v_max_f32 v108, v108, v99\n"
    "v_max3_f32 v110, v100, v101, v102\n"
    "v_max_f32 v110, v110, v103\n"
    "v_max3_f32 v112, v104, v105, v106\n"
    "v_max_f32 v112, v112, v107\n"
    "v_mov_b32 v109, v108\n"
    "v_mov_b32 v111, v110\n"
    "v_mov_b32 v113, v112\n"
    "v_permlane32_swap_b32 v108, v109\n"
    "v_permlane32_swap_b32 v110, v111\n"
    "v_permlane32_swap_b32 v112, v113\n"
    "v_max_f32 v108, v108, v109\n"
    "v_max_f32 v110, v110, v111\n"
    "v_max_f32 v112, v112, v113\n"
    "v_mov_b32 v109, v108\n"
    "v_mov_b32 v111, v110\n"
    "v_mov_b32 v113, v112\n"
    "v_permlane16_swap_b32 v108, v109\n"
    "v_permlane16_swap_b32 v110, v111\n"
    "v_permlane16_swap_b32 v112, v113\n"
    "v_max_f32 v108, v108, v109\n"
    "v_max_f32 v110, v110, v111\n"
    "v_max_f32 v112, v112, v113\n"
    "v_sub_f32 v144, 0, v108\n"
    "v_sub_f32 v145, 0, v108\n"
    "v_sub_f32 v146, 0, v108\n"
    "v_sub_f32 v147, 0, v108\n"
    "v_sub_f32 v96, v96, v108\n"
    "v_sub_f32 v97, v97, v108\n"
    "v_sub_f32 v98, v98, v108\n"
    "v_sub_f32 v99, v99, v108\n"
    "v_sub_f32 v148, 0, v110\n"
    "v_sub_f32 v149, 0, v110\n"
    "v_sub_f32 v150, 0, v110\n"
    "v_sub_f32 v151, 0, v110\n"
    "v_sub_f32 v100, v100, v110\n"
    "v_sub_f32 v101, v101, v110\n"
    "v_sub_f32 v102, v102, v110\n"
    "v_sub_f32 v103, v103, v110\n"
    "v_sub_f32 v152, 0, v112\n"
    "v_sub_f32 v153, 0, v112\n"
    "v_sub_f32 v154, 0, v112\n"
    "v_sub_f32 v155, 0, v112\n"
    "v_sub_f32 v104, v104, v112\n"
    "v_sub_f32 v105, v105, v112\n"
    "v_sub_f32 v106, v106, v112\n"
    "v_sub_f32 v107, v107, v112\n"
    "v_mfma_f32_16x16x32_bf16 v[108:111], v[156:159], v[0:3], v[144:147]\n"
    "ds_read_b128 v[180:183], v226 offset:896\n"
    "v_mfma_f32_16x16x32_bf16 v[112:115], v[156:159], v[4:7], v[148:151]\n"
    "v_exp_f32 v96, v96\n"
    "v_mfma_f32_16x16x32_bf16 v[116:119], v[156:159], v[8:11], v[152:155]\n"
    "v_exp_f32 v97, v97\n"
    "v_mfma_f32_16x16x32_bf16 v[108:111], v[160:163], v[12:15], v[108:111]\n"
    "ds_read_b128 v[184:187], v226 offset:960\n"
    "v_cvt_pk_bf16_f32 v120, v96, v97\n"
    "v_mfma_f32_16x16x32_bf16 v[112:115], v[160:163], v[16:19], v[112:115]\n"
    "v_exp_f32 v98, v98\n"
    "v_mfma_f32_16x16x32_bf16 v[116:119], v[160:163], v[20:23], v[116:119]\n"
    "s_waitcnt vmcnt(25)\n"
    "s_barrier\n"
    "v_add_u32 v226, s68, v224\n"
    "v_mfma_f32_16x16x32_bf16 v[108:111], v[164:167], v[24:27], v[108:111]\n"
    "ds_read_b128 v[156:159], v226\n"
    "s_add_u32 s68, s68, 16896\n"
    "v_mfma_f32_16x16x32_bf16 v[112:115], v[164:167], v[28:31], v[112:115]\n"
    "s_cmp_eq_u32 s68, 152064\n"
    "s_cselect_b32 s68, 0, s68\n"
    "v_mfma_f32_16x16x32_bf16 v[116:119], v[164:167], v[32:35], v[116:119]\n"
    "v_exp_f32 v99, v99\n"
    "v_mfma_f32_16x16x32_bf16 v[108:111], v[168:171], v[36:39], v[108:111]\n"
    "ds_read_b128 v[160:163], v226 offset:64\n"
    "v_cvt_pk_bf16_f32 v121, v98, v99\n"
    "v_mfma_f32_16x16x32_bf16 v[112:115], v[168:171], v[40:43], v[112:115]\n"
    "v_exp_f32 v100, v100\n"
    "v_mfma_f32_16x16x32_bf16 v[116:119], v[168:171], v[44:47], v[116:119]\n"
    "s_waitcnt lgkmcnt(2)\n"
    "v_mfma_f32_16x16x32_bf16 v[108:111], v[172:175], v[48:51], v[108:111]\n"
    "ds_read_b128 v[164:167], v226 offset:128\n"
    "v_mfma_f32_16x16x32_bf16 v[112:115], v[172:175], v[52:55], v[112:115]\n"
    "v_exp_f32 v101, v101\n"
    "v_mfma_f32_16x16x32_bf16 v[116:119], v[172:175], v[56:59], v[116:119]\n"
    "v_cvt_pk_bf16_f32 v124, v100, v101\n"
    "v_mfma_f32_16x16x32_bf16 v[108:111], v[176:179], v[60:63], v[108:111]\n"
    "ds_read_b128 v[168:171], v226 offset:192\n"
    "v_mfma_f32_16x16x32_bf16 v[112:115], v[176:179], v[64:67], v[112:115]\n"
    "v_exp_f32 v102, v102\n"
    "v_mfma_f32_16x16x32_bf16 v[116:119], v[176:179], v[68:71], v[116:119]\n"
    "v_exp_f32 v103, v103\n"
    "v_mfma_f32_16x16x32_bf16 v[108:111], v[180:183], v[72:75], v[108:111]\n"
    "ds_read_b128 v[172:175], v226 offset:256\n"
    "v_cvt_pk_bf16_f32 v125, v102, v103\n"
    "v_mfma_f32_16x16x32_bf16 v[112:115], v[180:183], v[76:79], v[112:115]\n"
    "v_exp_f32 v104, v104\n"
    "v_mfma_f32_16x16x32_bf16 v[116:119], v[180:183], v[80:83], v[116:119]\n"
    "v_exp_f32 v105, v105\n"
    "v_mfma_f32_16x16x32_bf16 v[108:111], v[184:187], v[84:87], v[108:111]\n"
    "ds_read_b128 v[176:179], v226 offset:320\n"
    "v_cvt_pk_bf16_f32 v128, v104, v105\n"
    "v_mfma_f32_16x16x32_bf16 v[112:115], v[184:187], v[88:91], v[112:115]\n"
    "v_exp_f32 v106, v106\n"
    "v_mfma_f32_16x16x32_bf16 v[116:119], v[184:187], v[92:95], v[116:119]\n"
    "s_waitcnt lgkmcnt(2)\n"
    "v_exp_f32 v107, v107\n"
    "s_nop 0\n"
    "v_cvt_pk_bf16_f32 v129, v106, v107\n"
    "s_nop 3\n"
    "v_max3_f32 v96, v108, v109, v110\n"
    "v_max3_f32 v97, v112, v113, v114\n"
    "v_max3_f32 v96, v96, v111, v116\n"
    "v_max3_f32 v97, v97, v115, v117\n"
    "v_max3_f32 v96, v96, v118, v119\n"
    "v_max_f32 v96, v96, v97\n"
    "v_cmp_lt_f32 vcc, s88, v96\n"
    "s_nop 0\n"
    "s_cbranch_vccnz LR_p\n"
    "LB_p:\n"
    "v_exp_f32 v108, v108\n"
    "v_exp_f32 v109, v109\n"
    "v_exp_f32 v110, v110\n"
    "v_exp_f32 v111, v111\n"
    "v_cvt_pk_bf16_f32 v122, v108, v109\n"
    "v_cvt_pk_bf16_f32 v123, v110, v111\n"
    "s_barrier\n"
    "s_add_u32 m0, s69, s70\n"
    "s_nop 0\n"
    "buffer_load_dwordx4 v228, s[24:27], s71 offen nt lds\n"
    "buffer_load_dwordx4 v229, s[24:27], s71 offen offset:1056 nt lds\n"
    "buffer_load_dwordx4 v230, s[24:27], s71 offen offset:2112 nt lds\n"
    "buffer_load_dwordx4 v231, s[24:27], s71 offen offset:3168 nt lds\n"
    "s_add_u32 s69, s69, 16896\n"
    "s_cmp_eq_u32 s69, 152064\n"
    "s_cselect_b32 s69, 0, s69\n"
    "s_add_u32 s71, s71, 16384\n"
    "v_mfma_f32_16x16x32_bf16 v[96:99], v[156:159], v[0:3], v[144:147]\n"
    "ds_read_b128 v[180:183], v226 offset:384\n"
    "v_add_u32 v227, s68, v225\n"
    "v_mfma_f32_16x16x32_bf16 v[100:103], v[156:159], v[4:7], v[148:151]\n"
    "s_add_u32 s68, s68, 16896\n"
    "v_mfma_f32_16x16x32_bf16 v[104:107], v[156:159], v[8:11], v[152:155]\n"
    "s_cmp_eq_u32 s68, 152064\n"
    "s_cselect_b32 s68, 0, s68\n"
    "v_mfma_f32_16x16x32_bf16 v[96:99], v[160:163], v[12:15], v[96:99]\n"
    "ds_read_b128 v[184:187], v226 offset:448\n"
    "v_mfma_f32_16x16x32_bf16 v[100:103], v[160:163], v[16:19], v[100:103]\n"
    "v_exp_f32 v112, v112\n"
    "v_mfma_f32_16x16x32_bf16 v[104:107], v[160:163], v[20:23], v[104:107]\n"
    "v_exp_f32 v113, v113\n"
    "v_mfma_f32_16x16x32_bf16 v[96:99], v[164:167], v[24:27], v[96:99]\n"
    "v_cvt_pk_bf16_f32 v126, v112, v113\n"
    "v_mfma_f32_16x16x32_bf16 v[100:103], v[164:167], v[28:31], v[100:103]\n"
    "v_exp_f32 v114, v114\n"
    "v_mfma_f32_16x16x32_bf16 v[104:107], v[164:167], v[32:35], v[104:107]\n"
    "v_exp_f32 v115, v115\n"
    "v_mfma_f32_16x16x32_bf16 v[96:99], v[168:171], v[36:39], v[96:99]\n"
    "ds_read_b64_tr_b16 v[156:157], v227\n"
    "v_cvt_pk_bf16_f32 v127, v114, v115\n"
    "v_mfma_f32_16x16x32_bf16 v[100:103], v[168:171], v[40:43], v[100:103]\n"
    "ds_read_b64_tr_b16 v[158:159], v227 offset:512\n"
    "v_mfma_f32_16x16x32_bf16 v[104:107], v[168:171], v[44:47], v[104:107]\n"
    "s_waitcnt lgkmcnt(2)\n"
    "v_mfma_f32_16x16x32_bf16 v[96:99], v[172:175], v[48:51], v[96:99]\n"
    "ds_read_b64_tr_b16 v[160:161], v227 offset:32\n"
    "v_mfma_f32_16x16x32_bf16 v[100:103], v[172:175], v[52:55], v[100:103]\n"
    "ds_read_b64_tr_b16 v[162:163], v227 offset:544\n"
    "v_mfma_f32_16x16x32_bf16 v[104:107], v[172:175], v[56:59], v[104:107]\n"
    "v_exp_f32 v116, v116\n"
    "v_mfma_f32_16x16x32_bf16 v[96:99], v[176:179], v[60:63], v[96:99]\n"
    "ds_read_b64_tr_b16 v[164:165], v227 offset:64\n"
    "v_mfma_f32_16x16x32_bf16 v[100:103], v[176:179], v[64:67], v[100:103]\n"
    "ds_read_b64_tr_b16 v[166:167], v227 offset:576\n"
    "v_mfma_f32_16x16x32_bf16 v[104:107], v[176:179], v[68:71], v[104:107]\n"
    "v_exp_f32 v117, v117\n"
    "v_mfma_f32_16x16x32_bf16 v[96:99], v[180:183], v[72:75], v[96:99]\n"
    "ds_read_b64_tr_b16 v[168:169], v227 offset:96\n"
    "v_cvt_pk_bf16_f32 v130, v116, v117\n"
    "v_mfma_f32_16x16x32_bf16 v[100:103], v[180:183], v[76:79], v[100:103]\n"
    "ds_read_b64_tr_b16 v[170:171], v227 offset:608\n"
    "v_mfma_f32_16x16x32_bf16 v[104:107], v[180:183], v[80:83], v[104:107]\n"
    "v_exp_f32 v118, v118\n"
    "v_mfma_f32_16x16x32_bf16 v[96:99], v[184:187], v[84:87], v[96:99]\n"
    "ds_read_b64_tr_b16 v[172:173], v227 offset:128\n"
    "v_exp_f32 v119, v119\n"
    "v_mfma_f32_16x16x32_bf16 v[100:103], v[184:187], v[88:91], v[100:103]\n"
    "ds_read_b64_tr_b16 v[174:175], v227 offset:640\n"
    "v_cvt_pk_bf16_f32 v131, v118, v119\n"
    "v_mfma_f32_16x16x32_bf16 v[104:107], v[184:187], v[92:95], v[104:107]\n"
    "v_mfma_f32_16x16x32_bf16 v[240:243], v[188:191], v[120:123], 0\n"
    "ds_read_b64_tr_b16 v[176:177], v227 offset:160\n"
    "v_mfma_f32_16x16x32_bf16 v[244:247], v[188:191], v[124:127], 0\n"
    "ds_read_b64_tr_b16 v[178:179], v227 offset:672\n"
    "v_mfma_f32_16x16x32_bf16 v[248:251], v[188:191], v[128:131], 0\n"
    "s_waitcnt lgkmcnt(4)\n"
    "v_mfma_f32_16x16x32_bf16 a[0:3], v[156:159], v[120:123], 0\n"
    "ds_read_b64_tr_b16 v[180:181], v227 offset:192\n"
    "v_max3_f32 v108, v96, v97, v98\n"
    "v_mfma_f32_16x16x32_bf16 a[8:11], v[156:159], v[124:127], 0\n"
    "ds_read_b64_tr_b16 v[182:183], v227 offset:704\n"
    "v_max3_f32 v109, v100, v101, v102\n"
    "v_mfma_f32_16x16x32_bf16 a[16:19], v[156:159], v[128:131], 0\n"
    "v_max3_f32 v108, v108, v99, v104\n"
    "v_max3_f32 v109, v109, v103, v105\n"
    "v_mfma_f32_16x16x32_bf16 a[4:7], v[160:163], v[120:123], 0\n"
    "ds_read_b64_tr_b16 v[184:185], v227 offset:224\n"
    "v_max3_f32 v108, v108, v106, v107\n"
    "v_mfma_f32_16x16x32_bf16 a[12:15], v[160:163], v[124:127], 0\n"
    "ds_read_b64_tr_b16 v[186:187], v227 offset:736\n"
    "v_max_f32 v108, v108, v109\n"
    "v_mfma_f32_16x16x32_bf16 a[20:23], v[160:163], v[128:131], 0\n"
    "v_cmp_lt_f32 vcc, s88, v108\n"
    "s_nop 0\n"
    "s_cbranch_vccnz LR_s0a\n"
    "LB_s0a:\n"
    "v_mfma_f32_16x16x32_bf16 a[24:27], v[164:167], v[120:123], 0\n"
    "ds_read_b128 v[156:159], v226 offset:512\n"
    "v_mfma_f32_16x16x32_bf16 a[32:35], v[164:167], v[124:127], 0\n"
    "v_exp_f32 v96, v96\n"
    "v_mfma_f32_16x16x32_bf16 a[40:43], v[164:167], v[128:131], 0\n"
    "v_exp_f32 v97, v97\n"
    "v_mfma_f32_16x16x32_bf16 a[28:31], v[168:171], v[120:123], 0\n"
    "ds_read_b128 v[160:163], v226 offset:576\n"
    "v_cvt_pk_bf16_f32 v132, v96, v97\n"
    "v_mfma_f32_16x16x32_bf16 a[36:39], v[168:171], v[124:127], 0\n"
    "v_exp_f32 v98, v98\n"
    "v_mfma_f32_16x16x32_bf16 a[44:47], v[168:171], v[128:131], 0\n"
    "s_waitcnt lgkmcnt(2)\n"
    "v_mfma_f32_16x16x32_bf16 a[48:51], v[172:175], v[120:123], 0\n"
    "ds_read_b128 v[164:167], v226 offset:640\n"
    "v_mfma_f32_16x16x32_bf16 a[56:59], v[172:175], v[124:127], 0\n"
    "v_exp_f32 v99, v99\n"
    "v_mfma_f32_16x16x32_bf16 a[64:67], v[172:175], v[128:131], 0\n"
    "v_cvt_pk_bf16_f32 v133, v98, v99\n"
    "v_mfma_f32_16x16x32_bf16 a[52:55], v[176:179], v[120:123], 0\n"
    "ds_read_b128 v[168:171], v226 offset:704\n"
    "v_mfma_f32_16x16x32_bf16 a[60:63], v[176:179], v[124:127], 0\n"
    "v_exp_f32 v100, v100\n"
    "v_mfma_f32_16x16x32_bf16 a[68:71], v[176:179], v[128:131], 0\n"
    "v_exp_f32 v101, v101\n"
    "v_mfma_f32_16x16x32_bf16 a[72:75], v[180:183], v[120:123], 0\n"
    "ds_read_b128 v[172:175], v226 offset:768\n"
    "v_cvt_pk_bf16_f32 v136, v100, v101\n"
    "v_mfma_f32_16x16x32_bf16 a[80:83], v[180:183], v[124:127], 0\n"
    "v_exp_f32 v102, v102\n"
    "v_mfma_f32_16x16x32_bf16 a[88:91], v[180:183], v[128:131], 0\n"
    "v_exp_f32 v103, v103\n"
    "v_mfma_f32_16x16x32_bf16 a[76:79], v[184:187], v[120:123], 0\n"
    "ds_read_b128 v[176:179], v226 offset:832\n"
    "v_cvt_pk_bf16_f32 v137, v102, v103\n"
    "v_mfma_f32_16x16x32_bf16 a[84:87], v[184:187], v[124:127], 0\n"
    "v_exp_f32 v104, v104\n"
    "v_mfma_f32_16x16x32_bf16 a[92:95], v[184:187], v[128:131], 0\n"
    "s_waitcnt lgkmcnt(2)\n"
    "v_mfma_f32_16x16x32_bf16 v[108:111], v[156:159], v[0:3], v[144:147]\n"
    "ds_read_b128 v[180:183], v226 offset:896\n"
    "v_mfma_f32_16x16x32_bf16 v[112:115], v[156:159], v[4:7], v[148:151]\n"
    "v_exp_f32 v105, v105\n"
    "v_mfma_f32_16x16x32_bf16 v[116:119], v[156:159], v[8:11], v[152:155]\n"
    "v_cvt_pk_bf16_f32 v140, v104, v105\n"
    "v_mfma_f32_16x16x32_bf16 v[108:111], v[160:163], v[12:15], v[108:111]\n"
    "ds_read_b128 v[184:187], v226 offset:960\n"
    "v_mfma_f32_16x16x32_bf16 v[112:115], v[160:163], v[16:19], v[112:115]\n"
    "v_exp_f32 v106, v106\n"
    "v_mfma_f32_16x16x32_bf16 v[116:119], v[160:163], v[20:23], v[116:119]\n"
    "v_exp_f32 v107, v107\n"
    "v_mfma_f32_16x16x32_bf16 v[108:111], v[164:167], v[24:27], v[108:111]\n"
    "ds_read_b64_tr_b16 v[156:157], v227 offset:256\n"
    "v_cvt_pk_bf16_f32 v141, v106, v107\n"
    "v_mfma_f32_16x16x32_bf16 v[112:115], v[164:167], v[28:31], v[112:115]\n"
    "ds_read_b64_tr_b16 v[158:159], v227 offset:768\n"
    "v_mfma_f32_16x16x32_bf16 v[116:119], v[164:167], v[32:35], v[116:119]\n"
    "v_mfma_f32_16x16x32_bf16 v[108:111], v[168:171], v[36:39], v[108:111]\n"
    "ds_read_b64_tr_b16 v[160:161], v227 offset:288\n"
    "v_mfma_f32_16x16x32_bf16 v[112:115], v[168:171], v[40:43], v[112:115]\n"
    "ds_read_b64_tr_b16 v[162:163], v227 offset:800\n"
    "v_mfma_f32_16x16x32_bf16 v[116:119], v[168:171], v[44:47], v[116:119]\n"
    "s_waitcnt lgkmcnt(4)\n"
    "s_waitcnt vmcnt(21)\n"
    "s_barrier\n"
    "v_add_u32 v226, s68, v224\n"
    "v_mfma_f32_16x16x32_bf16 v[108:111], v[172:175], v[48:51], v[108:111]\n"
    "ds_read_b64_tr_b16 v[164:165], v227 offset:320\n"
    "s_add_u32 m0, s69, s70\n"
    "v_mfma_f32_16x16x32_bf16 v[112:115], v[172:175], v[52:55], v[112:115]\n"
    "ds_read_b64_tr_b16 v[166:167], v227 offset:832\n"
    "s_add_u32 s68, s68, 16896\n"
    "v_mfma_f32_16x16x32_bf16 v[116:119], v[172:175], v[56:59], v[116:119]\n"
    "buffer_load_dwordx4 v228, s[28:31], s72 offen nt lds\n"
    "v_mfma_f32_16x16x32_bf16 v[108:111], v[176:179], v[60:63], v[108:111]\n"
    "ds_read_b64_tr_b16 v[168:169], v227 offset:352\n"
    "v_mfma_f32_16x16x32_bf16 v[112:115], v[176:179], v[64:67], v[112:115]\n"
    "ds_read_b64_tr_b16 v[170:171], v227 offset:864\n"
    "v_mfma_f32_16x16x32_bf16 v[116:119], v[176:179], v[68:71], v[116:119]\n"
    "buffer_load_dwordx4 v229, s[28:31], s72 offen offset:1056 nt lds\n"
    "v_mfma_f32_16x16x32_bf16 v[108:111], v[180:183], v[72:75], v[108:111]\n"
    "ds_read_b64_tr_b16 v[172:173], v227 offset:384\n"
    "v_mfma_f32_16x16x32_bf16 v[112:115], v[180:183], v[76:79], v[112:115]\n"
    "ds_read_b64_tr_b16 v[174:175], v227 offset:896\n"
    "v_mfma_f32_16x16x32_bf16 v[116:119], v[180:183], v[80:83], v[116:119]\n"
    "buffer_load_dwordx4 v230, s[28:31], s72 offen offset:2112 nt lds\n"
    "v_mfma_f32_16x16x32_bf16 v[108:111], v[184:187], v[84:87], v[108:111]\n"
    "ds_read_b64_tr_b16 v[176:177], v227 offset:416\n"
    "v_mfma_f32_16x16x32_bf16 v[112:115], v[184:187], v[88:91], v[112:115]\n"
    "ds_read_b64_tr_b16 v[178:179], v227 offset:928\n"
    "v_mfma_f32_16x16x32_bf16 v[116:119], v[184:187], v[92:95], v[116:119]\n"
    "s_waitcnt lgkmcnt(4)\n"
    "buffer_load_dwordx4 v231, s[28:31], s72 offen offset:3168 nt lds\n"
    "v_mfma_f32_16x16x32_bf16 a[96:99], v[156:159], v[120:123], 0\n"
    "ds_read_b64_tr_b16 v[180:181], v227 offset:448\n"
    "s_add_u32 s69, s69, 16896\n"
    "v_mfma_f32_16x16x32_bf16 a[104:107], v[156:159], v[124:127], 0\n"
    "ds_read_b64_tr_b16 v[182:183], v227 offset:960\n"
    "v_mfma_f32_16x16x32_bf16 a[112:115], v[156:159], v[128:131], 0\n"
    "s_cmp_eq_u32 s69, 152064\n"
    "s_cselect_b32 s69, 0, s69\n"
    "v_mfma_f32_16x16x32_bf16 a[100:103], v[160:163], v[120:123], 0\n"
    "ds_read_b64_tr_b16 v[184:185], v227 offset:480\n"
    "s_add_u32 s72, s72, 16384\n"
    "v_mfma_f32_16x16x32_bf16 a[108:111], v[160:163], v[124:127], 0\n"
    "ds_read_b64_tr_b16 v[186:187], v227 offset:992\n"
    "v_max3_f32 v96, v108, v109, v110\n"
    "v_mfma_f32_16x16x32_bf16 a[116:119], v[160:163], v[128:131], 0\n"
    "s_cmp_eq_u32 s68, 152064\n"
    "s_cselect_b32 s68, 0, s68\n"
    "v_mfma_f32_16x16x32_bf16 a[120:123], v[164:167], v[120:123], 0\n"
    "ds_read_b128 v[156:159], v226\n"
    "v_max3_f32 v97, v112, v113, v114\n"
    "v_mfma_f32_16x16x32_bf16 a[128:131], v[164:167], v[124:127], 0\n"
    "v_max3_f32 v96, v96, v111, v116\n"
    "v_max3_f32 v97, v97, v115, v117\n"
    "v_mfma_f32_16x16x32_bf16 a[136:139], v[164:167], v[128:131], 0\n"
    "v_max3_f32 v96, v96, v118, v119\n"
    "v_max_f32 v96, v96, v97\n"
    "v_mfma_f32_16x16x32_bf16 a[124:127], v[168:171], v[120:123], 0\n"
    "ds_read_b128 v[160:163], v226 offset:64\n"
    "v_cmp_lt_f32 vcc, s88, v96\n"
    "s_nop 0\n"
    "s_cbranch_vccnz LR_s0b\n"
    "LB_s0b:\n"
    "v_mfma_f32_16x16x32_bf16 a[132:135], v[168:171], v[124:127], 0\n"
    "v_exp_f32 v108, v108\n"
    "v_mfma_f32_16x16x32_bf16 a[140:143], v[168:171], v[128:131], 0\n"
    "s_waitcnt lgkmcnt(2)\n"
    "v_mfma_f32_16x16x32_bf16 a[144:147], v[172:175], v[120:123], 0\n"
    "ds_read_b128 v[164:167], v226 offset:128\n"
    "v_mfma_f32_16x16x32_bf16 a[152:155], v[172:175], v[124:127], 0\n"
    "v_exp_f32 v109, v109\n"
    "v_mfma_f32_16x16x32_bf16 a[160:163], v[172:175], v[128:131], 0\n"
    "v_cvt_pk_bf16_f32 v134, v108, v109\n"
    "v_mfma_f32_16x16x32_bf16 a[148:151], v[176:179], v[120:123], 0\n"
    "ds_read_b128 v[168:171], v226 offset:192\n"
    "v_mfma_f32_16x16x32_bf16 a[156:159], v[176:179], v[124:127], 0\n"
    "v_exp_f32 v110, v110\n"
    "v_mfma_f32_16x16x32_bf16 a[164:167], v[176:179], v[128:131], 0\n"
    "v_exp_f32 v111, v111\n"
    "v_mfma_f32_16x16x32_bf16 a[168:171], v[180:183], v[120:123], 0\n"
    "ds_read_b128 v[172:175], v226 offset:256\n"
    "v_cvt_pk_bf16_f32 v135, v110, v111\n"
    "v_mfma_f32_16x16x32_bf16 a[176:179], v[180:183], v[124:127], 0\n"
    "v_mfma_f32_16x16x32_bf16 a[184:187], v[180:183], v[128:131], 0\n"
    "v_mfma_f32_16x16x32_bf16 a[172:175], v[184:187], v[120:123], 0\n"
    "ds_read_b128 v[176:179], v226 offset:320\n"
    "v_mfma_f32_16x16x32_bf16 a[180:183], v[184:187], v[124:127], 0\n"
    "v_mfma_f32_16x16x32_bf16 a[188:191], v[184:187], v[128:131], 0\n"
    "s_waitcnt lgkmcnt(2)\n"
    "s_mov_b32 s75, 5\n"
    "LAL:\n"
    "v_mfma_f32_16x16x32_bf16 v[96:99], v[156:159], v[0:3], v[144:147]\n"
    "ds_read_b128 v[180:183], v226 offset:384\n"
    "v_add_u32 v227, s68, v225\n"
    "v_mfma_f32_16x16x32_bf16 v[100:103], v[156:159], v[4:7], v[148:151]\n"
    "s_add_u32 s68, s68, 16896\n"
    "v_mfma_f32_16x16x32_bf16 v[104:107], v[156:159], v[8:11], v[152:155]\n"
    "s_cmp_eq_u32 s68, 152064\n"
    "s_cselect_b32 s68, 0, s68\n"
    "v_mfma_f32_16x16x32_bf16 v[96:99], v[160:163], v[12:15], v[96:99]\n"
    "ds_read_b128 v[184:187], v226 offset:448\n"
    "v_mfma_f32_16x16x32_bf16 v[100:103], v[160:163], v[16:19], v[100:103]\n"
    "v_exp_f32 v112, v112\n"
    "v_mfma_f32_16x16x32_bf16 v[104:107], v[160:163], v[20:23], v[104:107]\n"
    "v_exp_f32 v113, v113\n"
    "v_mfma_f32_16x16x32_bf16 v[96:99], v[164:167], v[24:27], v[96:99]\n"
    "v_cvt_pk_bf16_f32 v138, v112, v113\n"
    "v_mfma_f32_16x16x32_bf16 v[100:103], v[164:167], v[28:31], v[100:103]\n"
    "v_exp_f32 v114, v114\n"
    "v_mfma_f32_16x16x32_bf16 v[104:107], v[164:167], v[32:35], v[104:107]\n"
    "v_exp_f32 v115, v115\n"
    "v_mfma_f32_16x16x32_bf16 v[96:99], v[168:171], v[36:39], v[96:99]\n"
    "ds_read_b64_tr_b16 v[156:157], v227\n"
    "v_cvt_pk_bf16_f32 v139, v114, v115\n"
    "v_mfma_f32_16x16x32_bf16 v[100:103], v[168:171], v[40:43], v[100:103]\n"
    "ds_read_b64_tr_b16 v[158:159], v227 offset:512\n"
    "v_mfma_f32_16x16x32_bf16 v[104:107], v[168:171], v[44:47], v[104:107]\n"
    "s_waitcnt lgkmcnt(2)\n"
    "v_mfma_f32_16x16x32_bf16 v[96:99], v[172:175], v[48:51], v[96:99]\n"
    "ds_read_b64_tr_b16 v[160:161], v227 offset:32\n"
    "v_mfma_f32_16x16x32_bf16 v[100:103], v[172:175], v[52:55], v[100:103]\n"
    "ds_read_b64_tr_b16 v[162:163], v227 offset:544\n"
    "v_mfma_f32_16x16x32_bf16 v[104:107], v[172:175], v[56:59], v[104:107]\n"
    "v_exp_f32 v116, v116\n"
    "v_mfma_f32_16x16x32_bf16 v[96:99], v[176:179], v[60:63], v[96:99]\n"
    "ds_read_b64_tr_b16 v[164:165], v227 offset:64\n"
    "v_mfma_f32_16x16x32_bf16 v[100:103], v[176:179], v[64:67], v[100:103]\n"
    "ds_read_b64_tr_b16 v[166:167], v227 offset:576\n"
    "v_mfma_f32_16x16x32_bf16 v[104:107], v[176:179], v[68:71], v[104:107]\n"
    "v_exp_f32 v117, v117\n"
    "v_mfma_f32_16x16x32_bf16 v[96:99], v[180:183], v[72:75], v[96:99]\n"
    "ds_read_b64_tr_b16 v[168:169], v227 offset:96\n"
    "v_cvt_pk_bf16_f32 v142, v116, v117\n"
    "v_mfma_f32_16x16x32_bf16 v[100:103], v[180:183], v[76:79], v[100:103]\n"
    "ds_read_b64_tr_b16 v[170:171], v227 offset:608\n"
    "v_mfma_f32_16x16x32_bf16 v[104:107], v[180:183], v[80:83], v[104:107]\n"
    "v_exp_f32 v118, v118\n"
    "v_mfma_f32_16x16x32_bf16 v[96:99], v[184:187], v[84:87], v[96:99]\n"
    "ds_read_b64_tr_b16 v[172:173], v227 offset:128\n"
    "v_exp_f32 v119, v119\n"
    "v_mfma_f32_16x16x32_bf16 v[100:103], v[184:187], v[88:91], v[100:103]\n"
    "ds_read_b64_tr_b16 v[174:175], v227 offset:640\n"
    "v_cvt_pk_bf16_f32 v143, v118, v119\n"
    "v_mfma_f32_16x16x32_bf16 v[104:107], v[184:187], v[92:95], v[104:107]\n"
    "v_mfma_f32_16x16x32_bf16 v[240:243], v[188:191], v[132:135], v[240:243]\n"
    "ds_read_b64_tr_b16 v[176:177], v227 offset:160\n"
    "v_mfma_f32_16x16x32_bf16 v[244:247], v[188:191], v[136:139], v[244:247]\n"
    "ds_read_b64_tr_b16 v[178:179], v227 offset:672\n"
    "v_mfma_f32_16x16x32_bf16 v[248:251], v[188:191], v[140:143], v[248:251]\n"
    "s_waitcnt lgkmcnt(4)\n"
    "v_mfma_f32_16x16x32_bf16 a[0:3], v[156:159], v[132:135], a[0:3]\n"
    "ds_read_b64_tr_b16 v[180:181], v227 offset:192\n"
    "v_max3_f32 v108, v96, v97, v98\n"
    "v_mfma_f32_16x16x32_bf16 a[8:11], v[156:159], v[136:139], a[8:11]\n"
    "ds_read_b64_tr_b16 v[182:183], v227 offset:704\n"
    "v_max3_f32 v109, v100, v101, v102\n"
    "v_mfma_f32_16x16x32_bf16 a[16:19], v[156:159], v[140:143], a[16:19]\n"
    "v_max3_f32 v108, v108, v99, v104\n"
    "v_max3_f32 v109, v109, v103, v105\n"
    "v_mfma_f32_16x16x32_bf16 a[4:7], v[160:163], v[132:135], a[4:7]\n"
    "ds_read_b64_tr_b16 v[184:185], v227 offset:224\n"
    "v_max3_f32 v108, v108, v106, v107\n"
    "v_mfma_f32_16x16x32_bf16 a[12:15], v[160:163], v[136:139], a[12:15]\n"
    "ds_read_b64_tr_b16 v[186:187], v227 offset:736\n"
    "v_max_f32 v108, v108, v109\n"
    "v_mfma_f32_16x16x32_bf16 a[20:23], v[160:163], v[140:143], a[20:23]\n"
    "v_cmp_lt_f32 vcc, s88, v108\n"
    "s_nop 0\n"
    "s_cbranch_vccnz LR_l0a\n"
    "LB_l0a:\n"
    "v_mfma_f32_16x16x32_bf16 a[24:27], v[164:167], v[132:135], a[24:27]\n"
    "ds_read_b128 v[156:159], v226 offset:512\n"
    "v_mfma_f32_16x16x32_bf16 a[32:35], v[164:167], v[136:139], a[32:35]\n"
    "v_exp_f32 v96, v96\n"
    "v_mfma_f32_16x16x32_bf16 a[40:43], v[164:167], v[140:143], a[40:43]\n"
    "v_exp_f32 v97, v97\n"
    "v_mfma_f32_16x16x32_bf16 a[28:31], v[168:171], v[132:135], a[28:31]\n"
    "ds_read_b128 v[160:163], v226 offset:576\n"
    "v_cvt_pk_bf16_f32 v120, v96, v97\n"
    "v_mfma_f32_16x16x32_bf16 a[36:39], v[168:171], v[136:139], a[36:39]\n"
    "v_exp_f32 v98, v98\n"
    "v_mfma_f32_16x16x32_bf16 a[44:47], v[168:171], v[140:143], a[44:47]\n"
    "s_waitcnt lgkmcnt(2)\n"
    "v_mfma_f32_16x16x32_bf16 a[48:51], v[172:175], v[132:135], a[48:51]\n"
    "ds_read_b128 v[164:167], v226 offset:640\n"
    "v_mfma_f32_16x16x32_bf16 a[56:59], v[172:175], v[136:139], a[56:59]\n"
    "v_exp_f32 v99, v99\n"
    "v_mfma_f32_16x16x32_bf16 a[64:67], v[172:175], v[140:143], a[64:67]\n"
    "v_cvt_pk_bf16_f32 v121, v98, v99\n"
    "v_mfma_f32_16x16x32_bf16 a[52:55], v[176:179], v[132:135], a[52:55]\n"
    "ds_read_b128 v[168:171], v226 offset:704\n"
    "v_mfma_f32_16x16x32_bf16 a[60:63], v[176:179], v[136:139], a[60:63]\n"
    "v_exp_f32 v100, v100\n"
    "v_mfma_f32_16x16x32_bf16 a[68:71], v[176:179], v[140:143], a[68:71]\n"
    "v_exp_f32 v101, v101\n"
    "v_mfma_f32_16x16x32_bf16 a[72:75], v[180:183], v[132:135], a[72:75]\n"
    "ds_read_b128 v[172:175], v226 offset:768\n"
    "v_cvt_pk_bf16_f32 v124, v100, v101\n"
    "v_mfma_f32_16x16x32_bf16 a[80:83], v[180:183], v[136:139], a[80:83]\n"
    "v_exp_f32 v102, v102\n"
    "v_mfma_f32_16x16x32_bf16 a[88:91], v[180:183], v[140:143], a[88:91]\n"
    "v_exp_f32 v103, v103\n"
    "v_mfma_f32_16x16x32_bf16 a[76:79], v[184:187], v[132:135], a[76:79]\n"
    "ds_read_b128 v[176:179], v226 offset:832\n"
    "v_cvt_pk_bf16_f32 v125, v102, v103\n"
    "v_mfma_f32_16x16x32_bf16 a[84:87], v[184:187], v[136:139], a[84:87]\n"
    "v_exp_f32 v104, v104\n"
    "v_mfma_f32_16x16x32_bf16 a[92:95], v[184:187], v[140:143], a[92:95]\n"
    "s_waitcnt lgkmcnt(2)\n"
    "v_mfma_f32_16x16x32_bf16 v[108:111], v[156:159], v[0:3], v[144:147]\n"
    "ds_read_b128 v[180:183], v226 offset:896\n"
    "v_mfma_f32_16x16x32_bf16 v[112:115], v[156:159], v[4:7], v[148:151]\n"
    "v_exp_f32 v105, v105\n"
    "v_mfma_f32_16x16x32_bf16 v[116:119], v[156:159], v[8:11], v[152:155]\n"
    "v_cvt_pk_bf16_f32 v128, v104, v105\n"
    "v_mfma_f32_16x16x32_bf16 v[108:111], v[160:163], v[12:15], v[108:111]\n"
    "ds_read_b128 v[184:187], v226 offset:960\n"
    "v_mfma_f32_16x16x32_bf16 v[112:115], v[160:163], v[16:19], v[112:115]\n"
    "v_exp_f32 v106, v106\n"
    "v_mfma_f32_16x16x32_bf16 v[116:119], v[160:163], v[20:23], v[116:119]\n"
    "v_exp_f32 v107, v107\n"
    "v_mfma_f32_16x16x32_bf16 v[108:111], v[164:167], v[24:27], v[108:111]\n"
    "ds_read_b64_tr_b16 v[156:157], v227 offset:256\n"
    "v_cvt_pk_bf16_f32 v129, v106, v107\n"
    "v_mfma_f32_16x16x32_bf16 v[112:115], v[164:167], v[28:31], v[112:115]\n"
    "ds_read_b64_tr_b16 v[158:159], v227 offset:768\n"
    "v_mfma_f32_16x16x32_bf16 v[116:119], v[164:167], v[32:35], v[116:119]\n"
    "v_mfma_f32_16x16x32_bf16 v[108:111], v[168:171], v[36:39], v[108:111]\n"
    "ds_read_b64_tr_b16 v[160:161], v227 offset:288\n"
    "v_mfma_f32_16x16x32_bf16 v[112:115], v[168:171], v[40:43], v[112:115]\n"
    "ds_read_b64_tr_b16 v[162:163], v227 offset:800\n"
    "v_mfma_f32_16x16x32_bf16 v[116:119], v[168:171], v[44:47], v[116:119]\n"
    "s_waitcnt lgkmcnt(4)\n"
    "s_waitcnt vmcnt(16)\n"
    "s_barrier\n"
    "v_add_u32 v226, s68, v224\n"
    "v_mfma_f32_16x16x32_bf16 v[108:111], v[172:175], v[48:51], v[108:111]\n"
    "ds_read_b64_tr_b16 v[164:165], v227 offset:320\n"
    "s_add_u32 m0, s69, s70\n"
    "v_mfma_f32_16x16x32_bf16 v[112:115], v[172:175], v[52:55], v[112:115]\n"
    "ds_read_b64_tr_b16 v[166:167], v227 offset:832\n"
    "s_add_u32 s68, s68, 16896\n"
    "v_mfma_f32_16x16x32_bf16 v[116:119], v[172:175], v[56:59], v[116:119]\n"
    "buffer_load_dwordx4 v228, s[24:27], s71 offen nt lds\n"
    "s_cmp_eq_u32 s68, 152064\n"
    "s_cselect_b32 s68, 0, s68\n"
    "v_mfma_f32_16x16x32_bf16 v[108:111], v[176:179], v[60:63], v[108:111]\n"
    "ds_read_b64_tr_b16 v[168:169], v227 offset:352\n"
    "v_mfma_f32_16x16x32_bf16 v[112:115], v[176:179], v[64:67], v[112:115]\n"
    "ds_read_b64_tr_b16 v[170:171], v227 offset:864\n"
    "v_mfma_f32_16x16x32_bf16 v[116:119], v[176:179], v[68:71], v[116:119]\n"
    "buffer_load_dwordx4 v229, s[24:27], s71 offen offset:1056 nt lds\n"
    "v_mfma_f32_16x16x32_bf16 v[108:111], v[180:183], v[72:75], v[108:111]\n"
    "ds_read_b64_tr_b16 v[172:173], v227 offset:384\n"
    "v_mfma_f32_16x16x32_bf16 v[112:115], v[180:183], v[76:79], v[112:115]\n"
    "ds_read_b64_tr_b16 v[174:175], v227 offset:896\n"
    "v_mfma_f32_16x16x32_bf16 v[116:119], v[180:183], v[80:83], v[116:119]\n"
    "buffer_load_dwordx4 v230, s[24:27], s71 offen offset:2112 nt lds\n"
    "v_mfma_f32_16x16x32_bf16 v[108:111], v[184:187], v[84:87], v[108:111]\n"
    "ds_read_b64_tr_b16 v[176:177], v227 offset:416\n"
    "v_mfma_f32_16x16x32_bf16 v[112:115], v[184:187], v[88:91], v[112:115]\n"
    "ds_read_b64_tr_b16 v[178:179], v227 offset:928\n"
    "v_mfma_f32_16x16x32_bf16 v[116:119], v[184:187], v[92:95], v[116:119]\n"
    "s_waitcnt lgkmcnt(4)\n"
    "buffer_load_dwordx4 v231, s[24:27], s71 offen offset:3168 nt lds\n"
    "v_mfma_f32_16x16x32_bf16 a[96:99], v[156:159], v[132:135], a[96:99]\n"
    "ds_read_b64_tr_b16 v[180:181], v227 offset:448\n"
    "s_add_u32 s69, s69, 16896\n"
    "v_mfma_f32_16x16x32_bf16 a[104:107], v[156:159], v[136:139], a[104:107]\n"
    "ds_read_b64_tr_b16 v[182:183], v227 offset:960\n"
    "v_mfma_f32_16x16x32_bf16 a[112:115], v[156:159], v[140:143], a[112:115]\n"
    "s_cmp_eq_u32 s69, 152064\n"
    "s_cselect_b32 s69, 0, s69\n"
    "v_mfma_f32_16x16x32_bf16 a[100:103], v[160:163], v[132:135], a[100:103]\n"
    "ds_read_b64_tr_b16 v[184:185], v227 offset:480\n"
    "s_add_u32 s71, s71, 16384\n"
    "v_mfma_f32_16x16x32_bf16 a[108:111], v[160:163], v[136:139], a[108:111]\n"
    "ds_read_b64_tr_b16 v[186:187], v227 offset:992\n"
    "s_add_u32 m0, s69, s70\n"
    "v_mfma_f32_16x16x32_bf16 a[116:119], v[160:163], v[140:143], a[116:119]\n"
    "buffer_load_dwordx4 v228, s[28:31], s72 offen nt lds\n"
    "v_max3_f32 v96, v108, v109, v110\n"
    "v_mfma_f32_16x16x32_bf16 a[120:123], v[164:167], v[132:135], a[120:123]\n"
    "ds_read_b128 v[156:159], v226\n"
    "v_max3_f32 v97, v112, v113, v114\n"
    "v_mfma_f32_16x16x32_bf16 a[128:131], v[164:167], v[136:139], a[128:131]\n"
    "v_max3_f32 v96, v96, v111, v116\n"
    "v_max3_f32 v97, v97, v115, v117\n"
    "v_mfma_f32_16x16x32_bf16 a[136:139], v[164:167], v[140:143], a[136:139]\n"
    "buffer_load_dwordx4 v229, s[28:31], s72 offen offset:1056 nt lds\n"
    "v_max3_f32 v96, v96, v118, v119\n"
    "v_mfma_f32_16x16x32_bf16 a[124:127], v[168:171], v[132:135], a[124:127]\n"
    "ds_read_b128 v[160:163], v226 offset:64\n"
    "v_max_f32 v96, v96, v97\n"
    "v_mfma_f32_16x16x32_bf16 a[132:135], v[168:171], v[136:139], a[132:135]\n"
    "v_cmp_lt_f32 vcc, s88, v96\n"
    "s_nop 0\n"
    "s_cbranch_vccnz LR_l0b\n"
    "LB_l0b:\n"
    "v_mfma_f32_16x16x32_bf16 a[140:143], v[168:171], v[140:143], a[140:143]\n"
    "s_waitcnt lgkmcnt(2)\n"
    "buffer_load_dwordx4 v230, s[28:31], s72 offen offset:2112 nt lds\n"
    "v_mfma_f32_16x16x32_bf16 a[144:147], v[172:175], v[132:135], a[144:147]\n"
    "ds_read_b128 v[164:167], v226 offset:128\n"
    "v_mfma_f32_16x16x32_bf16 a[152:155], v[172:175], v[136:139], a[152:155]\n"
    "v_exp_f32 v108, v108\n"
    "v_mfma_f32_16x16x32_bf16 a[160:163], v[172:175], v[140:143], a[160:163]\n"
    "buffer_load_dwordx4 v231, s[28:31], s72 offen offset:3168 nt lds\n"
    "s_add_u32 s69, s69, 16896\n"
    "v_mfma_f32_16x16x32_bf16 a[148:151], v[176:179], v[132:135], a[148:151]\n"
    "ds_read_b128 v[168:171], v226 offset:192\n"
    "v_mfma_f32_16x16x32_bf16 a[156:159], v[176:179], v[136:139], a[156:159]\n"
    "s_cmp_eq_u32 s69, 152064\n"
    "s_cselect_b32 s69, 0, s69\n"
    "v_mfma_f32_16x16x32_bf16 a[164:167], v[176:179], v[140:143], a[164:167]\n"
    "s_add_u32 s72, s72, 16384\n"
    "v_mfma_f32_16x16x32_bf16 a[168:171], v[180:183], v[132:135], a[168:171]\n"
    "ds_read_b128 v[172:175], v226 offset:256\n"
    "v_mfma_f32_16x16x32_bf16 a[176:179], v[180:183], v[136:139], a[176:179]\n"
    "v_exp_f32 v109, v109\n"
    "v_mfma_f32_16x16x32_bf16 a[184:187], v[180:183], v[140:143], a[184:187]\n"
    "v_cvt_pk_bf16_f32 v122, v108, v109\n"
    "v_mfma_f32_16x16x32_bf16 a[172:175], v[184:187], v[132:135], a[172:175]\n"
    "ds_read_b128 v[176:179], v226 offset:320\n"
    "v_mfma_f32_16x16x32_bf16 a[180:183], v[184:187], v[136:139], a[180:183]\n"
    "v_exp_f32 v110, v110\n"
    "v_mfma_f32_16x16x32_bf16 a[188:191], v[184:187], v[140:143], a[188:191]\n"
    "s_waitcnt lgkmcnt(2)\n"
    "v_exp_f32 v111, v111\n"
    "s_nop 0\n"
    "v_cvt_pk_bf16_f32 v123, v110, v111\n"
    "v_mfma_f32_16x16x32_bf16 v[96:99], v[156:159], v[0:3], v[144:147]\n"
    "ds_read_b128 v[180:183], v226 offset:384\n"
    "v_add_u32 v227, s68, v225\n"
    "v_mfma_f32_16x16x32_bf16 v[100:103], v[156:159], v[4:7], v[148:151]\n"
    "s_add_u32 s68, s68, 16896\n"
    "v_mfma_f32_16x16x32_bf16 v[104:107], v[156:159], v[8:11], v[152:155]\n"
    "s_cmp_eq_u32 s68, 152064\n"
    "s_cselect_b32 s68, 0, s68\n"
    "v_mfma_f32_16x16x32_bf16 v[96:99], v[160:163], v[12:15], v[96:99]\n"
    "ds_read_b128 v[184:187], v226 offset:448\n"
    "v_mfma_f32_16x16x32_bf16 v[100:103], v[160:163], v[16:19], v[100:103]\n"
    "v_exp_f32 v112, v112\n"
    "v_mfma_f32_16x16x32_bf16 v[104:107], v[160:163], v[20:23], v[104:107]\n"
    "v_exp_f32 v113, v113\n"
    "v_mfma_f32_16x16x32_bf16 v[96:99], v[164:167], v[24:27], v[96:99]\n"
    "v_cvt_pk_bf16_f32 v126, v112, v113\n"
    "v_mfma_f32_16x16x32_bf16 v[100:103], v[164:167], v[28:31], v[100:103]\n"
    "v_exp_f32 v114, v114\n"
    "v_mfma_f32_16x16x32_bf16 v[104:107], v[164:167], v[32:35], v[104:107]\n"
    "v_exp_f32 v115, v115\n"
    "v_mfma_f32_16x16x32_bf16 v[96:99], v[168:171], v[36:39], v[96:99]\n"
    "ds_read_b64_tr_b16 v[156:157], v227\n"
    "v_cvt_pk_bf16_f32 v127, v114, v115\n"
    "v_mfma_f32_16x16x32_bf16 v[100:103], v[168:171], v[40:43], v[100:103]\n"
    "ds_read_b64_tr_b16 v[158:159], v227 offset:512\n"
    "v_mfma_f32_16x16x32_bf16 v[104:107], v[168:171], v[44:47], v[104:107]\n"
    "s_waitcnt lgkmcnt(2)\n"
    "v_mfma_f32_16x16x32_bf16 v[96:99], v[172:175], v[48:51], v[96:99]\n"
    "ds_read_b64_tr_b16 v[160:161], v227 offset:32\n"
    "v_mfma_f32_16x16x32_bf16 v[100:103], v[172:175], v[52:55], v[100:103]\n"
    "ds_read_b64_tr_b16 v[162:163], v227 offset:544\n"
    "v_mfma_f32_16x16x32_bf16 v[104:107], v[172:175], v[56:59], v[104:107]\n"
    "v_exp_f32 v116, v116\n"
    "v_mfma_f32_16x16x32_bf16 v[96:99], v[176:179], v[60:63], v[96:99]\n"
    "ds_read_b64_tr_b16 v[164:165], v227 offset:64\n"
    "v_mfma_f32_16x16x32_bf16 v[100:103], v[176:179], v[64:67], v[100:103]\n"
    "ds_read_b64_tr_b16 v[166:167], v227 offset:576\n"
    "v_mfma_f32_16x16x32_bf16 v[104:107], v[176:179], v[68:71], v[104:107]\n"
    "v_exp_f32 v117, v117\n"
    "v_mfma_f32_16x16x32_bf16 v[96:99], v[180:183], v[72:75], v[96:99]\n"
    "ds_read_b64_tr_b16 v[168:169], v227 offset:96\n"
    "v_cvt_pk_bf16_f32 v130, v116, v117\n"
    "v_mfma_f32_16x16x32_bf16 v[100:103], v[180:183], v[76:79], v[100:103]\n"
    "ds_read_b64_tr_b16 v[170:171], v227 offset:608\n"
    "v_mfma_f32_16x16x32_bf16 v[104:107], v[180:183], v[80:83], v[104:107]\n"
    "v_exp_f32 v118, v118\n"
    "v_mfma_f32_16x16x32_bf16 v[96:99], v[184:187], v[84:87], v[96:99]\n"
    "ds_read_b64_tr_b16 v[172:173], v227 offset:128\n"
    "v_exp_f32 v119, v119\n"
    "v_mfma_f32_16x16x32_bf16 v[100:103], v[184:187], v[88:91], v[100:103]\n"
    "ds_read_b64_tr_b16 v[174:175], v227 offset:640\n"
    "v_cvt_pk_bf16_f32 v131, v118, v119\n"
    "v_mfma_f32_16x16x32_bf16 v[104:107], v[184:187], v[92:95], v[104:107]\n"
    "v_mfma_f32_16x16x32_bf16 v[240:243], v[188:191], v[120:123], v[240:243]\n"
    "ds_read_b64_tr_b16 v[176:177], v227 offset:160\n"
    "v_mfma_f32_16x16x32_bf16 v[244:247], v[188:191], v[124:127], v[244:247]\n"
    "ds_read_b64_tr_b16 v[178:179], v227 offset:672\n"
    "v_mfma_f32_16x16x32_bf16 v[248:251], v[188:191], v[128:131], v[248:251]\n"
    "s_waitcnt lgkmcnt(4)\n"
    "v_mfma_f32_16x16x32_bf16 a[0:3], v[156:159], v[120:123], a[0:3]\n"
    "ds_read_b64_tr_b16 v[180:181], v227 offset:192\n"
    "v_max3_f32 v108, v96, v97, v98\n"
    "v_mfma_f32_16x16x32_bf16 a[8:11], v[156:159], v[124:127], a[8:11]\n"
    "ds_read_b64_tr_b16 v[182:183], v227 offset:704\n"
    "v_max3_f32 v109, v100, v101, v102\n"
    "v_mfma_f32_16x16x32_bf16 a[16:19], v[156:159], v[128:131], a[16:19]\n"
    "v_max3_f32 v108, v108, v99, v104\n"
    "v_max3_f32 v109, v109, v103, v105\n"
    "v_mfma_f32_16x16x32_bf16 a[4:7], v[160:163], v[120:123], a[4:7]\n"
    "ds_read_b64_tr_b16 v[184:185], v227 offset:224\n"
    "v_max3_f32 v108, v108, v106, v107\n"
    "v_mfma_f32_16x16x32_bf16 a[12:15], v[160:163], v[124:127], a[12:15]\n"
    "ds_read_b64_tr_b16 v[186:187], v227 offset:736\n"
    "v_max_f32 v108, v108, v109\n"
    "v_mfma_f32_16x16x32_bf16 a[20:23], v[160:163], v[128:131], a[20:23]\n"
    "v_cmp_lt_f32 vcc, s88, v108\n"
    "s_nop 0\n"
    "s_cbranch_vccnz LR_l1a\n"
    "LB_l1a:\n"
    "v_mfma_f32_16x16x32_bf16 a[24:27], v[164:167], v[120:123], a[24:27]\n"
    "ds_read_b128 v[156:159], v226 offset:512\n"
    "v_mfma_f32_16x16x32_bf16 a[32:35], v[164:167], v[124:127], a[32:35]\n"
    "v_exp_f32 v96, v96\n"
    "v_mfma_f32_16x16x32_bf16 a[40:43], v[164:167], v[128:131], a[40:43]\n"
    "v_exp_f32 v97, v97\n"
    "v_mfma_f32_16x16x32_bf16 a[28:31], v[168:171], v[120:123], a[28:31]\n"
    "ds_read_b128 v[160:163], v226 offset:576\n"
    "v_cvt_pk_bf16_f32 v132, v96, v97\n"
    "v_mfma_f32_16x16x32_bf16 a[36:39], v[168:171], v[124:127], a[36:39]\n"
    "v_exp_f32 v98, v98\n"
    "v_mfma_f32_16x16x32_bf16 a[44:47], v[168:171], v[128:131], a[44:47]\n"
    "s_waitcnt lgkmcnt(2)\n"
    "v_mfma_f32_16x16x32_bf16 a[48:51], v[172:175], v[120:123], a[48:51]\n"
    "ds_read_b128 v[164:167], v226 offset:640\n"
    "v_mfma_f32_16x16x32_bf16 a[56:59], v[172:175], v[124:127], a[56:59]\n"
    "v_exp_f32 v99, v99\n"
    "v_mfma_f32_16x16x32_bf16 a[64:67], v[172:175], v[128:131], a[64:67]\n"
    "v_cvt_pk_bf16_f32 v133, v98, v99\n"
    "v_mfma_f32_16x16x32_bf16 a[52:55], v[176:179], v[120:123], a[52:55]\n"
    "ds_read_b128 v[168:171], v226 offset:704\n"
    "v_mfma_f32_16x16x32_bf16 a[60:63], v[176:179], v[124:127], a[60:63]\n"
    "v_exp_f32 v100, v100\n"
    "v_mfma_f32_16x16x32_bf16 a[68:71], v[176:179], v[128:131], a[68:71]\n"
    "v_exp_f32 v101, v101\n"
    "v_mfma_f32_16x16x32_bf16 a[72:75], v[180:183], v[120:123], a[72:75]\n"
    "ds_read_b128 v[172:175], v226 offset:768\n"
    "v_cvt_pk_bf16_f32 v136, v100, v101\n"
    "v_mfma_f32_16x16x32_bf16 a[80:83], v[180:183], v[124:127], a[80:83]\n"
    "v_exp_f32 v102, v102\n"
    "v_mfma_f32_16x16x32_bf16 a[88:91], v[180:183], v[128:131], a[88:91]\n"
    "v_exp_f32 v103, v103\n"
    "v_mfma_f32_16x16x32_bf16 a[76:79], v[184:187], v[120:123], a[76:79]\n"
    "ds_read_b128 v[176:179], v226 offset:832\n"
    "v_cvt_pk_bf16_f32 v137, v102, v103\n"
    "v_mfma_f32_16x16x32_bf16 a[84:87], v[184:187], v[124:127], a[84:87]\n"
    "v_exp_f32 v104, v104\n"
    "v_mfma_f32_16x16x32_bf16 a[92:95], v[184:187], v[128:131], a[92:95]\n"
    "s_waitcnt lgkmcnt(2)\n"
    "v_mfma_f32_16x16x32_bf16 v[108:111], v[156:159], v[0:3], v[144:147]\n"
    "ds_read_b128 v[180:183], v226 offset:896\n"
    "v_mfma_f32_16x16x32_bf16 v[112:115], v[156:159], v[4:7], v[148:151]\n"
    "v_exp_f32 v105, v105\n"
    "v_mfma_f32_16x16x32_bf16 v[116:119], v[156:159], v[8:11], v[152:155]\n"
    "v_cvt_pk_bf16_f32 v140, v104, v105\n"
    "v_mfma_f32_16x16x32_bf16 v[108:111], v[160:163], v[12:15], v[108:111]\n"
    "ds_read_b128 v[184:187], v226 offset:960\n"
    "v_mfma_f32_16x16x32_bf16 v[112:115], v[160:163], v[16:19], v[112:115]\n"
    "v_exp_f32 v106, v106\n"
    "v_mfma_f32_16x16x32_bf16 v[116:119], v[160:163], v[20:23], v[116:119]\n"
    "v_exp_f32 v107, v107\n"
    "v_mfma_f32_16x16x32_bf16 v[108:111], v[164:167], v[24:27], v[108:111]\n"
    "ds_read_b64_tr_b16 v[156:157], v227 offset:256\n"
    "v_cvt_pk_bf16_f32 v141, v106, v107\n"
    "v_mfma_f32_16x16x32_bf16 v[112:115], v[164:167], v[28:31], v[112:115]\n"
    "ds_read_b64_tr_b16 v[158:159], v227 offset:768\n"
    "v_mfma_f32_16x16x32_bf16 v[116:119], v[164:167], v[32:35], v[116:119]\n"
    "v_mfma_f32_16x16x32_bf16 v[108:111], v[168:171], v[36:39], v[108:111]\n"
    "ds_read_b64_tr_b16 v[160:161], v227 offset:288\n"
    "v_mfma_f32_16x16x32_bf16 v[112:115], v[168:171], v[40:43], v[112:115]\n"
    "ds_read_b64_tr_b16 v[162:163], v227 offset:800\n"
    "v_mfma_f32_16x16x32_bf16 v[116:119], v[168:171], v[44:47], v[116:119]\n"
    "s_waitcnt lgkmcnt(4)\n"
    "s_waitcnt vmcnt(16)\n"
    "s_barrier\n"
    "v_add_u32 v226, s68, v224\n"
    "v_mfma_f32_16x16x32_bf16 v[108:111], v[172:175], v[48:51], v[108:111]\n"
    "ds_read_b64_tr_b16 v[164:165], v227 offset:320\n"
    "s_add_u32 m0, s69, s70\n"
    "v_mfma_f32_16x16x32_bf16 v[112:115], v[172:175], v[52:55], v[112:115]\n"
    "ds_read_b64_tr_b16 v[166:167], v227 offset:832\n"
    "s_add_u32 s68, s68, 16896\n"
    "v_mfma_f32_16x16x32_bf16 v[116:119], v[172:175], v[56:59], v[116:119]\n"
    "buffer_load_dwordx4 v228, s[24:27], s71 offen nt lds\n"
    "s_cmp_eq_u32 s68, 152064\n"
    "s_cselect_b32 s68, 0, s68\n"
    "v_mfma_f32_16x16x32_bf16 v[108:111], v[176:179], v[60:63], v[108:111]\n"
    "ds_read_b64_tr_b16 v[168:169], v227 offset:352\n"
    "v_mfma_f32_16x16x32_bf16 v[112:115], v[176:179], v[64:67], v[112:115]\n"
    "ds_read_b64_tr_b16 v[170:171], v227 offset:864\n"
    "v_mfma_f32_16x16x32_bf16 v[116:119], v[176:179], v[68:71], v[116:119]\n"
    "buffer_load_dwordx4 v229, s[24:27], s71 offen offset:1056 nt lds\n"
    "v_mfma_f32_16x16x32_bf16 v[108:111], v[180:183], v[72:75], v[108:111]\n"
    "ds_read_b64_tr_b16 v[172:173], v227 offset:384\n"
    "v_mfma_f32_16x16x32_bf16 v[112:115], v[180:183], v[76:79], v[112:115]\n"
    "ds_read_b64_tr_b16 v[174:175], v227 offset:896\n"
    "v_mfma_f32_16x16x32_bf16 v[116:119], v[180:183], v[80:83], v[116:119]\n"
    "buffer_load_dwordx4 v230, s[24:27], s71 offen offset:2112 nt lds\n"
    "v_mfma_f32_16x16x32_bf16 v[108:111], v[184:187], v[84:87], v[108:111]\n"
    "ds_read_b64_tr_b16 v[176:177], v227 offset:416\n"
    "v_mfma_f32_16x16x32_bf16 v[112:115], v[184:187], v[88:91], v[112:115]\n"
    "ds_read_b64_tr_b16 v[178:179], v227 offset:928\n"
    "v_mfma_f32_16x16x32_bf16 v[116:119], v[184:187], v[92:95], v[116:119]\n"
    "s_waitcnt lgkmcnt(4)\n"
    "buffer_load_dwordx4 v231, s[24:27], s71 offen offset:3168 nt lds\n"
    "v_mfma_f32_16x16x32_bf16 a[96:99], v[156:159], v[120:123], a[96:99]\n"
    "ds_read_b64_tr_b16 v[180:181], v227 offset:448\n"
    "s_add_u32 s69, s69, 16896\n"
    "v_mfma_f32_16x16x32_bf16 a[104:107], v[156:159], v[124:127], a[104:107]\n"
    "ds_read_b64_tr_b16 v[182:183], v227 offset:960\n"
    "v_mfma_f32_16x16x32_bf16 a[112:115], v[156:159], v[128:131], a[112:115]\n"
    "s_cmp_eq_u32 s69, 152064\n"
    "s_cselect_b32 s69, 0, s69\n"
    "v_mfma_f32_16x16x32_bf16 a[100:103], v[160:163], v[120:123], a[100:103]\n"
    "ds_read_b64_tr_b16 v[184:185], v227 offset:480\n"
    "s_add_u32 s71, s71, 16384\n"
    "v_mfma_f32_16x16x32_bf16 a[108:111], v[160:163], v[124:127], a[108:111]\n"
    "ds_read_b64_tr_b16 v[186:187], v227 offset:992\n"
    "s_add_u32 m0, s69, s70\n"
    "v_mfma_f32_16x16x32_bf16 a[116:119], v[160:163], v[128:131], a[116:119]\n"
    "buffer_load_dwordx4 v228, s[28:31], s72 offen nt lds\n"
    "v_max3_f32 v96, v108, v109, v110\n"
    "v_mfma_f32_16x16x32_bf16 a[120:123], v[164:167], v[120:123], a[120:123]\n"
    "ds_read_b128 v[156:159], v226\n"
    "v_max3_f32 v97, v112, v113, v114\n"
    "v_mfma_f32_16x16x32_bf16 a[128:131], v[164:167], v[124:127], a[128:131]\n"
    "v_max3_f32 v96, v96, v111, v116\n"
    "v_max3_f32 v97, v97, v115, v117\n"
    "v_mfma_f32_16x16x32_bf16 a[136:139], v[164:167], v[128:131], a[136:139]\n"
    "buffer_load_dwordx4 v229, s[28:31], s72 offen offset:1056 nt lds\n"
    "v_max3_f32 v96, v96, v118, v119\n"
    "v_mfma_f32_16x16x32_bf16 a[124:127], v[168:171], v[120:123], a[124:127]\n"
    "ds_read_b128 v[160:163], v226 offset:64\n"
    "v_max_f32 v96, v96, v97\n"
    "v_mfma_f32_16x16x32_bf16 a[132:135], v[168:171], v[124:127], a[132:135]\n"
    "v_cmp_lt_f32 vcc, s88, v96\n"
    "s_nop 0\n"
    "s_cbranch_vccnz LR_l1b\n"
    "LB_l1b:\n"
    "v_mfma_f32_16x16x32_bf16 a[140:143], v[168:171], v[128:131], a[140:143]\n"
    "s_waitcnt lgkmcnt(2)\n"
    "buffer_load_dwordx4 v230, s[28:31], s72 offen offset:2112 nt lds\n"
    "v_mfma_f32_16x16x32_bf16 a[144:147], v[172:175], v[120:123], a[144:147]\n"
    "ds_read_b128 v[164:167], v226 offset:128\n"
    "v_mfma_f32_16x16x32_bf16 a[152:155], v[172:175], v[124:127], a[152:155]\n"
    "v_exp_f32 v108, v108\n"
    "v_mfma_f32_16x16x32_bf16 a[160:163], v[172:175], v[128:131], a[160:163]\n"
    "buffer_load_dwordx4 v231, s[28:31], s72 offen offset:3168 nt lds\n"
    "s_add_u32 s69, s69, 16896\n"
    "v_mfma_f32_16x16x32_bf16 a[148:151], v[176:179], v[120:123], a[148:151]\n"
    "ds_read_b128 v[168:171], v226 offset:192\n"
    "v_mfma_f32_16x16x32_bf16 a[156:159], v[176:179], v[124:127], a[156:159]\n"
    "s_cmp_eq_u32 s69, 152064\n"
    "s_cselect_b32 s69, 0, s69\n"
    "v_mfma_f32_16x16x32_bf16 a[164:167], v[176:179], v[128:131], a[164:167]\n"
    "s_add_u32 s72, s72, 16384\n"
    "v_mfma_f32_16x16x32_bf16 a[168:171], v[180:183], v[120:123], a[168:171]\n"
    "ds_read_b128 v[172:175], v226 offset:256\n"
    "v_mfma_f32_16x16x32_bf16 a[176:179], v[180:183], v[124:127], a[176:179]\n"
    "v_exp_f32 v109, v109\n"
    "v_mfma_f32_16x16x32_bf16 a[184:187], v[180:183], v[128:131], a[184:187]\n"
    "v_cvt_pk_bf16_f32 v134, v108, v109\n"
    "v_mfma_f32_16x16x32_bf16 a[172:175], v[184:187], v[120:123], a[172:175]\n"
    "ds_read_b128 v[176:179], v226 offset:320\n"
    "v_mfma_f32_16x16x32_bf16 a[180:183], v[184:187], v[124:127], a[180:183]\n"
    "v_exp_f32 v110, v110\n"
    "v_mfma_f32_16x16x32_bf16 a[188:191], v[184:187], v[128:131], a[188:191]\n"
    "s_waitcnt lgkmcnt(2)\n"
    "v_exp_f32 v111, v111\n"
    "s_nop 0\n"
    "v_cvt_pk_bf16_f32 v135, v110, v111\n"
    "s_sub_u32 s75, s75, 1\n"
    "s_cmp_lg_u32 s75, 0\n"
    "s_cbranch_scc1 LAL\n"
    "v_mfma_f32_16x16x32_bf16 v[96:99], v[156:159], v[0:3], v[144:147]\n"
    "ds_read_b128 v[180:183], v226 offset:384\n"
    "v_add_u32 v227, s68, v225\n"
    "v_mfma_f32_16x16x32_bf16 v[100:103], v[156:159], v[4:7], v[148:151]\n"
    "s_add_u32 s68, s68, 16896\n"
    "v_mfma_f32_16x16x32_bf16 v[104:107], v[156:159], v[8:11], v[152:155]\n"
    "s_cmp_eq_u32 s68, 152064\n"
    "s_cselect_b32 s68, 0, s68\n"
    "v_mfma_f32_16x16x32_bf16 v[96:99], v[160:163], v[12:15], v[96:99]\n"
    "ds_read_b128 v[184:187], v226 offset:448\n"
    "v_mfma_f32_16x16x32_bf16 v[100:103], v[160:163], v[16:19], v[100:103]\n"
    "v_exp_f32 v112, v112\n"
    "v_mfma_f32_16x16x32_bf16 v[104:107], v[160:163], v[20:23], v[104:107]\n"
    "v_exp_f32 v113, v113\n"
    "v_mfma_f32_16x16x32_bf16 v[96:99], v[164:167], v[24:27], v[96:99]\n"
    "v_cvt_pk_bf16_f32 v138, v112, v113\n"
    "v_mfma_f32_16x16x32_bf16 v[100:103], v[164:167], v[28:31], v[100:103]\n"
    "v_exp_f32 v114, v114\n"
    "v_mfma_f32_16x16x32_bf16 v[104:107], v[164:167], v[32:35], v[104:107]\n"
    "v_exp_f32 v115, v115\n"
    "v_mfma_f32_16x16x32_bf16 v[96:99], v[168:171], v[36:39], v[96:99]\n"
    "ds_read_b64_tr_b16 v[156:157], v227\n"
    "v_cvt_pk_bf16_f32 v139, v114, v115\n"
    "v_mfma_f32_16x16x32_bf16 v[100:103], v[168:171], v[40:43], v[100:103]\n"
    "ds_read_b64_tr_b16 v[158:159], v227 offset:512\n"
    "v_mfma_f32_16x16x32_bf16 v[104:107], v[168:171], v[44:47], v[104:107]\n"
    "s_waitcnt lgkmcnt(2)\n"
    "v_mfma_f32_16x16x32_bf16 v[96:99], v[172:175], v[48:51], v[96:99]\n"
    "ds_read_b64_tr_b16 v[160:161], v227 offset:32\n"
    "v_mfma_f32_16x16x32_bf16 v[100:103], v[172:175], v[52:55], v[100:103]\n"
    "ds_read_b64_tr_b16 v[162:163], v227 offset:544\n"
    "v_mfma_f32_16x16x32_bf16 v[104:107], v[172:175], v[56:59], v[104:107]\n"
    "v_exp_f32 v116, v116\n"
    "v_mfma_f32_16x16x32_bf16 v[96:99], v[176:179], v[60:63], v[96:99]\n"
    "ds_read_b64_tr_b16 v[164:165], v227 offset:64\n"
    "v_mfma_f32_16x16x32_bf16 v[100:103], v[176:179], v[64:67], v[100:103]\n"
    "ds_read_b64_tr_b16 v[166:167], v227 offset:576\n"
    "v_mfma_f32_16x16x32_bf16 v[104:107], v[176:179], v[68:71], v[104:107]\n"
    "v_exp_f32 v117, v117\n"
    "v_mfma_f32_16x16x32_bf16 v[96:99], v[180:183], v[72:75], v[96:99]\n"
    "ds_read_b64_tr_b16 v[168:169], v227 offset:96\n"
    "v_cvt_pk_bf16_f32 v142, v116, v117\n"
    "v_mfma_f32_16x16x32_bf16 v[100:103], v[180:183], v[76:79], v[100:103]\n"
    "ds_read_b64_tr_b16 v[170:171], v227 offset:608\n"
    "v_mfma_f32_16x16x32_bf16 v[104:107], v[180:183], v[80:83], v[104:107]\n"
    "v_exp_f32 v118, v118\n"
    "v_mfma_f32_16x16x32_bf16 v[96:99], v[184:187], v[84:87], v[96:99]\n"
    "ds_read_b64_tr_b16 v[172:173], v227 offset:128\n"
    "v_exp_f32 v119, v119\n"
    "v_mfma_f32_16x16x32_bf16 v[100:103], v[184:187], v[88:91], v[100:103]\n"
    "ds_read_b64_tr_b16 v[174:175], v227 offset:640\n"
    "v_cvt_pk_bf16_f32 v143, v118, v119\n"
    "v_mfma_f32_16x16x32_bf16 v[104:107], v[184:187], v[92:95], v[104:107]\n"
    "v_mfma_f32_16x16x32_bf16 v[240:243], v[188:191], v[132:135], v[240:243]\n"
    "ds_read_b64_tr_b16 v[176:177], v227 offset:160\n"
    "v_mfma_f32_16x16x32_bf16 v[244:247], v[188:191], v[136:139], v[244:247]\n"
    "ds_read_b64_tr_b16 v[178:179], v227 offset:672\n"
    "v_mfma_f32_16x16x32_bf16 v[248:251], v[188:191], v[140:143], v[248:251]\n"
    "s_waitcnt lgkmcnt(4)\n"
    "v_mfma_f32_16x16x32_bf16 a[0:3], v[156:159], v[132:135], a[0:3]\n"
    "ds_read_b64_tr_b16 v[180:181], v227 offset:192\n"
    "v_max3_f32 v108, v96, v97, v98\n"
    "v_mfma_f32_16x16x32_bf16 a[8:11], v[156:159], v[136:139], a[8:11]\n"
    "ds_read_b64_tr_b16 v[182:183], v227 offset:704\n"
    "v_max3_f32 v109, v100, v101, v102\n"
    "v_mfma_f32_16x16x32_bf16 a[16:19], v[156:159], v[140:143], a[16:19]\n"
    "v_max3_f32 v108, v108, v99, v104\n"
    "v_max3_f32 v109, v109, v103, v105\n"
    "v_mfma_f32_16x16x32_bf16 a[4:7], v[160:163], v[132:135], a[4:7]\n"
    "ds_read_b64_tr_b16 v[184:185], v227 offset:224\n"
    "v_max3_f32 v108, v108, v106, v107\n"
    "v_mfma_f32_16x16x32_bf16 a[12:15], v[160:163], v[136:139], a[12:15]\n"
    "ds_read_b64_tr_b16 v[186:187], v227 offset:736\n"
    "v_max_f32 v108, v108, v109\n"
    "v_mfma_f32_16x16x32_bf16 a[20:23], v[160:163], v[140:143], a[20:23]\n"
    "v_cmp_lt_f32 vcc, s88, v108\n"
    "s_nop 0\n"
    "s_cbranch_vccnz LR_s11a\n"
    "LB_s11a:\n"
    "v_mfma_f32_16x16x32_bf16 a[24:27], v[164:167], v[132:135], a[24:27]\n"
    "ds_read_b128 v[156:159], v226 offset:512\n"
    "v_mfma_f32_16x16x32_bf16 a[32:35], v[164:167], v[136:139], a[32:35]\n"
    "v_exp_f32 v96, v96\n"
    "v_mfma_f32_16x16x32_bf16 a[40:43], v[164:167], v[140:143], a[40:43]\n"
    "v_exp_f32 v97, v97\n"
    "v_mfma_f32_16x16x32_bf16 a[28:31], v[168:171], v[132:135], a[28:31]\n"
    "ds_read_b128 v[160:163], v226 offset:576\n"
    "v_cvt_pk_bf16_f32 v120, v96, v97\n"
    "v_mfma_f32_16x16x32_bf16 a[36:39], v[168:171], v[136:139], a[36:39]\n"
    "v_exp_f32 v98, v98\n"
    "v_mfma_f32_16x16x32_bf16 a[44:47], v[168:171], v[140:143], a[44:47]\n"
    "s_waitcnt lgkmcnt(2)\n"
    "v_mfma_f32_16x16x32_bf16 a[48:51], v[172:175], v[132:135], a[48:51]\n"
    "ds_read_b128 v[164:167], v226 offset:640\n"
    "v_mfma_f32_16x16x32_bf16 a[56:59], v[172:175], v[136:139], a[56:59]\n"
    "v_exp_f32 v99, v99\n"
    "v_mfma_f32_16x16x32_bf16 a[64:67], v[172:175], v[140:143], a[64:67]\n"
    "v_cvt_pk_bf16_f32 v121, v98, v99\n"
    "v_mfma_f32_16x16x32_bf16 a[52:55], v[176:179], v[132:135], a[52:55]\n"
    "ds_read_b128 v[168:171], v226 offset:704\n"
    "v_mfma_f32_16x16x32_bf16 a[60:63], v[176:179], v[136:139], a[60:63]\n"
    "v_exp_f32 v100, v100\n"
    "v_mfma_f32_16x16x32_bf16 a[68:71], v[176:179], v[140:143], a[68:71]\n"
    "v_exp_f32 v101, v101\n"
    "v_mfma_f32_16x16x32_bf16 a[72:75], v[180:183], v[132:135], a[72:75]\n"
    "ds_read_b128 v[172:175], v226 offset:768\n"
    "v_cvt_pk_bf16_f32 v124, v100, v101\n"
    "v_mfma_f32_16x16x32_bf16 a[80:83], v[180:183], v[136:139], a[80:83]\n"
    "v_exp_f32 v102, v102\n"
    "v_mfma_f32_16x16x32_bf16 a[88:91], v[180:183], v[140:143], a[88:91]\n"
    "v_exp_f32 v103, v103\n"
    "v_mfma_f32_16x16x32_bf16 a[76:79], v[184:187], v[132:135], a[76:79]\n"
    "ds_read_b128 v[176:179], v226 offset:832\n"
    "v_cvt_pk_bf16_f32 v125, v102, v103\n"
    "v_mfma_f32_16x16x32_bf16 a[84:87], v[184:187], v[136:139], a[84:87]\n"
    "v_exp_f32 v104, v104\n"
    "v_mfma_f32_16x16x32_bf16 a[92:95], v[184:187], v[140:143], a[92:95]\n"
    "s_waitcnt lgkmcnt(2)\n"
    "v_mfma_f32_16x16x32_bf16 v[108:111], v[156:159], v[0:3], v[144:147]\n"
    "ds_read_b128 v[180:183], v226 offset:896\n"
    "v_mfma_f32_16x16x32_bf16 v[112:115], v[156:159], v[4:7], v[148:151]\n"
    "v_exp_f32 v105, v105\n"
    "v_mfma_f32_16x16x32_bf16 v[116:119], v[156:159], v[8:11], v[152:155]\n"
    "v_cvt_pk_bf16_f32 v128, v104, v105\n"
    "v_mfma_f32_16x16x32_bf16 v[108:111], v[160:163], v[12:15], v[108:111]\n"
    "ds_read_b128 v[184:187], v226 offset:960\n"
    "v_mfma_f32_16x16x32_bf16 v[112:115], v[160:163], v[16:19], v[112:115]\n"
    "v_exp_f32 v106, v106\n"
    "v_mfma_f32_16x16x32_bf16 v[116:119], v[160:163], v[20:23], v[116:119]\n"
    "v_exp_f32 v107, v107\n"
    "v_mfma_f32_16x16x32_bf16 v[108:111], v[164:167], v[24:27], v[108:111]\n"
    "ds_read_b64_tr_b16 v[156:157], v227 offset:256\n"
    "v_cvt_pk_bf16_f32 v129, v106, v107\n"
    "v_mfma_f32_16x16x32_bf16 v[112:115], v[164:167], v[28:31], v[112:115]\n"
    "ds_read_b64_tr_b16 v[158:159], v227 offset:768\n"
    "v_mfma_f32_16x16x32_bf16 v[116:119], v[164:167], v[32:35], v[116:119]\n"
    "v_mfma_f32_16x16x32_bf16 v[108:111], v[168:171], v[36:39], v[108:111]\n"
    "ds_read_b64_tr_b16 v[160:161], v227 offset:288\n"
    "v_mfma_f32_16x16x32_bf16 v[112:115], v[168:171], v[40:43], v[112:115]\n"
    "ds_read_b64_tr_b16 v[162:163], v227 offset:800\n"
    "v_mfma_f32_16x16x32_bf16 v[116:119], v[168:171], v[44:47], v[116:119]\n"
    "s_waitcnt lgkmcnt(4)\n"
    "s_waitcnt vmcnt(16)\n"
    "s_barrier\n"
    "v_add_u32 v226, s68, v224\n"
    "v_mfma_f32_16x16x32_bf16 v[108:111], v[172:175], v[48:51], v[108:111]\n"
    "ds_read_b64_tr_b16 v[164:165], v227 offset:320\n"
    "s_add_u32 s69, s69, 16896\n"
    "v_mfma_f32_16x16x32_bf16 v[112:115], v[172:175], v[52:55], v[112:115]\n"
    "ds_read_b64_tr_b16 v[166:167], v227 offset:832\n"
    "s_add_u32 s68, s68, 16896\n"
    "v_mfma_f32_16x16x32_bf16 v[116:119], v[172:175], v[56:59], v[116:119]\n"
    "s_cmp_eq_u32 s69, 152064\n"
    "s_cselect_b32 s69, 0, s69\n"
    "v_mfma_f32_16x16x32_bf16 v[108:111], v[176:179], v[60:63], v[108:111]\n"
    "ds_read_b64_tr_b16 v[168:169], v227 offset:352\n"
    "s_add_u32 m0, s69, s70\n"
    "v_mfma_f32_16x16x32_bf16 v[112:115], v[176:179], v[64:67], v[112:115]\n"
    "ds_read_b64_tr_b16 v[170:171], v227 offset:864\n"
    "s_cmp_eq_u32 s68, 152064\n"
    "s_cselect_b32 s68, 0, s68\n"
    "v_mfma_f32_16x16x32_bf16 v[116:119], v[176:179], v[68:71], v[116:119]\n"
    "buffer_load_dwordx4 v228, s[28:31], s72 offen nt lds\n"
    "v_mfma_f32_16x16x32_bf16 v[108:111], v[180:183], v[72:75], v[108:111]\n"
    "ds_read_b64_tr_b16 v[172:173], v227 offset:384\n"
    "v_mfma_f32_16x16x32_bf16 v[112:115], v[180:183], v[76:79], v[112:115]\n"
    "ds_read_b64_tr_b16 v[174:175], v227 offset:896\n"
    "v_mfma_f32_16x16x32_bf16 v[116:119], v[180:183], v[80:83], v[116:119]\n"
    "buffer_load_dwordx4 v229, s[28:31], s72 offen offset:1056 nt lds\n"
    "v_mfma_f32_16x16x32_bf16 v[108:111], v[184:187], v[84:87], v[108:111]\n"
    "ds_read_b64_tr_b16 v[176:177], v227 offset:416\n"
    "v_mfma_f32_16x16x32_bf16 v[112:115], v[184:187], v[88:91], v[112:115]\n"
    "ds_read_b64_tr_b16 v[178:179], v227 offset:928\n"
    "v_mfma_f32_16x16x32_bf16 v[116:119], v[184:187], v[92:95], v[116:119]\n"
    "s_waitcnt lgkmcnt(4)\n"
    "buffer_load_dwordx4 v230, s[28:31], s72 offen offset:2112 nt lds\n"
    "v_mfma_f32_16x16x32_bf16 a[96:99], v[156:159], v[132:135], a[96:99]\n"
    "ds_read_b64_tr_b16 v[180:181], v227 offset:448\n"
    "v_mfma_f32_16x16x32_bf16 a[104:107], v[156:159], v[136:139], a[104:107]\n"
    "ds_read_b64_tr_b16 v[182:183], v227 offset:960\n"
    "v_mfma_f32_16x16x32_bf16 a[112:115], v[156:159], v[140:143], a[112:115]\n"
    "buffer_load_dwordx4 v231, s[28:31], s72 offen offset:3168 nt lds\n"
    "s_add_u32 s69, s69, 16896\n"
    "v_mfma_f32_16x16x32_bf16 a[100:103], v[160:163], v[132:135], a[100:103]\n"
    "ds_read_b64_tr_b16 v[184:185], v227 offset:480\n"
    "v_max3_f32 v96, v108, v109, v110\n"
    "v_mfma_f32_16x16x32_bf16 a[108:111], v[160:163], v[136:139], a[108:111]\n"
    "ds_read_b64_tr_b16 v[186:187], v227 offset:992\n"
    "v_max3_f32 v97, v112, v113, v114\n"
    "v_mfma_f32_16x16x32_bf16 a[116:119], v[160:163], v[140:143], a[116:119]\n"
    "s_cmp_eq_u32 s69, 152064\n"
    "s_cselect_b32 s69, 0, s69\n"
    "v_mfma_f32_16x16x32_bf16 a[120:123], v[164:167], v[132:135], a[120:123]\n"
    "ds_read_b128 v[156:159], v226\n"
    "s_add_u32 s72, s72, 16384\n"
    "v_mfma_f32_16x16x32_bf16 a[128:131], v[164:167], v[136:139], a[128:131]\n"
    "v_max3_f32 v96, v96, v111, v116\n"
    "v_max3_f32 v97, v97, v115, v117\n"
    "v_mfma_f32_16x16x32_bf16 a[136:139], v[164:167], v[140:143], a[136:139]\n"
    "v_max3_f32 v96, v96, v118, v119\n"
    "v_max_f32 v96, v96, v97\n"
    "v_mfma_f32_16x16x32_bf16 a[124:127], v[168:171], v[132:135], a[124:127]\n"
    "ds_read_b128 v[160:163], v226 offset:64\n"
    "v_cmp_lt_f32 vcc, s88, v96\n"
    "s_nop 0\n"
    "s_cbranch_vccnz LR_s11b\n"
    "LB_s11b:\n"
    "v_mfma_f32_16x16x32_bf16 a[132:135], v[168:171], v[136:139], a[132:135]\n"
    "v_exp_f32 v108, v108\n"
    "v_mfma_f32_16x16x32_bf16 a[140:143], v[168:171], v[140:143], a[140:143]\n"
    "s_waitcnt lgkmcnt(2)\n"
    "v_mfma_f32_16x16x32_bf16 a[144:147], v[172:175], v[132:135], a[144:147]\n"
    "ds_read_b128 v[164:167], v226 offset:128\n"
    "v_mfma_f32_16x16x32_bf16 a[152:155], v[172:175], v[136:139], a[152:155]\n"
    "v_exp_f32 v109, v109\n"
    "v_mfma_f32_16x16x32_bf16 a[160:163], v[172:175], v[140:143], a[160:163]\n"
    "v_cvt_pk_bf16_f32 v122, v108, v109\n"
    "v_mfma_f32_16x16x32_bf16 a[148:151], v[176:179], v[132:135], a[148:151]\n"
    "ds_read_b128 v[168:171], v226 offset:192\n"
    "v_mfma_f32_16x16x32_bf16 a[156:159], v[176:179], v[136:139], a[156:159]\n"
    "v_exp_f32 v110, v110\n"
    "v_mfma_f32_16x16x32_bf16 a[164:167], v[176:179], v[140:143], a[164:167]\n"
    "v_exp_f32 v111, v111\n"
    "v_mfma_f32_16x16x32_bf16 a[168:171], v[180:183], v[132:135], a[168:171]\n"
    "ds_read_b128 v[172:175], v226 offset:256\n"
    "v_cvt_pk_bf16_f32 v123, v110, v111\n"
    "v_mfma_f32_16x16x32_bf16 a[176:179], v[180:183], v[136:139], a[176:179]\n"
    "v_mfma_f32_16x16x32_bf16 a[184:187], v[180:183], v[140:143], a[184:187]\n"
    "v_mfma_f32_16x16x32_bf16 a[172:175], v[184:187], v[132:135], a[172:175]\n"
    "ds_read_b128 v[176:179], v226 offset:320\n"
    "v_mfma_f32_16x16x32_bf16 a[180:183], v[184:187], v[136:139], a[180:183]\n"
    "v_mfma_f32_16x16x32_bf16 a[188:191], v[184:187], v[140:143], a[188:191]\n"
    "s_waitcnt lgkmcnt(2)\n"
    "v_mfma_f32_16x16x32_bf16 v[96:99], v[156:159], v[0:3], v[144:147]\n"
    "ds_read_b128 v[180:183], v226 offset:384\n"
    "v_add_u32 v227, s68, v225\n"
    "v_mfma_f32_16x16x32_bf16 v[100:103], v[156:159], v[4:7], v[148:151]\n"
    "s_add_u32 s68, s68, 16896\n"
    "v_mfma_f32_16x16x32_bf16 v[104:107], v[156:159], v[8:11], v[152:155]\n"
    "s_cmp_eq_u32 s68, 152064\n"
    "s_cselect_b32 s68, 0, s68\n"
    "v_mfma_f32_16x16x32_bf16 v[96:99], v[160:163], v[12:15], v[96:99]\n"
    "ds_read_b128 v[184:187], v226 offset:448\n"
    "v_mfma_f32_16x16x32_bf16 v[100:103], v[160:163], v[16:19], v[100:103]\n"
    "v_exp_f32 v112, v112\n"
    "v_mfma_f32_16x16x32_bf16 v[104:107], v[160:163], v[20:23], v[104:107]\n"
    "v_exp_f32 v113, v113\n"
    "v_mfma_f32_16x16x32_bf16 v[96:99], v[164:167], v[24:27], v[96:99]\n"
    "v_cvt_pk_bf16_f32 v126, v112, v113\n"
    "v_mfma_f32_16x16x32_bf16 v[100:103], v[164:167], v[28:31], v[100:103]\n"
    "v_exp_f32 v114, v114\n"
    "v_mfma_f32_16x16x32_bf16 v[104:107], v[164:167], v[32:35], v[104:107]\n"
    "v_exp_f32 v115, v115\n"
    "v_mfma_f32_16x16x32_bf16 v[96:99], v[168:171], v[36:39], v[96:99]\n"
    "ds_read_b64_tr_b16 v[156:157], v227\n"
    "v_cvt_pk_bf16_f32 v127, v114, v115\n"
    "v_mfma_f32_16x16x32_bf16 v[100:103], v[168:171], v[40:43], v[100:103]\n"
    "ds_read_b64_tr_b16 v[158:159], v227 offset:512\n"
    "v_mfma_f32_16x16x32_bf16 v[104:107], v[168:171], v[44:47], v[104:107]\n"
    "s_waitcnt lgkmcnt(2)\n"
    "v_mfma_f32_16x16x32_bf16 v[96:99], v[172:175], v[48:51], v[96:99]\n"
    "ds_read_b64_tr_b16 v[160:161], v227 offset:32\n"
    "v_mfma_f32_16x16x32_bf16 v[100:103], v[172:175], v[52:55], v[100:103]\n"
    "ds_read_b64_tr_b16 v[162:163], v227 offset:544\n"
    "v_mfma_f32_16x16x32_bf16 v[104:107], v[172:175], v[56:59], v[104:107]\n"
    "v_exp_f32 v116, v116\n"
    "v_mfma_f32_16x16x32_bf16 v[96:99], v[176:179], v[60:63], v[96:99]\n"
    "ds_read_b64_tr_b16 v[164:165], v227 offset:64\n"
    "v_mfma_f32_16x16x32_bf16 v[100:103], v[176:179], v[64:67], v[100:103]\n"
    "ds_read_b64_tr_b16 v[166:167], v227 offset:576\n"
    "v_mfma_f32_16x16x32_bf16 v[104:107], v[176:179], v[68:71], v[104:107]\n"
    "v_exp_f32 v117, v117\n"
    "v_mfma_f32_16x16x32_bf16 v[96:99], v[180:183], v[72:75], v[96:99]\n"
    "ds_read_b64_tr_b16 v[168:169], v227 offset:96\n"
    "v_cvt_pk_bf16_f32 v130, v116, v117\n"
    "v_mfma_f32_16x16x32_bf16 v[100:103], v[180:183], v[76:79], v[100:103]\n"
    "ds_read_b64_tr_b16 v[170:171], v227 offset:608\n"
    "v_mfma_f32_16x16x32_bf16 v[104:107], v[180:183], v[80:83], v[104:107]\n"
    "v_exp_f32 v118, v118\n"
    "v_mfma_f32_16x16x32_bf16 v[96:99], v[184:187], v[84:87], v[96:99]\n"
    "ds_read_b64_tr_b16 v[172:173], v227 offset:128\n"
    "v_exp_f32 v119, v119\n"
    "v_mfma_f32_16x16x32_bf16 v[100:103], v[184:187], v[88:91], v[100:103]\n"
    "ds_read_b64_tr_b16 v[174:175], v227 offset:640\n"
    "v_cvt_pk_bf16_f32 v131, v118, v119\n"
    "v_mfma_f32_16x16x32_bf16 v[104:107], v[184:187], v[92:95], v[104:107]\n"
    "v_mfma_f32_16x16x32_bf16 v[240:243], v[188:191], v[120:123], v[240:243]\n"
    "ds_read_b64_tr_b16 v[176:177], v227 offset:160\n"
    "v_mfma_f32_16x16x32_bf16 v[244:247], v[188:191], v[124:127], v[244:247]\n"
    "ds_read_b64_tr_b16 v[178:179], v227 offset:672\n"
    "v_mfma_f32_16x16x32_bf16 v[248:251], v[188:191], v[128:131], v[248:251]\n"
    "s_waitcnt lgkmcnt(4)\n"
    "v_mfma_f32_16x16x32_bf16 a[0:3], v[156:159], v[120:123], a[0:3]\n"
    "ds_read_b64_tr_b16 v[180:181], v227 offset:192\n"
    "v_max3_f32 v108, v96, v97, v98\n"
    "v_mfma_f32_16x16x32_bf16 a[8:11], v[156:159], v[124:127], a[8:11]\n"
    "ds_read_b64_tr_b16 v[182:183], v227 offset:704\n"
    "v_max3_f32 v109, v100, v101, v102\n"
    "v_mfma_f32_16x16x32_bf16 a[16:19], v[156:159], v[128:131], a[16:19]\n"
    "v_max3_f32 v108, v108, v99, v104\n"
    "v_max3_f32 v109, v109, v103, v105\n"
    "v_mfma_f32_16x16x32_bf16 a[4:7], v[160:163], v[120:123], a[4:7]\n"
    "ds_read_b64_tr_b16 v[184:185], v227 offset:224\n"
    "v_max3_f32 v108, v108, v106, v107\n"
    "v_mfma_f32_16x16x32_bf16 a[12:15], v[160:163], v[124:127], a[12:15]\n"
    "ds_read_b64_tr_b16 v[186:187], v227 offset:736\n"
    "v_max_f32 v108, v108, v109\n"
    "v_mfma_f32_16x16x32_bf16 a[20:23], v[160:163], v[128:131], a[20:23]\n"
    "v_cmp_lt_f32 vcc, s88, v108\n"
    "s_nop 0\n"
    "s_cbranch_vccnz LR_s12a\n"
    "LB_s12a:\n"
    "v_mfma_f32_16x16x32_bf16 a[24:27], v[164:167], v[120:123], a[24:27]\n"
    "ds_read_b128 v[156:159], v226 offset:512\n"
    "v_mfma_f32_16x16x32_bf16 a[32:35], v[164:167], v[124:127], a[32:35]\n"
    "v_exp_f32 v96, v96\n"
    "v_mfma_f32_16x16x32_bf16 a[40:43], v[164:167], v[128:131], a[40:43]\n"
    "v_exp_f32 v97, v97\n"
    "v_mfma_f32_16x16x32_bf16 a[28:31], v[168:171], v[120:123], a[28:31]\n"
    "ds_read_b128 v[160:163], v226 offset:576\n"
    "v_cvt_pk_bf16_f32 v132, v96, v97\n"
    "v_mfma_f32_16x16x32_bf16 a[36:39], v[168:171], v[124:127], a[36:39]\n"
    "v_exp_f32 v98, v98\n"
    "v_mfma_f32_16x16x32_bf16 a[44:47], v[168:171], v[128:131], a[44:47]\n"
    "s_waitcnt lgkmcnt(2)\n"
    "v_mfma_f32_16x16x32_bf16 a[48:51], v[172:175], v[120:123], a[48:51]\n"
    "ds_read_b128 v[164:167], v226 offset:640\n"
    "v_mfma_f32_16x16x32_bf16 a[56:59], v[172:175], v[124:127], a[56:59]\n"
    "v_exp_f32 v99, v99\n"
    "v_mfma_f32_16x16x32_bf16 a[64:67], v[172:175], v[128:131], a[64:67]\n"
    "v_cvt_pk_bf16_f32 v133, v98, v99\n"
    "v_mfma_f32_16x16x32_bf16 a[52:55], v[176:179], v[120:123], a[52:55]\n"
    "ds_read_b128 v[168:171], v226 offset:704\n"
    "v_mfma_f32_16x16x32_bf16 a[60:63], v[176:179], v[124:127], a[60:63]\n"
    "v_exp_f32 v100, v100\n"
    "v_mfma_f32_16x16x32_bf16 a[68:71], v[176:179], v[128:131], a[68:71]\n"
    "v_exp_f32 v101, v101\n"
    "v_mfma_f32_16x16x32_bf16 a[72:75], v[180:183], v[120:123], a[72:75]\n"
    "ds_read_b128 v[172:175], v226 offset:768\n"
    "v_cvt_pk_bf16_f32 v136, v100, v101\n"
    "v_mfma_f32_16x16x32_bf16 a[80:83], v[180:183], v[124:127], a[80:83]\n"
    "v_exp_f32 v102, v102\n"
    "v_mfma_f32_16x16x32_bf16 a[88:91], v[180:183], v[128:131], a[88:91]\n"
    "v_exp_f32 v103, v103\n"
    "v_mfma_f32_16x16x32_bf16 a[76:79], v[184:187], v[120:123], a[76:79]\n"
    "ds_read_b128 v[176:179], v226 offset:832\n"
    "v_cvt_pk_bf16_f32 v137, v102, v103\n"
    "v_mfma_f32_16x16x32_bf16 a[84:87], v[184:187], v[124:127], a[84:87]\n"
    "v_exp_f32 v104, v104\n"
    "v_mfma_f32_16x16x32_bf16 a[92:95], v[184:187], v[128:131], a[92:95]\n"
    "s_waitcnt lgkmcnt(2)\n"
    "v_mfma_f32_16x16x32_bf16 v[108:111], v[156:159], v[0:3], v[144:147]\n"
    "ds_read_b128 v[180:183], v226 offset:896\n"
    "v_mfma_f32_16x16x32_bf16 v[112:115], v[156:159], v[4:7], v[148:151]\n"
    "v_exp_f32 v105, v105\n"
    "v_mfma_f32_16x16x32_bf16 v[116:119], v[156:159], v[8:11], v[152:155]\n"
    "v_cvt_pk_bf16_f32 v140, v104, v105\n"
    "v_mfma_f32_16x16x32_bf16 v[108:111], v[160:163], v[12:15], v[108:111]\n"
    "ds_read_b128 v[184:187], v226 offset:960\n"
    "v_mfma_f32_16x16x32_bf16 v[112:115], v[160:163], v[16:19], v[112:115]\n"
    "v_exp_f32 v106, v106\n"
    "v_mfma_f32_16x16x32_bf16 v[116:119], v[160:163], v[20:23], v[116:119]\n"
    "v_exp_f32 v107, v107\n"
    "v_mfma_f32_16x16x32_bf16 v[108:111], v[164:167], v[24:27], v[108:111]\n"
    "ds_read_b64_tr_b16 v[156:157], v227 offset:256\n"
    "v_cvt_pk_bf16_f32 v141, v106, v107\n"
    "v_mfma_f32_16x16x32_bf16 v[112:115], v[164:167], v[28:31], v[112:115]\n"
    "ds_read_b64_tr_b16 v[158:159], v227 offset:768\n"
    "v_mfma_f32_16x16x32_bf16 v[116:119], v[164:167], v[32:35], v[116:119]\n"
    "v_mfma_f32_16x16x32_bf16 v[108:111], v[168:171], v[36:39], v[108:111]\n"
    "ds_read_b64_tr_b16 v[160:161], v227 offset:288\n"
    "v_mfma_f32_16x16x32_bf16 v[112:115], v[168:171], v[40:43], v[112:115]\n"
    "ds_read_b64_tr_b16 v[162:163], v227 offset:800\n"
    "v_mfma_f32_16x16x32_bf16 v[116:119], v[168:171], v[44:47], v[116:119]\n"
    "s_waitcnt lgkmcnt(4)\n"
    "s_waitcnt vmcnt(12)\n"
    "s_barrier\n"
    "v_add_u32 v226, s68, v224\n"
    "v_mfma_f32_16x16x32_bf16 v[108:111], v[172:175], v[48:51], v[108:111]\n"
    "ds_read_b64_tr_b16 v[164:165], v227 offset:320\n"
    "s_add_u32 m0, s69, s70\n"
    "v_mfma_f32_16x16x32_bf16 v[112:115], v[172:175], v[52:55], v[112:115]\n"
    "ds_read_b64_tr_b16 v[166:167], v227 offset:832\n"
    "s_add_u32 s68, s68, 16896\n"
    "v_mfma_f32_16x16x32_bf16 v[116:119], v[172:175], v[56:59], v[116:119]\n"
    "buffer_load_dwordx4 v232, s[56:59], s73 offen lds\n"
    "s_cmp_eq_u32 s68, 152064\n"
    "s_cselect_b32 s68, 0, s68\n"
    "v_mfma_f32_16x16x32_bf16 v[108:111], v[176:179], v[60:63], v[108:111]\n"
    "ds_read_b64_tr_b16 v[168:169], v227 offset:352\n"
    "v_mfma_f32_16x16x32_bf16 v[112:115], v[176:179], v[64:67], v[112:115]\n"
    "ds_read_b64_tr_b16 v[170:171], v227 offset:864\n"
    "v_mfma_f32_16x16x32_bf16 v[116:119], v[176:179], v[68:71], v[116:119]\n"
    "buffer_load_dwordx4 v233, s[56:59], s73 offen offset:1056 lds\n"
    "v_mfma_f32_16x16x32_bf16 v[108:111], v[180:183], v[72:75], v[108:111]\n"
    "ds_read_b64_tr_b16 v[172:173], v227 offset:384\n"
    "v_mfma_f32_16x16x32_bf16 v[112:115], v[180:183], v[76:79], v[112:115]\n"
    "ds_read_b64_tr_b16 v[174:175], v227 offset:896\n"
    "v_mfma_f32_16x16x32_bf16 v[116:119], v[180:183], v[80:83], v[116:119]\n"
    "buffer_load_dwordx4 v234, s[56:59], s73 offen offset:2112 lds\n"
    "v_mfma_f32_16x16x32_bf16 v[108:111], v[184:187], v[84:87], v[108:111]\n"
    "ds_read_b64_tr_b16 v[176:177], v227 offset:416\n"
    "v_mfma_f32_16x16x32_bf16 v[112:115], v[184:187], v[88:91], v[112:115]\n"
    "ds_read_b64_tr_b16 v[178:179], v227 offset:928\n"
    "v_mfma_f32_16x16x32_bf16 v[116:119], v[184:187], v[92:95], v[116:119]\n"
    "s_waitcnt lgkmcnt(4)\n"
    "buffer_load_dwordx4 v235, s[56:59], s73 offen offset:3168 lds\n"
    "v_mfma_f32_16x16x32_bf16 a[96:99], v[156:159], v[120:123], a[96:99]\n"
    "ds_read_b64_tr_b16 v[180:181], v227 offset:448\n"
    "s_add_u32 s69, s69, 16896\n"
    "v_mfma_f32_16x16x32_bf16 a[104:107], v[156:159], v[124:127], a[104:107]\n"
    "ds_read_b64_tr_b16 v[182:183], v227 offset:960\n"
    "v_mfma_f32_16x16x32_bf16 a[112:115], v[156:159], v[128:131], a[112:115]\n"
    "s_cmp_eq_u32 s69, 152064\n"
    "s_cselect_b32 s69, 0, s69\n"
    "v_mfma_f32_16x16x32_bf16 a[100:103], v[160:163], v[120:123], a[100:103]\n"
    "ds_read_b64_tr_b16 v[184:185], v227 offset:480\n"
    "s_add_u32 s73, s73, s74\n"
    "v_mfma_f32_16x16x32_bf16 a[108:111], v[160:163], v[124:127], a[108:111]\n"
    "ds_read_b64_tr_b16 v[186:187], v227 offset:992\n"
    "s_add_u32 m0, s69, s70\n"
    "v_mfma_f32_16x16x32_bf16 a[116:119], v[160:163], v[128:131], a[116:119]\n"
    "buffer_load_dwordx4 v232, s[56:59], s73 offen lds\n"
    "v_max3_f32 v96, v108, v109, v110\n"
    "v_mfma_f32_16x16x32_bf16 a[120:123], v[164:167], v[120:123], a[120:123]\n"
    "ds_read_b128 v[156:159], v226\n"
    "v_max3_f32 v97, v112, v113, v114\n"
    "v_mfma_f32_16x16x32_bf16 a[128:131], v[164:167], v[124:127], a[128:131]\n"
    "v_max3_f32 v96, v96, v111, v116\n"
    "v_max3_f32 v97, v97, v115, v117\n"
    "v_mfma_f32_16x16x32_bf16 a[136:139], v[164:167], v[128:131], a[136:139]\n"
    "buffer_load_dwordx4 v233, s[56:59], s73 offen offset:1056 lds\n"
    "v_max3_f32 v96, v96, v118, v119\n"
    "v_mfma_f32_16x16x32_bf16 a[124:127], v[168:171], v[120:123], a[124:127]\n"
    "ds_read_b128 v[160:163], v226 offset:64\n"
    "v_max_f32 v96, v96, v97\n"
    "v_mfma_f32_16x16x32_bf16 a[132:135], v[168:171], v[124:127], a[132:135]\n"
    "v_cmp_lt_f32 vcc, s88, v96\n"
    "s_nop 0\n"
    "s_cbranch_vccnz LR_s12b\n"
    "LB_s12b:\n"
    "v_mfma_f32_16x16x32_bf16 a[140:143], v[168:171], v[128:131], a[140:143]\n"
    "s_waitcnt lgkmcnt(2)\n"
    "buffer_load_dwordx4 v234, s[56:59], s73 offen offset:2112 lds\n"
    "v_mfma_f32_16x16x32_bf16 a[144:147], v[172:175], v[120:123], a[144:147]\n"
    "ds_read_b128 v[164:167], v226 offset:128\n"
    "v_mfma_f32_16x16x32_bf16 a[152:155], v[172:175], v[124:127], a[152:155]\n"
    "v_exp_f32 v108, v108\n"
    "v_mfma_f32_16x16x32_bf16 a[160:163], v[172:175], v[128:131], a[160:163]\n"
    "buffer_load_dwordx4 v235, s[56:59], s73 offen offset:3168 lds\n"
    "s_add_u32 s69, s69, 16896\n"
    "v_mfma_f32_16x16x32_bf16 a[148:151], v[176:179], v[120:123], a[148:151]\n"
    "ds_read_b128 v[168:171], v226 offset:192\n"
    "v_mfma_f32_16x16x32_bf16 a[156:159], v[176:179], v[124:127], a[156:159]\n"
    "s_cmp_eq_u32 s69, 152064\n"
    "s_cselect_b32 s69, 0, s69\n"
    "v_mfma_f32_16x16x32_bf16 a[164:167], v[176:179], v[128:131], a[164:167]\n"
    "s_add_u32 s73, s73, s74\n"
    "v_mfma_f32_16x16x32_bf16 a[168:171], v[180:183], v[120:123], a[168:171]\n"
    "ds_read_b128 v[172:175], v226 offset:256\n"
    "v_mfma_f32_16x16x32_bf16 a[176:179], v[180:183], v[124:127], a[176:179]\n"
    "v_exp_f32 v109, v109\n"
    "v_mfma_f32_16x16x32_bf16 a[184:187], v[180:183], v[128:131], a[184:187]\n"
    "v_cvt_pk_bf16_f32 v134, v108, v109\n"
    "v_mfma_f32_16x16x32_bf16 a[172:175], v[184:187], v[120:123], a[172:175]\n"
    "ds_read_b128 v[176:179], v226 offset:320\n"
    "v_mfma_f32_16x16x32_bf16 a[180:183], v[184:187], v[124:127], a[180:183]\n"
    "v_exp_f32 v110, v110\n"
    "v_mfma_f32_16x16x32_bf16 a[188:191], v[184:187], v[128:131], a[188:191]\n"
    "s_waitcnt lgkmcnt(2)\n"
    "v_exp_f32 v111, v111\n"
    "s_nop 0\n"
    "v_cvt_pk_bf16_f32 v135, v110, v111\n"
    "v_mfma_f32_16x16x32_bf16 v[96:99], v[156:159], v[0:3], v[144:147]\n"
    "ds_read_b128 v[180:183], v226 offset:384\n"
    "v_add_u32 v227, s68, v225\n"
    "v_mfma_f32_16x16x32_bf16 v[100:103], v[156:159], v[4:7], v[148:151]\n"
    "s_add_u32 s68, s68, 16896\n"
    "v_mfma_f32_16x16x32_bf16 v[104:107], v[156:159], v[8:11], v[152:155]\n"
    "s_cmp_eq_u32 s68, 152064\n"
    "s_cselect_b32 s68, 0, s68\n"
    "v_mfma_f32_16x16x32_bf16 v[96:99], v[160:163], v[12:15], v[96:99]\n"
    "ds_read_b128 v[184:187], v226 offset:448\n"
    "v_mfma_f32_16x16x32_bf16 v[100:103], v[160:163], v[16:19], v[100:103]\n"
    "v_exp_f32 v112, v112\n"
    "v_mfma_f32_16x16x32_bf16 v[104:107], v[160:163], v[20:23], v[104:107]\n"
    "v_exp_f32 v113, v113\n"
    "v_mfma_f32_16x16x32_bf16 v[96:99], v[164:167], v[24:27], v[96:99]\n"
    "v_cvt_pk_bf16_f32 v138, v112, v113\n"
    "v_mfma_f32_16x16x32_bf16 v[100:103], v[164:167], v[28:31], v[100:103]\n"
    "v_exp_f32 v114, v114\n"
    "v_mfma_f32_16x16x32_bf16 v[104:107], v[164:167], v[32:35], v[104:107]\n"
    "v_exp_f32 v115, v115\n"
    "v_mfma_f32_16x16x32_bf16 v[96:99], v[168:171], v[36:39], v[96:99]\n"
    "ds_read_b64_tr_b16 v[156:157], v227\n"
    "v_cvt_pk_bf16_f32 v139, v114, v115\n"
    "v_mfma_f32_16x16x32_bf16 v[100:103], v[168:171], v[40:43], v[100:103]\n"
    "ds_read_b64_tr_b16 v[158:159], v227 offset:512\n"
    "v_mfma_f32_16x16x32_bf16 v[104:107], v[168:171], v[44:47], v[104:107]\n"
    "s_waitcnt lgkmcnt(2)\n"
    "v_mfma_f32_16x16x32_bf16 v[96:99], v[172:175], v[48:51], v[96:99]\n"
    "ds_read_b64_tr_b16 v[160:161], v227 offset:32\n"
    "v_mfma_f32_16x16x32_bf16 v[100:103], v[172:175], v[52:55], v[100:103]\n"
    "ds_read_b64_tr_b16 v[162:163], v227 offset:544\n"
    "v_mfma_f32_16x16x32_bf16 v[104:107], v[172:175], v[56:59], v[104:107]\n"
    "v_exp_f32 v116, v116\n"
    "v_mfma_f32_16x16x32_bf16 v[96:99], v[176:179], v[60:63], v[96:99]\n"
    "ds_read_b64_tr_b16 v[164:165], v227 offset:64\n"
    "v_mfma_f32_16x16x32_bf16 v[100:103], v[176:179], v[64:67], v[100:103]\n"
    "ds_read_b64_tr_b16 v[166:167], v227 offset:576\n"
    "v_mfma_f32_16x16x32_bf16 v[104:107], v[176:179], v[68:71], v[104:107]\n"
    "v_exp_f32 v117, v117\n"
    "v_mfma_f32_16x16x32_bf16 v[96:99], v[180:183], v[72:75], v[96:99]\n"
    "ds_read_b64_tr_b16 v[168:169], v227 offset:96\n"
    "v_cvt_pk_bf16_f32 v142, v116, v117\n"
    "v_mfma_f32_16x16x32_bf16 v[100:103], v[180:183], v[76:79], v[100:103]\n"
    "ds_read_b64_tr_b16 v[170:171], v227 offset:608\n"
    "v_mfma_f32_16x16x32_bf16 v[104:107], v[180:183], v[80:83], v[104:107]\n"
    "v_exp_f32 v118, v118\n"
    "v_mfma_f32_16x16x32_bf16 v[96:99], v[184:187], v[84:87], v[96:99]\n"
    "ds_read_b64_tr_b16 v[172:173], v227 offset:128\n"
    "v_exp_f32 v119, v119\n"
    "v_mfma_f32_16x16x32_bf16 v[100:103], v[184:187], v[88:91], v[100:103]\n"
    "ds_read_b64_tr_b16 v[174:175], v227 offset:640\n"
    "v_cvt_pk_bf16_f32 v143, v118, v119\n"
    "v_mfma_f32_16x16x32_bf16 v[104:107], v[184:187], v[92:95], v[104:107]\n"
    "v_mfma_f32_16x16x32_bf16 v[240:243], v[188:191], v[132:135], v[240:243]\n"
    "ds_read_b64_tr_b16 v[176:177], v227 offset:160\n"
    "v_mfma_f32_16x16x32_bf16 v[244:247], v[188:191], v[136:139], v[244:247]\n"
    "ds_read_b64_tr_b16 v[178:179], v227 offset:672\n"
    "v_mfma_f32_16x16x32_bf16 v[248:251], v[188:191], v[140:143], v[248:251]\n"
    "s_waitcnt lgkmcnt(4)\n"
    "v_mfma_f32_16x16x32_bf16 a[0:3], v[156:159], v[132:135], a[0:3]\n"
    "ds_read_b64_tr_b16 v[180:181], v227 offset:192\n"
    "v_max3_f32 v108, v96, v97, v98\n"
    "v_mfma_f32_16x16x32_bf16 a[8:11], v[156:159], v[136:139], a[8:11]\n"
    "ds_read_b64_tr_b16 v[182:183], v227 offset:704\n"
    "v_max3_f32 v109, v100, v101, v102\n"
    "v_mfma_f32_16x16x32_bf16 a[16:19], v[156:159], v[140:143], a[16:19]\n"
    "v_max3_f32 v108, v108, v99, v104\n"
    "v_max3_f32 v109, v109, v103, v105\n"
    "v_mfma_f32_16x16x32_bf16 a[4:7], v[160:163], v[132:135], a[4:7]\n"
    "ds_read_b64_tr_b16 v[184:185], v227 offset:224\n"
    "v_max3_f32 v108, v108, v106, v107\n"
    "v_mfma_f32_16x16x32_bf16 a[12:15], v[160:163], v[136:139], a[12:15]\n"
    "ds_read_b64_tr_b16 v[186:187], v227 offset:736\n"
    "v_max_f32 v108, v108, v109\n"
    "v_mfma_f32_16x16x32_bf16 a[20:23], v[160:163], v[140:143], a[20:23]\n"
    "v_cmp_lt_f32 vcc, s88, v108\n"
    "s_nop 0\n"
    "s_cbranch_vccnz LR_s13a\n"
    "LB_s13a:\n"
    "v_mfma_f32_16x16x32_bf16 a[24:27], v[164:167], v[132:135], a[24:27]\n"
    "ds_read_b128 v[156:159], v226 offset:512\n"
    "v_mfma_f32_16x16x32_bf16 a[32:35], v[164:167], v[136:139], a[32:35]\n"
    "v_exp_f32 v96, v96\n"
    "v_mfma_f32_16x16x32_bf16 a[40:43], v[164:167], v[140:143], a[40:43]\n"
    "v_exp_f32 v97, v97\n"
    "v_mfma_f32_16x16x32_bf16 a[28:31], v[168:171], v[132:135], a[28:31]\n"
    "ds_read_b128 v[160:163], v226 offset:576\n"
    "v_cvt_pk_bf16_f32 v120, v96, v97\n"
    "v_mfma_f32_16x16x32_bf16 a[36:39], v[168:171], v[136:139], a[36:39]\n"
    "v_exp_f32 v98, v98\n"
    "v_mfma_f32_16x16x32_bf16 a[44:47], v[168:171], v[140:143], a[44:47]\n"
    "s_waitcnt lgkmcnt(2)\n"
    "v_mfma_f32_16x16x32_bf16 a[48:51], v[172:175], v[132:135], a[48:51]\n"
    "ds_read_b128 v[164:167], v226 offset:640\n"
    "v_mfma_f32_16x16x32_bf16 a[56:59], v[172:175], v[136:139], a[56:59]\n"
    "v_exp_f32 v99, v99\n"
    "v_mfma_f32_16x16x32_bf16 a[64:67], v[172:175], v[140:143], a[64:67]\n"
    "v_cvt_pk_bf16_f32 v121, v98, v99\n"
    "v_mfma_f32_16x16x32_bf16 a[52:55], v[176:179], v[132:135], a[52:55]\n"
    "ds_read_b128 v[168:171], v226 offset:704\n"
    "v_mfma_f32_16x16x32_bf16 a[60:63], v[176:179], v[136:139], a[60:63]\n"
    "v_exp_f32 v100, v100\n"
    "v_mfma_f32_16x16x32_bf16 a[68:71], v[176:179], v[140:143], a[68:71]\n"
    "v_exp_f32 v101, v101\n"
    "v_mfma_f32_16x16x32_bf16 a[72:75], v[180:183], v[132:135], a[72:75]\n"
    "ds_read_b128 v[172:175], v226 offset:768\n"
    "v_cvt_pk_bf16_f32 v124, v100, v101\n"
    "v_mfma_f32_16x16x32_bf16 a[80:83], v[180:183], v[136:139], a[80:83]\n"
    "v_exp_f32 v102, v102\n"
    "v_mfma_f32_16x16x32_bf16 a[88:91], v[180:183], v[140:143], a[88:91]\n"
    "v_exp_f32 v103, v103\n"
    "v_mfma_f32_16x16x32_bf16 a[76:79], v[184:187], v[132:135], a[76:79]\n"
    "ds_read_b128 v[176:179], v226 offset:832\n"
    "v_cvt_pk_bf16_f32 v125, v102, v103\n"
    "v_mfma_f32_16x16x32_bf16 a[84:87], v[184:187], v[136:139], a[84:87]\n"
    "v_exp_f32 v104, v104\n"
    "v_mfma_f32_16x16x32_bf16 a[92:95], v[184:187], v[140:143], a[92:95]\n"
    "s_waitcnt lgkmcnt(2)\n"
    "v_mfma_f32_16x16x32_bf16 v[108:111], v[156:159], v[0:3], v[144:147]\n"
    "ds_read_b128 v[180:183], v226 offset:896\n"
    "v_mfma_f32_16x16x32_bf16 v[112:115], v[156:159], v[4:7], v[148:151]\n"
    "v_exp_f32 v105, v105\n"
    "v_mfma_f32_16x16x32_bf16 v[116:119], v[156:159], v[8:11], v[152:155]\n"
    "v_cvt_pk_bf16_f32 v128, v104, v105\n"
    "v_mfma_f32_16x16x32_bf16 v[108:111], v[160:163], v[12:15], v[108:111]\n"
    "ds_read_b128 v[184:187], v226 offset:960\n"
    "v_mfma_f32_16x16x32_bf16 v[112:115], v[160:163], v[16:19], v[112:115]\n"
    "v_exp_f32 v106, v106\n"
    "v_mfma_f32_16x16x32_bf16 v[116:119], v[160:163], v[20:23], v[116:119]\n"
    "v_exp_f32 v107, v107\n"
    "v_mfma_f32_16x16x32_bf16 v[108:111], v[164:167], v[24:27], v[108:111]\n"
    "ds_read_b64_tr_b16 v[156:157], v227 offset:256\n"
    "v_cvt_pk_bf16_f32 v129, v106, v107\n"
    "v_mfma_f32_16x16x32_bf16 v[112:115], v[164:167], v[28:31], v[112:115]\n"
    "ds_read_b64_tr_b16 v[158:159], v227 offset:768\n"
    "v_mfma_f32_16x16x32_bf16 v[116:119], v[164:167], v[32:35], v[116:119]\n"
    "v_mfma_f32_16x16x32_bf16 v[108:111], v[168:171], v[36:39], v[108:111]\n"
    "ds_read_b64_tr_b16 v[160:161], v227 offset:288\n"
    "v_mfma_f32_16x16x32_bf16 v[112:115], v[168:171], v[40:43], v[112:115]\n"
    "ds_read_b64_tr_b16 v[162:163], v227 offset:800\n"
    "v_mfma_f32_16x16x32_bf16 v[116:119], v[168:171], v[44:47], v[116:119]\n"
    "s_waitcnt lgkmcnt(4)\n"
    "s_waitcnt vmcnt(12)\n"
    "s_barrier\n"
    "v_add_u32 v226, s68, v224\n"
    "v_mfma_f32_16x16x32_bf16 v[108:111], v[172:175], v[48:51], v[108:111]\n"
    "ds_read_b64_tr_b16 v[164:165], v227 offset:320\n"
    "s_add_u32 m0, s69, s70\n"
    "v_mfma_f32_16x16x32_bf16 v[112:115], v[172:175], v[52:55], v[112:115]\n"
    "ds_read_b64_tr_b16 v[166:167], v227 offset:832\n"
    "s_add_u32 s68, s68, 16896\n"
    "v_mfma_f32_16x16x32_bf16 v[116:119], v[172:175], v[56:59], v[116:119]\n"
    "buffer_load_dwordx4 v232, s[56:59], s73 offen lds\n"
    "s_cmp_eq_u32 s68, 152064\n"
    "s_cselect_b32 s68, 0, s68\n"
    "v_mfma_f32_16x16x32_bf16 v[108:111], v[176:179], v[60:63], v[108:111]\n"
    "ds_read_b64_tr_b16 v[168:169], v227 offset:352\n"
    "v_mfma_f32_16x16x32_bf16 v[112:115], v[176:179], v[64:67], v[112:115]\n"
    "ds_read_b64_tr_b16 v[170:171], v227 offset:864\n"
    "v_mfma_f32_16x16x32_bf16 v[116:119], v[176:179], v[68:71], v[116:119]\n"
    "buffer_load_dwordx4 v233, s[56:59], s73 offen offset:1056 lds\n"
    "v_mfma_f32_16x16x32_bf16 v[108:111], v[180:183], v[72:75], v[108:111]\n"
    "ds_read_b64_tr_b16 v[172:173], v227 offset:384\n"
    "v_mfma_f32_16x16x32_bf16 v[112:115], v[180:183], v[76:79], v[112:115]\n"
    "ds_read_b64_tr_b16 v[174:175], v227 offset:896\n"
    "v_mfma_f32_16x16x32_bf16 v[116:119], v[180:183], v[80:83], v[116:119]\n"
    "buffer_load_dwordx4 v234, s[56:59], s73 offen offset:2112 lds\n"
    "v_mfma_f32_16x16x32_bf16 v[108:111], v[184:187], v[84:87], v[108:111]\n"
    "ds_read_b64_tr_b16 v[176:177], v227 offset:416\n"
    "v_mfma_f32_16x16x32_bf16 v[112:115], v[184:187], v[88:91], v[112:115]\n"
    "ds_read_b64_tr_b16 v[178:179], v227 offset:928\n"
    "v_mfma_f32_16x16x32_bf16 v[116:119], v[184:187], v[92:95], v[116:119]\n"
    "s_waitcnt lgkmcnt(4)\n"
    "buffer_load_dwordx4 v235, s[56:59], s73 offen offset:3168 lds\n"
    "v_mfma_f32_16x16x32_bf16 a[96:99], v[156:159], v[132:135], a[96:99]\n"
    "ds_read_b64_tr_b16 v[180:181], v227 offset:448\n"
    "s_add_u32 s69, s69, 16896\n"
    "v_mfma_f32_16x16x32_bf16 a[104:107], v[156:159], v[136:139], a[104:107]\n"
    "ds_read_b64_tr_b16 v[182:183], v227 offset:960\n"
    "v_mfma_f32_16x16x32_bf16 a[112:115], v[156:159], v[140:143], a[112:115]\n"
    "s_cmp_eq_u32 s69, 152064\n"
    "s_cselect_b32 s69, 0, s69\n"
    "v_mfma_f32_16x16x32_bf16 a[100:103], v[160:163], v[132:135], a[100:103]\n"
    "ds_read_b64_tr_b16 v[184:185], v227 offset:480\n"
    "s_add_u32 s73, s73, s74\n"
    "v_mfma_f32_16x16x32_bf16 a[108:111], v[160:163], v[136:139], a[108:111]\n"
    "ds_read_b64_tr_b16 v[186:187], v227 offset:992\n"
    "s_add_u32 m0, s69, s70\n"
    "v_mfma_f32_16x16x32_bf16 a[116:119], v[160:163], v[140:143], a[116:119]\n"
    "buffer_load_dwordx4 v232, s[56:59], s73 offen lds\n"
    "v_max3_f32 v96, v108, v109, v110\n"
    "v_mfma_f32_16x16x32_bf16 a[120:123], v[164:167], v[132:135], a[120:123]\n"
    "ds_read_b128 v[156:159], v226\n"
    "v_max3_f32 v97, v112, v113, v114\n"
    "v_mfma_f32_16x16x32_bf16 a[128:131], v[164:167], v[136:139], a[128:131]\n"
    "v_max3_f32 v96, v96, v111, v116\n"
    "v_max3_f32 v97, v97, v115, v117\n"
    "v_mfma_f32_16x16x32_bf16 a[136:139], v[164:167], v[140:143], a[136:139]\n"
    "buffer_load_dwordx4 v233, s[56:59], s73 offen offset:1056 lds\n"
    "v_max3_f32 v96, v96, v118, v119\n"
    "v_mfma_f32_16x16x32_bf16 a[124:127], v[168:171], v[132:135], a[124:127]\n"
    "ds_read_b128 v[160:163], v226 offset:64\n"
    "v_max_f32 v96, v96, v97\n"
    "v_mfma_f32_16x16x32_bf16 a[132:135], v[168:171], v[136:139], a[132:135]\n"
    "v_cmp_lt_f32 vcc, s88, v96\n"
    "s_nop 0\n"
    "s_cbranch_vccnz LR_s13b\n"
    "LB_s13b:\n"
    "v_mfma_f32_16x16x32_bf16 a[140:143], v[168:171], v[140:143], a[140:143]\n"
    "s_waitcnt lgkmcnt(2)\n"
    "buffer_load_dwordx4 v234, s[56:59], s73 offen offset:2112 lds\n"
    "v_mfma_f32_16x16x32_bf16 a[144:147], v[172:175], v[132:135], a[144:147]\n"
    "ds_read_b128 v[164:167], v226 offset:128\n"
    "v_mfma_f32_16x16x32_bf16 a[152:155], v[172:175], v[136:139], a[152:155]\n"
    "v_exp_f32 v108, v108\n"
    "v_mfma_f32_16x16x32_bf16 a[160:163], v[172:175], v[140:143], a[160:163]\n"
    "buffer_load_dwordx4 v235, s[56:59], s73 offen offset:3168 lds\n"
    "s_add_u32 s69, s69, 16896\n"
    "v_mfma_f32_16x16x32_bf16 a[148:151], v[176:179], v[132:135], a[148:151]\n"
    "ds_read_b128 v[168:171], v226 offset:192\n"
    "v_mfma_f32_16x16x32_bf16 a[156:159], v[176:179], v[136:139], a[156:159]\n"
    "s_cmp_eq_u32 s69, 152064\n"
    "s_cselect_b32 s69, 0, s69\n"
    "v_mfma_f32_16x16x32_bf16 a[164:167], v[176:179], v[140:143], a[164:167]\n"
    "s_add_u32 s73, s73, s81\n"
    "v_mfma_f32_16x16x32_bf16 a[168:171], v[180:183], v[132:135], a[168:171]\n"
    "ds_read_b128 v[172:175], v226 offset:256\n"
    "v_mfma_f32_16x16x32_bf16 a[176:179], v[180:183], v[136:139], a[176:179]\n"
    "v_exp_f32 v109, v109\n"
    "v_mfma_f32_16x16x32_bf16 a[184:187], v[180:183], v[140:143], a[184:187]\n"
    "v_cvt_pk_bf16_f32 v122, v108, v109\n"
    "v_mfma_f32_16x16x32_bf16 a[172:175], v[184:187], v[132:135], a[172:175]\n"
    "ds_read_b128 v[176:179], v226 offset:320\n"
    "v_mfma_f32_16x16x32_bf16 a[180:183], v[184:187], v[136:139], a[180:183]\n"
    "v_exp_f32 v110, v110\n"
    "v_mfma_f32_16x16x32_bf16 a[188:191], v[184:187], v[140:143], a[188:191]\n"
    "s_waitcnt lgkmcnt(2)\n"
    "v_exp_f32 v111, v111\n"
    "s_nop 0\n"
    "v_cvt_pk_bf16_f32 v123, v110, v111\n"
    "v_mfma_f32_16x16x32_bf16 v[96:99], v[156:159], v[0:3], v[144:147]\n"
    "ds_read_b128 v[180:183], v226 offset:384\n"
    "v_add_u32 v227, s68, v225\n"
    "v_mfma_f32_16x16x32_bf16 v[100:103], v[156:159], v[4:7], v[148:151]\n"
    "s_add_u32 s68, s68, 16896\n"
    "v_mfma_f32_16x16x32_bf16 v[104:107], v[156:159], v[8:11], v[152:155]\n"
    "s_cmp_eq_u32 s68, 152064\n"
    "s_cselect_b32 s68, 0, s68\n"
    "v_mfma_f32_16x16x32_bf16 v[96:99], v[160:163], v[12:15], v[96:99]\n"
    "ds_read_b128 v[184:187], v226 offset:448\n"
    "v_mfma_f32_16x16x32_bf16 v[100:103], v[160:163], v[16:19], v[100:103]\n"
    "v_exp_f32 v112, v112\n"
    "v_mfma_f32_16x16x32_bf16 v[104:107], v[160:163], v[20:23], v[104:107]\n"
    "v_exp_f32 v113, v113\n"
    "v_mfma_f32_16x16x32_bf16 v[96:99], v[164:167], v[24:27], v[96:99]\n"
    "v_cvt_pk_bf16_f32 v126, v112, v113\n"
    "v_mfma_f32_16x16x32_bf16 v[100:103], v[164:167], v[28:31], v[100:103]\n"
    "v_exp_f32 v114, v114\n"
    "v_mfma_f32_16x16x32_bf16 v[104:107], v[164:167], v[32:35], v[104:107]\n"
    "v_exp_f32 v115, v115\n"
    "v_mfma_f32_16x16x32_bf16 v[96:99], v[168:171], v[36:39], v[96:99]\n"
    "ds_read_b64_tr_b16 v[156:157], v227\n"
    "v_cvt_pk_bf16_f32 v127, v114, v115\n"
    "v_mfma_f32_16x16x32_bf16 v[100:103], v[168:171], v[40:43], v[100:103]\n"
    "ds_read_b64_tr_b16 v[158:159], v227 offset:512\n"
    "v_mfma_f32_16x16x32_bf16 v[104:107], v[168:171], v[44:47], v[104:107]\n"
    "s_waitcnt lgkmcnt(2)\n"
    "v_mfma_f32_16x16x32_bf16 v[96:99], v[172:175], v[48:51], v[96:99]\n"
    "ds_read_b64_tr_b16 v[160:161], v227 offset:32\n"
    "v_mfma_f32_16x16x32_bf16 v[100:103], v[172:175], v[52:55], v[100:103]\n"
    "ds_read_b64_tr_b16 v[162:163], v227 offset:544\n"
    "v_mfma_f32_16x16x32_bf16 v[104:107], v[172:175], v[56:59], v[104:107]\n"
    "v_exp_f32 v116, v116\n"
    "v_mfma_f32_16x16x32_bf16 v[96:99], v[176:179], v[60:63], v[96:99]\n"
    "ds_read_b64_tr_b16 v[164:165], v227 offset:64\n"
    "v_mfma_f32_16x16x32_bf16 v[100:103], v[176:179], v[64:67], v[100:103]\n"
    "ds_read_b64_tr_b16 v[166:167], v227 offset:576\n"
    "v_mfma_f32_16x16x32_bf16 v[104:107], v[176:179], v[68:71], v[104:107]\n"
    "v_exp_f32 v117, v117\n"
    "v_mfma_f32_16x16x32_bf16 v[96:99], v[180:183], v[72:75], v[96:99]\n"
    "ds_read_b64_tr_b16 v[168:169], v227 offset:96\n"
    "v_cvt_pk_bf16_f32 v130, v116, v117\n"
    "v_mfma_f32_16x16x32_bf16 v[100:103], v[180:183], v[76:79], v[100:103]\n"
    "ds_read_b64_tr_b16 v[170:171], v227 offset:608\n"
    "v_mfma_f32_16x16x32_bf16 v[104:107], v[180:183], v[80:83], v[104:107]\n"
    "v_exp_f32 v118, v118\n"
    "v_mfma_f32_16x16x32_bf16 v[96:99], v[184:187], v[84:87], v[96:99]\n"
    "ds_read_b64_tr_b16 v[172:173], v227 offset:128\n"
    "v_exp_f32 v119, v119\n"
    "v_mfma_f32_16x16x32_bf16 v[100:103], v[184:187], v[88:91], v[100:103]\n"
    "ds_read_b64_tr_b16 v[174:175], v227 offset:640\n"
    "v_cvt_pk_bf16_f32 v131, v118, v119\n"
    "v_mfma_f32_16x16x32_bf16 v[104:107], v[184:187], v[92:95], v[104:107]\n"
    "v_mfma_f32_16x16x32_bf16 v[240:243], v[188:191], v[120:123], v[240:243]\n"
    "ds_read_b64_tr_b16 v[176:177], v227 offset:160\n"
    "v_mfma_f32_16x16x32_bf16 v[244:247], v[188:191], v[124:127], v[244:247]\n"
    "ds_read_b64_tr_b16 v[178:179], v227 offset:672\n"
    "v_mfma_f32_16x16x32_bf16 v[248:251], v[188:191], v[128:131], v[248:251]\n"
    "s_waitcnt lgkmcnt(4)\n"
    "v_mfma_f32_16x16x32_bf16 a[0:3], v[156:159], v[120:123], a[0:3]\n"
    "ds_read_b64_tr_b16 v[180:181], v227 offset:192\n"
    "v_max3_f32 v108, v96, v97, v98\n"
    "v_mfma_f32_16x16x32_bf16 a[8:11], v[156:159], v[124:127], a[8:11]\n"
    "ds_read_b64_tr_b16 v[182:183], v227 offset:704\n"
    "v_max3_f32 v109, v100, v101, v102\n"
    "v_mfma_f32_16x16x32_bf16 a[16:19], v[156:159], v[128:131], a[16:19]\n"
    "v_max3_f32 v108, v108, v99, v104\n"
    "v_max3_f32 v109, v109, v103, v105\n"
    "v_mfma_f32_16x16x32_bf16 a[4:7], v[160:163], v[120:123], a[4:7]\n"
    "ds_read_b64_tr_b16 v[184:185], v227 offset:224\n"
    "v_max3_f32 v108, v108, v106, v107\n"
    "v_mfma_f32_16x16x32_bf16 a[12:15], v[160:163], v[124:127], a[12:15]\n"
    "ds_read_b64_tr_b16 v[186:187], v227 offset:736\n"
    "v_max_f32 v108, v108, v109\n"
    "v_mfma_f32_16x16x32_bf16 a[20:23], v[160:163], v[128:131], a[20:23]\n"
    "v_cmp_lt_f32 vcc, s88, v108\n"
    "s_nop 0\n"
    "s_cbranch_vccnz LR_s14a\n"
    "LB_s14a:\n"
    "v_mfma_f32_16x16x32_bf16 a[24:27], v[164:167], v[120:123], a[24:27]\n"
    "ds_read_b128 v[156:159], v226 offset:512\n"
    "v_mfma_f32_16x16x32_bf16 a[32:35], v[164:167], v[124:127], a[32:35]\n"
    "v_exp_f32 v96, v96\n"
    "v_mfma_f32_16x16x32_bf16 a[40:43], v[164:167], v[128:131], a[40:43]\n"
    "v_exp_f32 v97, v97\n"
    "v_mfma_f32_16x16x32_bf16 a[28:31], v[168:171], v[120:123], a[28:31]\n"
    "ds_read_b128 v[160:163], v226 offset:576\n"
    "v_cvt_pk_bf16_f32 v132, v96, v97\n"
    "v_mfma_f32_16x16x32_bf16 a[36:39], v[168:171], v[124:127], a[36:39]\n"
    "v_exp_f32 v98, v98\n"
    "v_mfma_f32_16x16x32_bf16 a[44:47], v[168:171], v[128:131], a[44:47]\n"
    "s_waitcnt lgkmcnt(2)\n"
    "v_mfma_f32_16x16x32_bf16 a[48:51], v[172:175], v[120:123], a[48:51]\n"
    "ds_read_b128 v[164:167], v226 offset:640\n"
    "v_mfma_f32_16x16x32_bf16 a[56:59], v[172:175], v[124:127], a[56:59]\n"
    "v_exp_f32 v99, v99\n"
    "v_mfma_f32_16x16x32_bf16 a[64:67], v[172:175], v[128:131], a[64:67]\n"
    "v_cvt_pk_bf16_f32 v133, v98, v99\n"
    "v_mfma_f32_16x16x32_bf16 a[52:55], v[176:179], v[120:123], a[52:55]\n"
    "ds_read_b128 v[168:171], v226 offset:704\n"
    "v_mfma_f32_16x16x32_bf16 a[60:63], v[176:179], v[124:127], a[60:63]\n"
    "v_exp_f32 v100, v100\n"
    "v_mfma_f32_16x16x32_bf16 a[68:71], v[176:179], v[128:131], a[68:71]\n"
    "v_exp_f32 v101, v101\n"
    "v_mfma_f32_16x16x32_bf16 a[72:75], v[180:183], v[120:123], a[72:75]\n"
    "ds_read_b128 v[172:175], v226 offset:768\n"
    "v_cvt_pk_bf16_f32 v136, v100, v101\n"
    "v_mfma_f32_16x16x32_bf16 a[80:83], v[180:183], v[124:127], a[80:83]\n"
    "v_exp_f32 v102, v102\n"
    "v_mfma_f32_16x16x32_bf16 a[88:91], v[180:183], v[128:131], a[88:91]\n"
    "v_exp_f32 v103, v103\n"
    "v_mfma_f32_16x16x32_bf16 a[76:79], v[184:187], v[120:123], a[76:79]\n"
    "ds_read_b128 v[176:179], v226 offset:832\n"
    "v_cvt_pk_bf16_f32 v137, v102, v103\n"
    "v_mfma_f32_16x16x32_bf16 a[84:87], v[184:187], v[124:127], a[84:87]\n"
    "v_exp_f32 v104, v104\n"
    "v_mfma_f32_16x16x32_bf16 a[92:95], v[184:187], v[128:131], a[92:95]\n"
    "s_waitcnt lgkmcnt(2)\n"
    "v_mfma_f32_16x16x32_bf16 v[108:111], v[156:159], v[0:3], v[144:147]\n"
    "ds_read_b128 v[180:183], v226 offset:896\n"
    "v_mfma_f32_16x16x32_bf16 v[112:115], v[156:159], v[4:7], v[148:151]\n"
    "v_exp_f32 v105, v105\n"
    "v_mfma_f32_16x16x32_bf16 v[116:119], v[156:159], v[8:11], v[152:155]\n"
    "v_cvt_pk_bf16_f32 v140, v104, v105\n"
    "v_mfma_f32_16x16x32_bf16 v[108:111], v[160:163], v[12:15], v[108:111]\n"
    "ds_read_b128 v[184:187], v226 offset:960\n"
    "v_mfma_f32_16x16x32_bf16 v[112:115], v[160:163], v[16:19], v[112:115]\n"
    "v_exp_f32 v106, v106\n"
    "v_mfma_f32_16x16x32_bf16 v[116:119], v[160:163], v[20:23], v[116:119]\n"
    "v_exp_f32 v107, v107\n"
    "v_mfma_f32_16x16x32_bf16 v[108:111], v[164:167], v[24:27], v[108:111]\n"
    "ds_read_b64_tr_b16 v[156:157], v227 offset:256\n"
    "v_cvt_pk_bf16_f32 v141, v106, v107\n"
    "v_mfma_f32_16x16x32_bf16 v[112:115], v[164:167], v[28:31], v[112:115]\n"
    "ds_read_b64_tr_b16 v[158:159], v227 offset:768\n"
    "v_mfma_f32_16x16x32_bf16 v[116:119], v[164:167], v[32:35], v[116:119]\n"
    "v_mfma_f32_16x16x32_bf16 v[108:111], v[168:171], v[36:39], v[108:111]\n"
    "ds_read_b64_tr_b16 v[160:161], v227 offset:288\n"
    "v_mfma_f32_16x16x32_bf16 v[112:115], v[168:171], v[40:43], v[112:115]\n"
    "ds_read_b64_tr_b16 v[162:163], v227 offset:800\n"
    "v_mfma_f32_16x16x32_bf16 v[116:119], v[168:171], v[44:47], v[116:119]\n"
    "s_waitcnt lgkmcnt(4)\n"
    "s_waitcnt vmcnt(16)\n"
    "s_barrier\n"
    "v_mfma_f32_16x16x32_bf16 v[108:111], v[172:175], v[48:51], v[108:111]\n"
    "ds_read_b64_tr_b16 v[164:165], v227 offset:320\n"
    "s_add_u32 m0, s69, s70\n"
    "v_mfma_f32_16x16x32_bf16 v[112:115], v[172:175], v[52:55], v[112:115]\n"
    "ds_read_b64_tr_b16 v[166:167], v227 offset:832\n"
    "s_add_u32 s68, s68, 16896\n"
    "v_mfma_f32_16x16x32_bf16 v[116:119], v[172:175], v[56:59], v[116:119]\n"
    "buffer_load_dwordx4 v232, s[56:59], s73 offen lds\n"
    "s_cmp_eq_u32 s68, 152064\n"
    "s_cselect_b32 s68, 0, s68\n"
    "v_mfma_f32_16x16x32_bf16 v[108:111], v[176:179], v[60:63], v[108:111]\n"
    "ds_read_b64_tr_b16 v[168:169], v227 offset:352\n"
    "v_mfma_f32_16x16x32_bf16 v[112:115], v[176:179], v[64:67], v[112:115]\n"
    "ds_read_b64_tr_b16 v[170:171], v227 offset:864\n"
    "v_mfma_f32_16x16x32_bf16 v[116:119], v[176:179], v[68:71], v[116:119]\n"
    "buffer_load_dwordx4 v233, s[56:59], s73 offen offset:1056 lds\n"
    "v_mfma_f32_16x16x32_bf16 v[108:111], v[180:183], v[72:75], v[108:111]\n"
    "ds_read_b64_tr_b16 v[172:173], v227 offset:384\n"
    "v_mfma_f32_16x16x32_bf16 v[112:115], v[180:183], v[76:79], v[112:115]\n"
    "ds_read_b64_tr_b16 v[174:175], v227 offset:896\n"
    "v_mfma_f32_16x16x32_bf16 v[116:119], v[180:183], v[80:83], v[116:119]\n"
    "buffer_load_dwordx4 v234, s[56:59], s73 offen offset:2112 lds\n"
    "v_mfma_f32_16x16x32_bf16 v[108:111], v[184:187], v[84:87], v[108:111]\n"
    "ds_read_b64_tr_b16 v[176:177], v227 offset:416\n"
    "v_mfma_f32_16x16x32_bf16 v[112:115], v[184:187], v[88:91], v[112:115]\n"
    "ds_read_b64_tr_b16 v[178:179], v227 offset:928\n"
    "v_mfma_f32_16x16x32_bf16 v[116:119], v[184:187], v[92:95], v[116:119]\n"
    "s_waitcnt lgkmcnt(4)\n"
    "buffer_load_dwordx4 v235, s[56:59], s73 offen offset:3168 lds\n"
    "v_mfma_f32_16x16x32_bf16 a[96:99], v[156:159], v[120:123], a[96:99]\n"
    "ds_read_b64_tr_b16 v[180:181], v227 offset:448\n"
    "s_add_u32 s69, s69, 16896\n"
    "v_mfma_f32_16x16x32_bf16 a[104:107], v[156:159], v[124:127], a[104:107]\n"
    "ds_read_b64_tr_b16 v[182:183], v227 offset:960\n"
    "v_mfma_f32_16x16x32_bf16 a[112:115], v[156:159], v[128:131], a[112:115]\n"
    "s_cmp_eq_u32 s69, 152064\n"
    "s_cselect_b32 s69, 0, s69\n"
    "v_mfma_f32_16x16x32_bf16 a[100:103], v[160:163], v[120:123], a[100:103]\n"
    "ds_read_b64_tr_b16 v[184:185], v227 offset:480\n"
    "s_add_u32 s73, s73, s74\n"
    "v_mfma_f32_16x16x32_bf16 a[108:111], v[160:163], v[124:127], a[108:111]\n"
    "ds_read_b64_tr_b16 v[186:187], v227 offset:992\n"
    "v_max3_f32 v96, v108, v109, v110\n"
    "v_mfma_f32_16x16x32_bf16 a[116:119], v[160:163], v[128:131], a[116:119]\n"
    "v_add_u32 v227, s68, v225\n"
    "v_max3_f32 v97, v112, v113, v114\n"
    "v_mfma_f32_16x16x32_bf16 a[120:123], v[164:167], v[120:123], a[120:123]\n"
    "ds_read_b64_tr_b16 v[156:157], v227\n"
    "s_add_u32 s68, s68, 16896\n"
    "v_mfma_f32_16x16x32_bf16 a[128:131], v[164:167], v[124:127], a[128:131]\n"
    "ds_read_b64_tr_b16 v[158:159], v227 offset:512\n"
    "v_max3_f32 v96, v96, v111, v116\n"
    "v_mfma_f32_16x16x32_bf16 a[136:139], v[164:167], v[128:131], a[136:139]\n"
    "s_cmp_eq_u32 s68, 152064\n"
    "s_cselect_b32 s68, 0, s68\n"
    "v_mfma_f32_16x16x32_bf16 a[124:127], v[168:171], v[120:123], a[124:127]\n"
    "ds_read_b64_tr_b16 v[160:161], v227 offset:32\n"
    "v_max3_f32 v97, v97, v115, v117\n"
    "v_mfma_f32_16x16x32_bf16 a[132:135], v[168:171], v[124:127], a[132:135]\n"
    "ds_read_b64_tr_b16 v[162:163], v227 offset:544\n"
    "v_max3_f32 v96, v96, v118, v119\n"
    "v_mfma_f32_16x16x32_bf16 a[140:143], v[168:171], v[128:131], a[140:143]\n"
    "s_waitcnt lgkmcnt(4)\n"
    "v_max_f32 v96, v96, v97\n"
    "v_mfma_f32_16x16x32_bf16 a[144:147], v[172:175], v[120:123], a[144:147]\n"
    "ds_read_b64_tr_b16 v[164:165], v227 offset:64\n"
    "v_cmp_lt_f32 vcc, s88, v96\n"
    "s_nop 0\n"
    "s_cbranch_vccnz LR_s14b\n"
    "LB_s14b:\n"
    "v_mfma_f32_16x16x32_bf16 a[152:155], v[172:175], v[124:127], a[152:155]\n"
    "ds_read_b64_tr_b16 v[166:167], v227 offset:576\n"
    "v_mfma_f32_16x16x32_bf16 a[160:163], v[172:175], v[128:131], a[160:163]\n"
    "v_exp_f32 v108, v108\n"
    "v_mfma_f32_16x16x32_bf16 a[148:151], v[176:179], v[120:123], a[148:151]\n"
    "ds_read_b64_tr_b16 v[168:169], v227 offset:96\n"
    "v_mfma_f32_16x16x32_bf16 a[156:159], v[176:179], v[124:127], a[156:159]\n"
    "ds_read_b64_tr_b16 v[170:171], v227 offset:608\n"
    "v_mfma_f32_16x16x32_bf16 a[164:167], v[176:179], v[128:131], a[164:167]\n"
    "v_exp_f32 v109, v109\n"
    "v_mfma_f32_16x16x32_bf16 a[168:171], v[180:183], v[120:123], a[168:171]\n"
    "ds_read_b64_tr_b16 v[172:173], v227 offset:128\n"
    "v_cvt_pk_bf16_f32 v134, v108, v109\n"
    "v_mfma_f32_16x16x32_bf16 a[176:179], v[180:183], v[124:127], a[176:179]\n"
    "ds_read_b64_tr_b16 v[174:175], v227 offset:640\n"
    "v_mfma_f32_16x16x32_bf16 a[184:187], v[180:183], v[128:131], a[184:187]\n"
    "v_exp_f32 v110, v110\n"
    "v_mfma_f32_16x16x32_bf16 a[172:175], v[184:187], v[120:123], a[172:175]\n"
    "ds_read_b64_tr_b16 v[176:177], v227 offset:160\n"
    "v_mfma_f32_16x16x32_bf16 a[180:183], v[184:187], v[124:127], a[180:183]\n"
    "ds_read_b64_tr_b16 v[178:179], v227 offset:672\n"
    "v_mfma_f32_16x16x32_bf16 a[188:191], v[184:187], v[128:131], a[188:191]\n"
    "s_waitcnt lgkmcnt(4)\n"
    "v_exp_f32 v111, v111\n"
    "s_nop 0\n"
    "v_cvt_pk_bf16_f32 v135, v110, v111\n"
    "v_exp_f32 v112, v112\n"
    "v_exp_f32 v113, v113\n"
    "v_exp_f32 v114, v114\n"
    "v_exp_f32 v115, v115\n"
    "v_exp_f32 v116, v116\n"
    "v_exp_f32 v117, v117\n"
    "v_exp_f32 v118, v118\n"
    "v_exp_f32 v119, v119\n"
    "v_cvt_pk_bf16_f32 v138, v112, v113\n"
    "v_cvt_pk_bf16_f32 v139, v114, v115\n"
    "v_cvt_pk_bf16_f32 v142, v116, v117\n"
    "v_cvt_pk_bf16_f32 v143, v118, v119\n"
    "v_mfma_f32_16x16x32_bf16 a[0:3], v[156:159], v[132:135], a[0:3]\n"
    "ds_read_b64_tr_b16 v[180:181], v227 offset:192\n"
    "v_mfma_f32_16x16x32_bf16 a[8:11], v[156:159], v[136:139], a[8:11]\n"
    "ds_read_b64_tr_b16 v[182:183], v227 offset:704\n"
    "v_mfma_f32_16x16x32_bf16 a[16:19], v[156:159], v[140:143], a[16:19]\n"
    "v_mfma_f32_16x16x32_bf16 a[4:7], v[160:163], v[132:135], a[4:7]\n"
    "ds_read_b64_tr_b16 v[184:185], v227 offset:224\n"
    "v_mfma_f32_16x16x32_bf16 a[12:15], v[160:163], v[136:139], a[12:15]\n"
    "ds_read_b64_tr_b16 v[186:187], v227 offset:736\n"
    "v_mfma_f32_16x16x32_bf16 a[20:23], v[160:163], v[140:143], a[20:23]\n"
    "v_mfma_f32_16x16x32_bf16 a[24:27], v[164:167], v[132:135], a[24:27]\n"
    "ds_read_b64_tr_b16 v[156:157], v227 offset:256\n"
    "v_mfma_f32_16x16x32_bf16 a[32:35], v[164:167], v[136:139], a[32:35]\n"
    "ds_read_b64_tr_b16 v[158:159], v227 offset:768\n"
    "v_mfma_f32_16x16x32_bf16 a[40:43], v[164:167], v[140:143], a[40:43]\n"
    "v_mfma_f32_16x16x32_bf16 a[28:31], v[168:171], v[132:135], a[28:31]\n"
    "ds_read_b64_tr_b16 v[160:161], v227 offset:288\n"
    "v_mfma_f32_16x16x32_bf16 a[36:39], v[168:171], v[136:139], a[36:39]\n"
    "ds_read_b64_tr_b16 v[162:163], v227 offset:800\n"
    "v_mfma_f32_16x16x32_bf16 a[44:47], v[168:171], v[140:143], a[44:47]\n"
    "s_waitcnt lgkmcnt(4)\n"
    "v_mfma_f32_16x16x32_bf16 a[48:51], v[172:175], v[132:135], a[48:51]\n"
    "ds_read_b64_tr_b16 v[164:165], v227 offset:320\n"
    "v_mfma_f32_16x16x32_bf16 a[56:59], v[172:175], v[136:139], a[56:59]\n"
    "ds_read_b64_tr_b16 v[166:167], v227 offset:832\n"
    "v_mfma_f32_16x16x32_bf16 a[64:67], v[172:175], v[140:143], a[64:67]\n"
    "v_mfma_f32_16x16x32_bf16 a[52:55], v[176:179], v[132:135], a[52:55]\n"
    "ds_read_b64_tr_b16 v[168:169], v227 offset:352\n"
    "v_mfma_f32_16x16x32_bf16 a[60:63], v[176:179], v[136:139], a[60:63]\n"
    "ds_read_b64_tr_b16 v[170:171], v227 offset:864\n"
    "v_mfma_f32_16x16x32_bf16 a[68:71], v[176:179], v[140:143], a[68:71]\n"
    "v_mfma_f32_16x16x32_bf16 a[72:75], v[180:183], v[132:135], a[72:75]\n"
    "ds_read_b64_tr_b16 v[172:173], v227 offset:384\n"
    "v_mfma_f32_16x16x32_bf16 a[80:83], v[180:183], v[136:139], a[80:83]\n"
    "ds_read_b64_tr_b16 v[174:175], v227 offset:896\n"
    "v_mfma_f32_16x16x32_bf16 a[88:91], v[180:183], v[140:143], a[88:91]\n"
    "v_mfma_f32_16x16x32_bf16 a[76:79], v[184:187], v[132:135], a[76:79]\n"
    "ds_read_b64_tr_b16 v[176:177], v227 offset:416\n"
    "v_mfma_f32_16x16x32_bf16 a[84:87], v[184:187], v[136:139], a[84:87]\n"
    "ds_read_b64_tr_b16 v[178:179], v227 offset:928\n"
    "v_mfma_f32_16x16x32_bf16 a[92:95], v[184:187], v[140:143], a[92:95]\n"
    "s_waitcnt lgkmcnt(4)\n"
    "v_mfma_f32_16x16x32_bf16 a[96:99], v[156:159], v[132:135], a[96:99]\n"
    "ds_read_b64_tr_b16 v[180:181], v227 offset:448\n"
    "v_mfma_f32_16x16x32_bf16 a[104:107], v[156:159], v[136:139], a[104:107]\n"
    "ds_read_b64_tr_b16 v[182:183], v227 offset:960\n"
    "v_mfma_f32_16x16x32_bf16 a[112:115], v[156:159], v[140:143], a[112:115]\n"
    "v_mfma_f32_16x16x32_bf16 a[100:103], v[160:163], v[132:135], a[100:103]\n"
    "ds_read_b64_tr_b16 v[184:185], v227 offset:480\n"
    "v_mfma_f32_16x16x32_bf16 a[108:111], v[160:163], v[136:139], a[108:111]\n"
    "ds_read_b64_tr_b16 v[186:187], v227 offset:992\n"
    "v_mfma_f32_16x16x32_bf16 a[116:119], v[160:163], v[140:143], a[116:119]\n"
    "v_mfma_f32_16x16x32_bf16 a[120:123], v[164:167], v[132:135], a[120:123]\n"
    "v_mfma_f32_16x16x32_bf16 a[128:131], v[164:167], v[136:139], a[128:131]\n"
    "v_mfma_f32_16x16x32_bf16 a[136:139], v[164:167], v[140:143], a[136:139]\n"
    "v_mfma_f32_16x16x32_bf16 a[124:127], v[168:171], v[132:135], a[124:127]\n"
    "v_mfma_f32_16x16x32_bf16 a[132:135], v[168:171], v[136:139], a[132:135]\n"
    "v_mfma_f32_16x16x32_bf16 a[140:143], v[168:171], v[140:143], a[140:143]\n"
    "s_waitcnt lgkmcnt(0)\n"
    "v_mfma_f32_16x16x32_bf16 a[144:147], v[172:175], v[132:135], a[144:147]\n"
    "v_mfma_f32_16x16x32_bf16 a[152:155], v[172:175], v[136:139], a[152:155]\n"
    "v_mfma_f32_16x16x32_bf16 a[160:163], v[172:175], v[140:143], a[160:163]\n"
    "v_mfma_f32_16x16x32_bf16 a[148:151], v[176:179], v[132:135], a[148:151]\n"
    "v_mfma_f32_16x16x32_bf16 a[156:159], v[176:179], v[136:139], a[156:159]\n"
    "v_mfma_f32_16x16x32_bf16 a[164:167], v[176:179], v[140:143], a[164:167]\n"
    "v_mfma_f32_16x16x32_bf16 a[168:171], v[180:183], v[132:135], a[168:171]\n"
    "v_mfma_f32_16x16x32_bf16 a[176:179], v[180:183], v[136:139], a[176:179]\n"
    "v_mfma_f32_16x16x32_bf16 a[184:187], v[180:183], v[140:143], a[184:187]\n"
    "v_mfma_f32_16x16x32_bf16 a[172:175], v[184:187], v[132:135], a[172:175]\n"
    "v_mfma_f32_16x16x32_bf16 a[180:183], v[184:187], v[136:139], a[180:183]\n"
    "v_mfma_f32_16x16x32_bf16 a[188:191], v[184:187], v[140:143], a[188:191]\n"
    "v_mfma_f32_16x16x32_bf16 v[240:243], v[188:191], v[132:135], v[240:243]\n"
    "v_mfma_f32_16x16x32_bf16 v[244:247], v[188:191], v[136:139], v[244:247]\n"
    "v_mfma_f32_16x16x32_bf16 v[248:251], v[188:191], v[140:143], v[248:251]\n"
    "s_barrier\n"
    "s_nop 4\n"
    "v_rcp_f32 v132, v240\n"
    "v_rcp_f32 v134, v244\n"
    "v_rcp_f32 v136, v248\n"
    "v_mov_b32 v133, v132\n"
    "v_mov_b32 v135, v134\n"
    "v_mov_b32 v137, v136\n"
    "s_cmp_eq_u32 s67, 0\n"
    "s_cbranch_scc0 LCO1\n"
    "s_mov_b32 s40, s68\n"
    "s_add_u32 s41, s40, 16896\n"
    "s_cmp_eq_u32 s41, 152064\n"
    "s_cselect_b32 s41, 0, s41\n"
    "s_add_u32 s42, s41, 16896\n"
    "s_cmp_eq_u32 s42, 152064\n"
    "s_cselect_b32 s42, 0, s42\n"
    "s_add_u32 s43, s42, 16896\n"
    "s_cmp_eq_u32 s43, 152064\n"
    "s_cselect_b32 s43, 0, s43\n"
    "s_add_u32 s52, s43, 16896\n"
    "s_cmp_eq_u32 s52, 152064\n"
    "s_cselect_b32 s52, 0, s52\n"
    "s_add_u32 s53, s52, 16896\n"
    "s_cmp_eq_u32 s53, 152064\n"
    "s_cselect_b32 s53, 0, s53\n"
    "s_add_u32 s54, s53, 16896\n"
    "s_cmp_eq_u32 s54, 152064\n"
    "s_cselect_b32 s54, 0, s54\n"
    "s_add_u32 s55, s54, 16896\n"
    "s_cmp_eq_u32 s55, 152064\n"
    "s_cselect_b32 s55, 0, s55\n"
    "s_add_u32 s33, s55, 16896\n"
    "s_cmp_eq_u32 s33, 152064\n"
    "s_cselect_b32 s33, 0, s33\n"
    "v_accvgpr_read_b32 v144, a0\n"
    "v_accvgpr_read_b32 v145, a1\n"
    "v_accvgpr_read_b32 v146, a2\n"
    "v_accvgpr_read_b32 v147, a3\n"
    "v_accvgpr_read_b32 v148, a4\n"
    "v_accvgpr_read_b32 v149, a5\n"
    "v_accvgpr_read_b32 v150, a6\n"
    "v_accvgpr_read_b32 v151, a7\n"
    "s_add_u32 s34, s33, s70\n"
    "s_add_u32 m0, s34, 0\n"
    "s_add_u32 s35, s76, 0\n"
    "buffer_load_dwordx4 v239, s[44:47], s35 offen lds\n"
    "v_accvgpr_read_b32 v152, a8\n"
    "v_accvgpr_read_b32 v153, a9\n"
    "v_accvgpr_read_b32 v154, a10\n"
    "v_accvgpr_read_b32 v155, a11\n"
    "v_accvgpr_read_b32 v156, a12\n"
    "v_accvgpr_read_b32 v157, a13\n"
    "v_accvgpr_read_b32 v158, a14\n"
    "v_accvgpr_read_b32 v159, a15\n"
    "v_pk_mul_f32 v[144:145], v[144:145], v[132:133]\n"
    "v_pk_mul_f32 v[146:147], v[146:147], v[132:133]\n"
    "v_pk_mul_f32 v[148:149], v[148:149], v[132:133]\n"
    "v_pk_mul_f32 v[150:151], v[150:151], v[132:133]\n"
    "s_add_u32 m0, s34, 1056\n"
    "s_add_u32 s35, s77, 0\n"
    "buffer_load_dwordx4 v239, s[44:47], s35 offen lds\n"
    "v_accvgpr_read_b32 v160, a16\n"
    "v_accvgpr_read_b32 v161, a17\n"
    "v_accvgpr_read_b32 v162, a18\n"
    "v_accvgpr_read_b32 v163, a19\n"
    "v_accvgpr_read_b32 v164, a20\n"
    "v_accvgpr_read_b32 v165, a21\n"
    "v_accvgpr_read_b32 v166, a22\n"
    "v_accvgpr_read_b32 v167, a23\n"
    "v_pk_mul_f32 v[152:153], v[152:153], v[134:135]\n"
    "v_pk_mul_f32 v[154:155], v[154:155], v[134:135]\n"
    "v_pk_mul_f32 v[156:157], v[156:157], v[134:135]\n"
    "v_pk_mul_f32 v[158:159], v[158:159], v[134:135]\n"
    "v_cvt_pk_bf16_f32 v176, v144, v145\n"
    "v_cvt_pk_bf16_f32 v177, v146, v147\n"
    "v_cvt_pk_bf16_f32 v178, v148, v149\n"
    "v_cvt_pk_bf16_f32 v179, v150, v151\n"
    "s_add_u32 m0, s34, 2112\n"
    "s_add_u32 s35, s78, 0\n"
    "buffer_load_dwordx4 v239, s[44:47], s35 offen lds\n"
    "v_accvgpr_read_b32 v168, a24\n"
    "v_accvgpr_read_b32 v169, a25\n"
    "v_accvgpr_read_b32 v170, a26\n"
    "v_accvgpr_read_b32 v171, a27\n"
    "v_accvgpr_read_b32 v172, a28\n"
    "v_accvgpr_read_b32 v173, a29\n"
    "v_accvgpr_read_b32 v174, a30\n"
    "v_accvgpr_read_b32 v175, a31\n"
    "v_pk_mul_f32 v[160:161], v[160:161], v[136:137]\n"
    "v_pk_mul_f32 v[162:163], v[162:163], v[136:137]\n"
    "v_pk_mul_f32 v[164:165], v[164:165], v[136:137]\n"
    "v_pk_mul_f32 v[166:167], v[166:167], v[136:137]\n"
    "v_cvt_pk_bf16_f32 v180, v152, v153\n"
    "v_cvt_pk_bf16_f32 v181, v154, v155\n"
    "v_cvt_pk_bf16_f32 v182, v156, v157\n"
    "v_cvt_pk_bf16_f32 v183, v158, v159\n"
    "v_accvgpr_write_b32 a192, v176\n"
    "v_accvgpr_write_b32 a193, v177\n"
    "v_accvgpr_write_b32 a194, v178\n"
    "v_accvgpr_write_b32 a195, v179\n"
    "s_add_u32 m0, s34, 3168\n"
    "s_add_u32 s35, s76, 64\n"
    "buffer_load_dwordx4 v239, s[44:47], s35 offen lds\n"
    "v_accvgpr_read_b32 v144, a32\n"
    "v_accvgpr_read_b32 v145, a33\n"
    "v_accvgpr_read_b32 v146, a34\n"
    "v_accvgpr_read_b32 v147, a35\n"
    "v_accvgpr_read_b32 v148, a36\n"
    "v_accvgpr_read_b32 v149, a37\n"
    "v_accvgpr_read_b32 v150, a38\n"
    "v_accvgpr_read_b32 v151, a39\n"
    "v_pk_mul_f32 v[168:169], v[168:169], v[132:133]\n"
    "v_pk_mul_f32 v[170:171], v[170:171], v[132:133]\n"
    "v_pk_mul_f32 v[172:173], v[172:173], v[132:133]\n"
    "v_pk_mul_f32 v[174:175], v[174:175], v[132:133]\n"
    "v_cvt_pk_bf16_f32 v184, v160, v161\n"
    "v_cvt_pk_bf16_f32 v185, v162, v163\n"
    "v_cvt_pk_bf16_f32 v186, v164, v165\n"
    "v_cvt_pk_bf16_f32 v187, v166, v167\n"
    "v_accvgpr_write_b32 a196, v180\n"
    "v_accvgpr_write_b32 a197, v181\n"
    "v_accvgpr_write_b32 a198, v182\n"
    "v_accvgpr_write_b32 a199, v183\n"
    "s_add_u32 s34, s54, s70\n"
    "s_add_u32 m0, s34, 0\n"
    "s_add_u32 s35, s77, 64\n"
    "buffer_load_dwordx4 v239, s[44:47], s35 offen lds\n"
    "v_accvgpr_read_b32 v152, a40\n"
    "v_accvgpr_read_b32 v153, a41\n"
    "v_accvgpr_read_b32 v154, a42\n"
    "v_accvgpr_read_b32 v155, a43\n"
    "v_accvgpr_read_b32 v156, a44\n"
    "v_accvgpr_read_b32 v157, a45\n"
    "v_accvgpr_read_b32 v158, a46\n"
    "v_accvgpr_read_b32 v159, a47\n"
    "v_pk_mul_f32 v[144:145], v[144:145], v[134:135]\n"
    "v_pk_mul_f32 v[146:147], v[146:147], v[134:135]\n"
    "v_pk_mul_f32 v[148:149], v[148:149], v[134:135]\n"
    "v_pk_mul_f32 v[150:151], v[150:151], v[134:135]\n"
    "v_cvt_pk_bf16_f32 v176, v168, v169\n"
    "v_cvt_pk_bf16_f32 v177, v170, v171\n"
    "v_cvt_pk_bf16_f32 v178, v172, v173\n"
    "v_cvt_pk_bf16_f32 v179, v174, v175\n"
    "v_accvgpr_write_b32 a200, v184\n"
    "v_accvgpr_write_b32 a201, v185\n"
    "v_accvgpr_write_b32 a202, v186\n"
    "v_accvgpr_write_b32 a203, v187\n"
    "s_add_u32 m0, s34, 1056\n"
    "s_add_u32 s35, s78, 64\n"
    "buffer_load_dwordx4 v239, s[44:47], s35 offen lds\n"
    "v_accvgpr_read_b32 v160, a48\n"
    "v_accvgpr_read_b32 v161, a49\n"
    "v_accvgpr_read_b32 v162, a50\n"
    "v_accvgpr_read_b32 v163, a51\n"
    "v_accvgpr_read_b32 v164, a52\n"
    "v_accvgpr_read_b32 v165, a53\n"
    "v_accvgpr_read_b32 v166, a54\n"
    "v_accvgpr_read_b32 v167, a55\n"
    "v_pk_mul_f32 v[152:153], v[152:153], v[136:137]\n"
    "v_pk_mul_f32 v[154:155], v[154:155], v[136:137]\n"
    "v_pk_mul_f32 v[156:157], v[156:157], v[136:137]\n"
    "v_pk_mul_f32 v[158:159], v[158:159], v[136:137]\n"
    "v_cvt_pk_bf16_f32 v180, v144, v145\n"
    "v_cvt_pk_bf16_f32 v181, v146, v147\n"
    "v_cvt_pk_bf16_f32 v182, v148, v149\n"
    "v_cvt_pk_bf16_f32 v183, v150, v151\n"
    "v_accvgpr_write_b32 a204, v176\n"
    "v_accvgpr_write_b32 a205, v177\n"
    "v_accvgpr_write_b32 a206, v178\n"
    "v_accvgpr_write_b32 a207, v179\n"
    "s_add_u32 m0, s34, 2112\n"
    "s_add_u32 s35, s76, 128\n"
    "buffer_load_dwordx4 v239, s[44:47], s35 offen lds\n"
    "v_accvgpr_read_b32 v168, a56\n"
    "v_accvgpr_read_b32 v169, a57\n"
    "v_accvgpr_read_b32 v170, a58\n"
    "v_accvgpr_read_b32 v171, a59\n"
    "v_accvgpr_read_b32 v172, a60\n"
    "v_accvgpr_read_b32 v173, a61\n"
    "v_accvgpr_read_b32 v174, a62\n"
    "v_accvgpr_read_b32 v175, a63\n"
    "v_pk_mul_f32 v[160:161], v[160:161], v[132:133]\n"
    "v_pk_mul_f32 v[162:163], v[162:163], v[132:133]\n"
    "v_pk_mul_f32 v[164:165], v[164:165], v[132:133]\n"
    "v_pk_mul_f32 v[166:167], v[166:167], v[132:133]\n"
    "v_cvt_pk_bf16_f32 v184, v152, v153\n"
    "v_cvt_pk_bf16_f32 v185, v154, v155\n"
    "v_cvt_pk_bf16_f32 v186, v156, v157\n"
    "v_cvt_pk_bf16_f32 v187, v158, v159\n"
    "v_accvgpr_write_b32 a208, v180\n"
    "v_accvgpr_write_b32 a209, v181\n"
    "v_accvgpr_write_b32 a210, v182\n"
    "v_accvgpr_write_b32 a211, v183\n"
    "s_add_u32 m0, s34, 3168\n"
    "s_add_u32 s35, s77, 128\n"
    "buffer_load_dwordx4 v239, s[44:47], s35 offen lds\n"
    "v_accvgpr_read_b32 v144, a64\n"
    "v_accvgpr_read_b32 v145, a65\n"
    "v_accvgpr_read_b32 v146, a66\n"
    "v_accvgpr_read_b32 v147, a67\n"
    "v_accvgpr_read_b32 v148, a68\n"
    "v_accvgpr_read_b32 v149, a69\n"
    "v_accvgpr_read_b32 v150, a70\n"
    "v_accvgpr_read_b32 v151, a71\n"
    "v_pk_mul_f32 v[168:169], v[168:169], v[134:135]\n"
    "v_pk_mul_f32 v[170:171], v[170:171], v[134:135]\n"
    "v_pk_mul_f32 v[172:173], v[172:173], v[134:135]\n"
    "v_pk_mul_f32 v[174:175], v[174:175], v[134:135]\n"
    "v_cvt_pk_bf16_f32 v176, v160, v161\n"
    "v_cvt_pk_bf16_f32 v177, v162, v163\n"
    "v_cvt_pk_bf16_f32 v178, v164, v165\n"
    "v_cvt_pk_bf16_f32 v179, v166, v167\n"
    "v_accvgpr_write_b32 a212, v184\n"
    "v_accvgpr_write_b32 a213, v185\n"
    "v_accvgpr_write_b32 a214, v186\n"
    "v_accvgpr_write_b32 a215, v187\n"
    "s_add_u32 s34, s53, s70\n"
    "s_add_u32 m0, s34, 0\n"
    "s_add_u32 s35, s78, 128\n"
    "buffer_load_dwordx4 v239, s[44:47], s35 offen lds\n"
    "v_accvgpr_read_b32 v152, a72\n"
    "v_accvgpr_read_b32 v153, a73\n"
    "v_accvgpr_read_b32 v154, a74\n"
    "v_accvgpr_read_b32 v155, a75\n"
    "v_accvgpr_read_b32 v156, a76\n"
    "v_accvgpr_read_b32 v157, a77\n"
    "v_accvgpr_read_b32 v158, a78\n"
    "v_accvgpr_read_b32 v159, a79\n"
    "v_pk_mul_f32 v[144:145], v[144:145], v[136:137]\n"
    "v_pk_mul_f32 v[146:147], v[146:147], v[136:137]\n"
    "v_pk_mul_f32 v[148:149], v[148:149], v[136:137]\n"
    "v_pk_mul_f32 v[150:151], v[150:151], v[136:137]\n"
    "v_cvt_pk_bf16_f32 v180, v168, v169\n"
    "v_cvt_pk_bf16_f32 v181, v170, v171\n"
    "v_cvt_pk_bf16_f32 v182, v172, v173\n"
    "v_cvt_pk_bf16_f32 v183, v174, v175\n"
    "v_accvgpr_write_b32 a216, v176\n"
    "v_accvgpr_write_b32 a217, v177\n"
    "v_accvgpr_write_b32 a218, v178\n"
    "v_accvgpr_write_b32 a219, v179\n"
    "s_add_u32 m0, s34, 1056\n"
    "s_add_u32 s35, s76, 192\n"
    "buffer_load_dwordx4 v239, s[44:47], s35 offen lds\n"
    "v_accvgpr_read_b32 v160, a80\n"
    "v_accvgpr_read_b32 v161, a81\n"
    "v_accvgpr_read_b32 v162, a82\n"
    "v_accvgpr_read_b32 v163, a83\n"
    "v_accvgpr_read_b32 v164, a84\n"
    "v_accvgpr_read_b32 v165, a85\n"
    "v_accvgpr_read_b32 v166, a86\n"
    "v_accvgpr_read_b32 v167, a87\n"
    "v_pk_mul_f32 v[152:153], v[152:153], v[132:133]\n"
    "v_pk_mul_f32 v[154:155], v[154:155], v[132:133]\n"
    "v_pk_mul_f32 v[156:157], v[156:157], v[132:133]\n"
    "v_pk_mul_f32 v[158:159], v[158:159], v[132:133]\n"
    "v_cvt_pk_bf16_f32 v184, v144, v145\n"
    "v_cvt_pk_bf16_f32 v185, v146, v147\n"
    "v_cvt_pk_bf16_f32 v186, v148, v149\n"
    "v_cvt_pk_bf16_f32 v187, v150, v151\n"
    "v_accvgpr_write_b32 a220, v180\n"
    "v_accvgpr_write_b32 a221, v181\n"
    "v_accvgpr_write_b32 a222, v182\n"
    "v_accvgpr_write_b32 a223, v183\n"
    "s_add_u32 m0, s34, 2112\n"
    "s_add_u32 s35, s77, 192\n"
    "buffer_load_dwordx4 v239, s[44:47], s35 offen lds\n"
    "v_accvgpr_read_b32 v168, a88\n"
    "v_accvgpr_read_b32 v169, a89\n"
    "v_accvgpr_read_b32 v170, a90\n"
    "v_accvgpr_read_b32 v171, a91\n"
    "v_accvgpr_read_b32 v172, a92\n"
    "v_accvgpr_read_b32 v173, a93\n"
    "v_accvgpr_read_b32 v174, a94\n"
    "v_accvgpr_read_b32 v175, a95\n"
    "v_pk_mul_f32 v[160:161], v[160:161], v[134:135]\n"
    "v_pk_mul_f32 v[162:163], v[162:163], v[134:135]\n"
    "v_pk_mul_f32 v[164:165], v[164:165], v[134:135]\n"
    "v_pk_mul_f32 v[166:167], v[166:167], v[134:135]\n"
    "v_cvt_pk_bf16_f32 v176, v152, v153\n"
    "v_cvt_pk_bf16_f32 v177, v154, v155\n"
    "v_cvt_pk_bf16_f32 v178, v156, v157\n"
    "v_cvt_pk_bf16_f32 v179, v158, v159\n"
    "v_accvgpr_write_b32 a224, v184\n"
    "v_accvgpr_write_b32 a225, v185\n"
    "v_accvgpr_write_b32 a226, v186\n"
    "v_accvgpr_write_b32 a227, v187\n"
    "s_add_u32 m0, s34, 3168\n"
    "s_add_u32 s35, s78, 192\n"
    "buffer_load_dwordx4 v239, s[44:47], s35 offen lds\n"
    "v_accvgpr_read_b32 v144, a96\n"
    "v_accvgpr_read_b32 v145, a97\n"
    "v_accvgpr_read_b32 v146, a98\n"
    "v_accvgpr_read_b32 v147, a99\n"
    "v_accvgpr_read_b32 v148, a100\n"
    "v_accvgpr_read_b32 v149, a101\n"
    "v_accvgpr_read_b32 v150, a102\n"
    "v_accvgpr_read_b32 v151, a103\n"
    "v_pk_mul_f32 v[168:169], v[168:169], v[136:137]\n"
    "v_pk_mul_f32 v[170:171], v[170:171], v[136:137]\n"
    "v_pk_mul_f32 v[172:173], v[172:173], v[136:137]\n"
    "v_pk_mul_f32 v[174:175], v[174:175], v[136:137]\n"
    "v_cvt_pk_bf16_f32 v180, v160, v161\n"
    "v_cvt_pk_bf16_f32 v181, v162, v163\n"
    "v_cvt_pk_bf16_f32 v182, v164, v165\n"
    "v_cvt_pk_bf16_f32 v183, v166, v167\n"
    "v_accvgpr_write_b32 a228, v176\n"
    "v_accvgpr_write_b32 a229, v177\n"
    "v_accvgpr_write_b32 a230, v178\n"
    "v_accvgpr_write_b32 a231, v179\n"
    "s_add_u32 s34, s55, s70\n"
    "s_add_u32 m0, s34, 0\n"
    "s_add_u32 s35, s76, 256\n"
    "buffer_load_dwordx4 v239, s[44:47], s35 offen lds\n"
    "v_accvgpr_read_b32 v152, a104\n"
    "v_accvgpr_read_b32 v153, a105\n"
    "v_accvgpr_read_b32 v154, a106\n"
    "v_accvgpr_read_b32 v155, a107\n"
    "v_accvgpr_read_b32 v156, a108\n"
    "v_accvgpr_read_b32 v157, a109\n"
    "v_accvgpr_read_b32 v158, a110\n"
    "v_accvgpr_read_b32 v159, a111\n"
    "v_pk_mul_f32 v[144:145], v[144:145], v[132:133]\n"
    "v_pk_mul_f32 v[146:147], v[146:147], v[132:133]\n"
    "v_pk_mul_f32 v[148:149], v[148:149], v[132:133]\n"
    "v_pk_mul_f32 v[150:151], v[150:151], v[132:133]\n"
    "v_cvt_pk_bf16_f32 v184, v168, v169\n"
    "v_cvt_pk_bf16_f32 v185, v170, v171\n"
    "v_cvt_pk_bf16_f32 v186, v172, v173\n"
    "v_cvt_pk_bf16_f32 v187, v174, v175\n"
    "v_accvgpr_write_b32 a232, v180\n"
    "v_accvgpr_write_b32 a233, v181\n"
    "v_accvgpr_write_b32 a234, v182\n"
    "v_accvgpr_write_b32 a235, v183\n"
    "s_add_u32 m0, s34, 1056\n"
    "s_add_u32 s35, s77, 256\n"
    "buffer_load_dwordx4 v239, s[44:47], s35 offen lds\n"
    "v_accvgpr_read_b32 v160, a112\n"
    "v_accvgpr_read_b32 v161, a113\n"
    "v_accvgpr_read_b32 v162, a114\n"
    "v_accvgpr_read_b32 v163, a115\n"
    "v_accvgpr_read_b32 v164, a116\n"
    "v_accvgpr_read_b32 v165, a117\n"
    "v_accvgpr_read_b32 v166, a118\n"
    "v_accvgpr_read_b32 v167, a119\n"
    "v_pk_mul_f32 v[152:153], v[152:153], v[134:135]\n"
    "v_pk_mul_f32 v[154:155], v[154:155], v[134:135]\n"
    "v_pk_mul_f32 v[156:157], v[156:157], v[134:135]\n"
    "v_pk_mul_f32 v[158:159], v[158:159], v[134:135]\n"
    "v_cvt_pk_bf16_f32 v176, v144, v145\n"
    "v_cvt_pk_bf16_f32 v177, v146, v147\n"
    "v_cvt_pk_bf16_f32 v178, v148, v149\n"
    "v_cvt_pk_bf16_f32 v179, v150, v151\n"
    "v_accvgpr_write_b32 a236, v184\n"
    "v_accvgpr_write_b32 a237, v185\n"
    "v_accvgpr_write_b32 a238, v186\n"
    "v_accvgpr_write_b32 a239, v187\n"
    "s_add_u32 m0, s34, 2112\n"
    "s_add_u32 s35, s78, 256\n"
    "buffer_load_dwordx4 v239, s[44:47], s35 offen lds\n"
    "v_accvgpr_read_b32 v168, a120\n"
    "v_accvgpr_read_b32 v169, a121\n"
    "v_accvgpr_read_b32 v170, a122\n"
    "v_accvgpr_read_b32 v171, a123\n"
    "v_accvgpr_read_b32 v172, a124\n"
    "v_accvgpr_read_b32 v173, a125\n"
    "v_accvgpr_read_b32 v174, a126\n"
    "v_accvgpr_read_b32 v175, a127\n"
    "v_pk_mul_f32 v[160:161], v[160:161], v[136:137]\n"
    "v_pk_mul_f32 v[162:163], v[162:163], v[136:137]\n"
    "v_pk_mul_f32 v[164:165], v[164:165], v[136:137]\n"
    "v_pk_mul_f32 v[166:167], v[166:167], v[136:137]\n"
    "v_cvt_pk_bf16_f32 v180, v152, v153\n"
    "v_cvt_pk_bf16_f32 v181, v154, v155\n"
    "v_cvt_pk_bf16_f32 v182, v156, v157\n"
    "v_cvt_pk_bf16_f32 v183, v158, v159\n"
    "v_accvgpr_write_b32 a240, v176\n"
    "v_accvgpr_write_b32 a241, v177\n"
    "v_accvgpr_write_b32 a242, v178\n"
    "v_accvgpr_write_b32 a243, v179\n"
    "s_add_u32 m0, s34, 3168\n"
    "s_add_u32 s35, s76, 320\n"
    "buffer_load_dwordx4 v239, s[44:47], s35 offen lds\n"
    "v_accvgpr_read_b32 v144, a128\n"
    "v_accvgpr_read_b32 v145, a129\n"
    "v_accvgpr_read_b32 v146, a130\n"
    "v_accvgpr_read_b32 v147, a131\n"
    "v_accvgpr_read_b32 v148, a132\n"
    "v_accvgpr_read_b32 v149, a133\n"
    "v_accvgpr_read_b32 v150, a134\n"
    "v_accvgpr_read_b32 v151, a135\n"
    "v_pk_mul_f32 v[168:169], v[168:169], v[132:133]\n"
    "v_pk_mul_f32 v[170:171], v[170:171], v[132:133]\n"
    "v_pk_mul_f32 v[172:173], v[172:173], v[132:133]\n"
    "v_pk_mul_f32 v[174:175], v[174:175], v[132:133]\n"
    "v_cvt_pk_bf16_f32 v184, v160, v161\n"
    "v_cvt_pk_bf16_f32 v185, v162, v163\n"
    "v_cvt_pk_bf16_f32 v186, v164, v165\n"
    "v_cvt_pk_bf16_f32 v187, v166, v167\n"
    "v_accvgpr_write_b32 a244, v180\n"
    "v_accvgpr_write_b32 a245, v181\n"
    "v_accvgpr_write_b32 a246, v182\n"
    "v_accvgpr_write_b32 a247, v183\n"
    "s_lshl_b32 s34, s64, 11\n"
    "s_add_u32 s34, s34, 153088\n"
    "s_add_u32 s34, s34, %[ldsb]\n"
    "s_add_u32 m0, s34, 0\n"
    "s_add_u32 s35, s77, 832\n"
    "buffer_load_dwordx4 v239, s[44:47], s35 offen lds\n"
    "v_accvgpr_read_b32 v152, a136\n"
    "v_accvgpr_read_b32 v153, a137\n"
    "v_accvgpr_read_b32 v154, a138\n"
    "v_accvgpr_read_b32 v155, a139\n"
    "v_accvgpr_read_b32 v156, a140\n"
    "v_accvgpr_read_b32 v157, a141\n"
    "v_accvgpr_read_b32 v158, a142\n"
    "v_accvgpr_read_b32 v159, a143\n"
    "v_pk_mul_f32 v[144:145], v[144:145], v[134:135]\n"
    "v_pk_mul_f32 v[146:147], v[146:147], v[134:135]\n"
    "v_pk_mul_f32 v[148:149], v[148:149], v[134:135]\n"
    "v_pk_mul_f32 v[150:151], v[150:151], v[134:135]\n"
    "v_cvt_pk_bf16_f32 v176, v168, v169\n"
    "v_cvt_pk_bf16_f32 v177, v170, v171\n"
    "v_cvt_pk_bf16_f32 v178, v172, v173\n"
    "v_cvt_pk_bf16_f32 v179, v174, v175\n"
    "v_accvgpr_write_b32 a248, v184\n"
    "v_accvgpr_write_b32 a249, v185\n"
    "v_accvgpr_write_b32 a250, v186\n"
    "v_accvgpr_write_b32 a251, v187\n"
    "s_add_u32 m0, s34, 1024\n"
    "s_add_u32 s35, s78, 832\n"
    "buffer_load_dwordx4 v239, s[44:47], s35 offen lds\n"
    "v_accvgpr_read_b32 v160, a144\n"
    "v_accvgpr_read_b32 v161, a145\n"
    "v_accvgpr_read_b32 v162, a146\n"
    "v_accvgpr_read_b32 v163, a147\n"
    "v_accvgpr_read_b32 v164, a148\n"
    "v_accvgpr_read_b32 v165, a149\n"
    "v_accvgpr_read_b32 v166, a150\n"
    "v_accvgpr_read_b32 v167, a151\n"
    "v_pk_mul_f32 v[152:153], v[152:153], v[136:137]\n"
    "v_pk_mul_f32 v[154:155], v[154:155], v[136:137]\n"
    "v_pk_mul_f32 v[156:157], v[156:157], v[136:137]\n"
    "v_pk_mul_f32 v[158:159], v[158:159], v[136:137]\n"
    "v_cvt_pk_bf16_f32 v192, v144, v145\n"
    "v_cvt_pk_bf16_f32 v193, v146, v147\n"
    "v_cvt_pk_bf16_f32 v194, v148, v149\n"
    "v_cvt_pk_bf16_f32 v195, v150, v151\n"
    "v_accvgpr_write_b32 a252, v176\n"
    "v_accvgpr_write_b32 a253, v177\n"
    "v_accvgpr_write_b32 a254, v178\n"
    "v_accvgpr_write_b32 a255, v179\n"
    "v_accvgpr_read_b32 v168, a152\n"
    "v_accvgpr_read_b32 v169, a153\n"
    "v_accvgpr_read_b32 v170, a154\n"
    "v_accvgpr_read_b32 v171, a155\n"
    "v_accvgpr_read_b32 v172, a156\n"
    "v_accvgpr_read_b32 v173, a157\n"
    "v_accvgpr_read_b32 v174, a158\n"
    "v_accvgpr_read_b32 v175, a159\n"
    "v_pk_mul_f32 v[160:161], v[160:161], v[132:133]\n"
    "v_pk_mul_f32 v[162:163], v[162:163], v[132:133]\n"
    "v_pk_mul_f32 v[164:165], v[164:165], v[132:133]\n"
    "v_pk_mul_f32 v[166:167], v[166:167], v[132:133]\n"
    "v_cvt_pk_bf16_f32 v196, v152, v153\n"
    "v_cvt_pk_bf16_f32 v197, v154, v155\n"
    "v_cvt_pk_bf16_f32 v198, v156, v157\n"
    "v_cvt_pk_bf16_f32 v199, v158, v159\n"
    "v_accvgpr_read_b32 v144, a160\n"
    "v_accvgpr_read_b32 v145, a161\n"
    "v_accvgpr_read_b32 v146, a162\n"
    "v_accvgpr_read_b32 v147, a163\n"
    "v_accvgpr_read_b32 v148, a164\n"
    "v_accvgpr_read_b32 v149, a165\n"
    "v_accvgpr_read_b32 v150, a166\n"
    "v_accvgpr_read_b32 v151, a167\n"
    "v_pk_mul_f32 v[168:169], v[168:169], v[134:135]\n"
    "v_pk_mul_f32 v[170:171], v[170:171], v[134:135]\n"
    "v_pk_mul_f32 v[172:173], v[172:173], v[134:135]\n"
    "v_pk_mul_f32 v[174:175], v[174:175], v[134:135]\n"
    "v_cvt_pk_bf16_f32 v200, v160, v161\n"
    "v_cvt_pk_bf16_f32 v201, v162, v163\n"
    "v_cvt_pk_bf16_f32 v202, v164, v165\n"
    "v_cvt_pk_bf16_f32 v203, v166, v167\n"
    "v_accvgpr_read_b32 v152, a168\n"
    "v_accvgpr_read_b32 v153, a169\n"
    "v_accvgpr_read_b32 v154, a170\n"
    "v_accvgpr_read_b32 v155, a171\n"
    "v_accvgpr_read_b32 v156, a172\n"
    "v_accvgpr_read_b32 v157, a173\n"
    "v_accvgpr_read_b32 v158, a174\n"
    "v_accvgpr_read_b32 v159, a175\n"
    "v_pk_mul_f32 v[144:145], v[144:145], v[136:137]\n"
    "v_pk_mul_f32 v[146:147], v[146:147], v[136:137]\n"
    "v_pk_mul_f32 v[148:149], v[148:149], v[136:137]\n"
    "v_pk_mul_f32 v[150:151], v[150:151], v[136:137]\n"
    "v_cvt_pk_bf16_f32 v204, v168, v169\n"
    "v_cvt_pk_bf16_f32 v205, v170, v171\n"
    "v_cvt_pk_bf16_f32 v206, v172, v173\n"
    "v_cvt_pk_bf16_f32 v207, v174, v175\n"
    "v_accvgpr_read_b32 v160, a176\n"
    "v_accvgpr_read_b32 v161, a177\n"
    "v_accvgpr_read_b32 v162, a178\n"
    "v_accvgpr_read_b32 v163, a179\n"
    "v_accvgpr_read_b32 v164, a180\n"
    "v_accvgpr_read_b32 v165, a181\n"
    "v_accvgpr_read_b32 v166, a182\n"
    "v_accvgpr_read_b32 v167, a183\n"
    "v_pk_mul_f32 v[152:153], v[152:153], v[132:133]\n"
    "v_pk_mul_f32 v[154:155], v[154:155], v[132:133]\n"
    "v_pk_mul_f32 v[156:157], v[156:157], v[132:133]\n"
    "v_pk_mul_f32 v[158:159], v[158:159], v[132:133]\n"
    "v_cvt_pk_bf16_f32 v208, v144, v145\n"
    "v_cvt_pk_bf16_f32 v209, v146, v147\n"
    "v_cvt_pk_bf16_f32 v210, v148, v149\n"
    "v_cvt_pk_bf16_f32 v211, v150, v151\n"
    "v_accvgpr_read_b32 v168, a184\n"
    "v_accvgpr_read_b32 v169, a185\n"
    "v_accvgpr_read_b32 v170, a186\n"
    "v_accvgpr_read_b32 v171, a187\n"
    "v_accvgpr_read_b32 v172, a188\n"
    "v_accvgpr_read_b32 v173, a189\n"
    "v_accvgpr_read_b32 v174, a190\n"
    "v_accvgpr_read_b32 v175, a191\n"
    "v_pk_mul_f32 v[160:161], v[160:161], v[134:135]\n"
    "v_pk_mul_f32 v[162:163], v[162:163], v[134:135]\n"
    "v_pk_mul_f32 v[164:165], v[164:165], v[134:135]\n"
    "v_pk_mul_f32 v[166:167], v[166:167], v[134:135]\n"
    "v_cvt_pk_bf16_f32 v212, v152, v153\n"
    "v_cvt_pk_bf16_f32 v213, v154, v155\n"
    "v_cvt_pk_bf16_f32 v214, v156, v157\n"
    "v_cvt_pk_bf16_f32 v215, v158, v159\n"
    "v_pk_mul_f32 v[168:169], v[168:169], v[136:137]\n"
    "v_pk_mul_f32 v[170:171], v[170:171], v[136:137]\n"
    "v_pk_mul_f32 v[172:173], v[172:173], v[136:137]\n"
    "v_pk_mul_f32 v[174:175], v[174:175], v[136:137]\n"
    "v_cvt_pk_bf16_f32 v216, v160, v161\n"
    "v_cvt_pk_bf16_f32 v217, v162, v163\n"
    "v_cvt_pk_bf16_f32 v218, v164, v165\n"
    "v_cvt_pk_bf16_f32 v219, v166, v167\n"
    "v_cvt_pk_bf16_f32 v220, v168, v169\n"
    "v_cvt_pk_bf16_f32 v221, v170, v171\n"
    "v_cvt_pk_bf16_f32 v222, v172, v173\n"
    "v_cvt_pk_bf16_f32 v223, v174, v175\n"
    "s_mov_b32 s67, 1\n"
    "s_branch LQ\n"
    "LCO1:\n"
    "v_accvgpr_read_b32 v144, a0\n"
    "v_accvgpr_read_b32 v145, a1\n"
    "v_accvgpr_read_b32 v146, a2\n"
    "v_accvgpr_read_b32 v147, a3\n"
    "v_accvgpr_read_b32 v148, a4\n"
    "v_accvgpr_read_b32 v149, a5\n"
    "v_accvgpr_read_b32 v150, a6\n"
    "v_accvgpr_read_b32 v151, a7\n"
    "v_accvgpr_read_b32 v152, a8\n"
    "v_accvgpr_read_b32 v153, a9\n"
    "v_accvgpr_read_b32 v154, a10\n"
    "v_accvgpr_read_b32 v155, a11\n"
    "v_accvgpr_read_b32 v156, a12\n"
    "v_accvgpr_read_b32 v157, a13\n"
    "v_accvgpr_read_b32 v158, a14\n"
    "v_accvgpr_read_b32 v159, a15\n"
    "v_pk_mul_f32 v[144:145], v[144:145], v[132:133]\n"
    "v_pk_mul_f32 v[146:147], v[146:147], v[132:133]\n"
    "v_pk_mul_f32 v[148:149], v[148:149], v[132:133]\n"
    "v_pk_mul_f32 v[150:151], v[150:151], v[132:133]\n"
    "v_accvgpr_read_b32 v160, a16\n"
    "v_accvgpr_read_b32 v161, a17\n"
    "v_accvgpr_read_b32 v162, a18\n"
    "v_accvgpr_read_b32 v163, a19\n"
    "v_accvgpr_read_b32 v164, a20\n"
    "v_accvgpr_read_b32 v165, a21\n"
    "v_accvgpr_read_b32 v166, a22\n"
    "v_accvgpr_read_b32 v167, a23\n"
    "v_pk_mul_f32 v[152:153], v[152:153], v[134:135]\n"
    "v_pk_mul_f32 v[154:155], v[154:155], v[134:135]\n"
    "v_pk_mul_f32 v[156:157], v[156:157], v[134:135]\n"
    "v_pk_mul_f32 v[158:159], v[158:159], v[134:135]\n"
    "v_cvt_pk_bf16_f32 v0, v144, v145\n"
    "v_cvt_pk_bf16_f32 v1, v146, v147\n"
    "v_cvt_pk_bf16_f32 v2, v148, v149\n"
    "v_cvt_pk_bf16_f32 v3, v150, v151\n"
    "v_accvgpr_read_b32 v168, a24\n"
    "v_accvgpr_read_b32 v169, a25\n"
    "v_accvgpr_read_b32 v170, a26\n"
    "v_accvgpr_read_b32 v171, a27\n"
    "v_accvgpr_read_b32 v172, a28\n"
    "v_accvgpr_read_b32 v173, a29\n"
    "v_accvgpr_read_b32 v174, a30\n"
    "v_accvgpr_read_b32 v175, a31\n"
    "v_pk_mul_f32 v[160:161], v[160:161], v[136:137]\n"
    "v_pk_mul_f32 v[162:163], v[162:163], v[136:137]\n"
    "v_pk_mul_f32 v[164:165], v[164:165], v[136:137]\n"
    "v_pk_mul_f32 v[166:167], v[166:167], v[136:137]\n"
    "v_cvt_pk_bf16_f32 v4, v152, v153\n"
    "v_cvt_pk_bf16_f32 v5, v154, v155\n"
    "v_cvt_pk_bf16_f32 v6, v156, v157\n"
    "v_cvt_pk_bf16_f32 v7, v158, v159\n"
    "v_accvgpr_read_b32 v144, a32\n"
    "v_accvgpr_read_b32 v145, a33\n"
    "v_accvgpr_read_b32 v146, a34\n"
    "v_accvgpr_read_b32 v147, a35\n"
    "v_accvgpr_read_b32 v148, a36\n"
    "v_accvgpr_read_b32 v149, a37\n"
    "v_accvgpr_read_b32 v150, a38\n"
    "v_accvgpr_read_b32 v151, a39\n"
    "v_pk_mul_f32 v[168:169], v[168:169], v[132:133]\n"
    "v_pk_mul_f32 v[170:171], v[170:171], v[132:133]\n"
    "v_pk_mul_f32 v[172:173], v[172:173], v[132:133]\n"
    "v_pk_mul_f32 v[174:175], v[174:175], v[132:133]\n"
    "v_cvt_pk_bf16_f32 v8, v160, v161\n"
    "v_cvt_pk_bf16_f32 v9, v162, v163\n"
    "v_cvt_pk_bf16_f32 v10, v164, v165\n"
    "v_cvt_pk_bf16_f32 v11, v166, v167\n"
    "v_accvgpr_read_b32 v152, a40\n"
    "v_accvgpr_read_b32 v153, a41\n"
    "v_accvgpr_read_b32 v154, a42\n"
    "v_accvgpr_read_b32 v155, a43\n"
    "v_accvgpr_read_b32 v156, a44\n"
    "v_accvgpr_read_b32 v157, a45\n"
    "v_accvgpr_read_b32 v158, a46\n"
    "v_accvgpr_read_b32 v159, a47\n"
    "v_pk_mul_f32 v[144:145], v[144:145], v[134:135]\n"
    "v_pk_mul_f32 v[146:147], v[146:147], v[134:135]\n"
    "v_pk_mul_f32 v[148:149], v[148:149], v[134:135]\n"
    "v_pk_mul_f32 v[150:151], v[150:151], v[134:135]\n"
    "v_cvt_pk_bf16_f32 v12, v168, v169\n"
    "v_cvt_pk_bf16_f32 v13, v170, v171\n"
    "v_cvt_pk_bf16_f32 v14, v172, v173\n"
    "v_cvt_pk_bf16_f32 v15, v174, v175\n"
    "v_accvgpr_read_b32 v160, a48\n"
    "v_accvgpr_read_b32 v161, a49\n"
    "v_accvgpr_read_b32 v162, a50\n"
    "v_accvgpr_read_b32 v163, a51\n"
    "v_accvgpr_read_b32 v164, a52\n"
    "v_accvgpr_read_b32 v165, a53\n"
    "v_accvgpr_read_b32 v166, a54\n"
    "v_accvgpr_read_b32 v167, a55\n"
    "v_pk_mul_f32 v[152:153], v[152:153], v[136:137]\n"
    "v_pk_mul_f32 v[154:155], v[154:155], v[136:137]\n"
    "v_pk_mul_f32 v[156:157], v[156:157], v[136:137]\n"
    "v_pk_mul_f32 v[158:159], v[158:159], v[136:137]\n"
    "v_cvt_pk_bf16_f32 v16, v144, v145\n"
    "v_cvt_pk_bf16_f32 v17, v146, v147\n"
    "v_cvt_pk_bf16_f32 v18, v148, v149\n"
    "v_cvt_pk_bf16_f32 v19, v150, v151\n"
    "v_accvgpr_read_b32 v168, a56\n"
    "v_accvgpr_read_b32 v169, a57\n"
    "v_accvgpr_read_b32 v170, a58\n"
    "v_accvgpr_read_b32 v171, a59\n"
    "v_accvgpr_read_b32 v172, a60\n"
    "v_accvgpr_read_b32 v173, a61\n"
    "v_accvgpr_read_b32 v174, a62\n"
    "v_accvgpr_read_b32 v175, a63\n"
    "v_pk_mul_f32 v[160:161], v[160:161], v[132:133]\n"
    "v_pk_mul_f32 v[162:163], v[162:163], v[132:133]\n"
    "v_pk_mul_f32 v[164:165], v[164:165], v[132:133]\n"
    "v_pk_mul_f32 v[166:167], v[166:167], v[132:133]\n"
    "v_cvt_pk_bf16_f32 v20, v152, v153\n"
    "v_cvt_pk_bf16_f32 v21, v154, v155\n"
    "v_cvt_pk_bf16_f32 v22, v156, v157\n"
    "v_cvt_pk_bf16_f32 v23, v158, v159\n"
    "v_accvgpr_read_b32 v144, a64\n"
    "v_accvgpr_read_b32 v145, a65\n"
    "v_accvgpr_read_b32 v146, a66\n"
    "v_accvgpr_read_b32 v147, a67\n"
    "v_accvgpr_read_b32 v148, a68\n"
    "v_accvgpr_read_b32 v149, a69\n"
    "v_accvgpr_read_b32 v150, a70\n"
    "v_accvgpr_read_b32 v151, a71\n"
    "v_pk_mul_f32 v[168:169], v[168:169], v[134:135]\n"
    "v_pk_mul_f32 v[170:171], v[170:171], v[134:135]\n"
    "v_pk_mul_f32 v[172:173], v[172:173], v[134:135]\n"
    "v_pk_mul_f32 v[174:175], v[174:175], v[134:135]\n"
    "v_cvt_pk_bf16_f32 v24, v160, v161\n"
    "v_cvt_pk_bf16_f32 v25, v162, v163\n"
    "v_cvt_pk_bf16_f32 v26, v164, v165\n"
    "v_cvt_pk_bf16_f32 v27, v166, v167\n"
    "v_accvgpr_read_b32 v152, a72\n"
    "v_accvgpr_read_b32 v153, a73\n"
    "v_accvgpr_read_b32 v154, a74\n"
    "v_accvgpr_read_b32 v155, a75\n"
    "v_accvgpr_read_b32 v156, a76\n"
    "v_accvgpr_read_b32 v157, a77\n"
    "v_accvgpr_read_b32 v158, a78\n"
    "v_accvgpr_read_b32 v159, a79\n"
    "v_pk_mul_f32 v[144:145], v[144:145], v[136:137]\n"
    "v_pk_mul_f32 v[146:147], v[146:147], v[136:137]\n"
    "v_pk_mul_f32 v[148:149], v[148:149], v[136:137]\n"
    "v_pk_mul_f32 v[150:151], v[150:151], v[136:137]\n"
    "v_cvt_pk_bf16_f32 v28, v168, v169\n"
    "v_cvt_pk_bf16_f32 v29, v170, v171\n"
    "v_cvt_pk_bf16_f32 v30, v172, v173\n"
    "v_cvt_pk_bf16_f32 v31, v174, v175\n"
    "v_accvgpr_read_b32 v160, a80\n"
    "v_accvgpr_read_b32 v161, a81\n"
    "v_accvgpr_read_b32 v162, a82\n"
    "v_accvgpr_read_b32 v163, a83\n"
    "v_accvgpr_read_b32 v164, a84\n"
    "v_accvgpr_read_b32 v165, a85\n"
    "v_accvgpr_read_b32 v166, a86\n"
    "v_accvgpr_read_b32 v167, a87\n"
    "v_pk_mul_f32 v[152:153], v[152:153], v[132:133]\n"
    "v_pk_mul_f32 v[154:155], v[154:155], v[132:133]\n"
    "v_pk_mul_f32 v[156:157], v[156:157], v[132:133]\n"
    "v_pk_mul_f32 v[158:159], v[158:159], v[132:133]\n"
    "v_cvt_pk_bf16_f32 v32, v144, v145\n"
    "v_cvt_pk_bf16_f32 v33, v146, v147\n"
    "v_cvt_pk_bf16_f32 v34, v148, v149\n"
    "v_cvt_pk_bf16_f32 v35, v150, v151\n"
    "v_accvgpr_read_b32 v168, a88\n"
    "v_accvgpr_read_b32 v169, a89\n"
    "v_accvgpr_read_b32 v170, a90\n"
    "v_accvgpr_read_b32 v171, a91\n"
    "v_accvgpr_read_b32 v172, a92\n"
    "v_accvgpr_read_b32 v173, a93\n"
    "v_accvgpr_read_b32 v174, a94\n"
    "v_accvgpr_read_b32 v175, a95\n"
    "v_pk_mul_f32 v[160:161], v[160:161], v[134:135]\n"
    "v_pk_mul_f32 v[162:163], v[162:163], v[134:135]\n"
    "v_pk_mul_f32 v[164:165], v[164:165], v[134:135]\n"
    "v_pk_mul_f32 v[166:167], v[166:167], v[134:135]\n"
    "v_cvt_pk_bf16_f32 v36, v152, v153\n"
    "v_cvt_pk_bf16_f32 v37, v154, v155\n"
    "v_cvt_pk_bf16_f32 v38, v156, v157\n"
    "v_cvt_pk_bf16_f32 v39, v158, v159\n"
    "v_accvgpr_read_b32 v144, a96\n"
    "v_accvgpr_read_b32 v145, a97\n"
    "v_accvgpr_read_b32 v146, a98\n"
    "v_accvgpr_read_b32 v147, a99\n"
    "v_accvgpr_read_b32 v148, a100\n"
    "v_accvgpr_read_b32 v149, a101\n"
    "v_accvgpr_read_b32 v150, a102\n"
    "v_accvgpr_read_b32 v151, a103\n"
    "v_pk_mul_f32 v[168:169], v[168:169], v[136:137]\n"
    "v_pk_mul_f32 v[170:171], v[170:171], v[136:137]\n"
    "v_pk_mul_f32 v[172:173], v[172:173], v[136:137]\n"
    "v_pk_mul_f32 v[174:175], v[174:175], v[136:137]\n"
    "v_cvt_pk_bf16_f32 v40, v160, v161\n"
    "v_cvt_pk_bf16_f32 v41, v162, v163\n"
    "v_cvt_pk_bf16_f32 v42, v164, v165\n"
    "v_cvt_pk_bf16_f32 v43, v166, v167\n"
    "v_accvgpr_read_b32 v152, a104\n"
    "v_accvgpr_read_b32 v153, a105\n"
    "v_accvgpr_read_b32 v154, a106\n"
    "v_accvgpr_read_b32 v155, a107\n"
    "v_accvgpr_read_b32 v156, a108\n"
    "v_accvgpr_read_b32 v157, a109\n"
    "v_accvgpr_read_b32 v158, a110\n"
    "v_accvgpr_read_b32 v159, a111\n"
    "v_pk_mul_f32 v[144:145], v[144:145], v[132:133]\n"
    "v_pk_mul_f32 v[146:147], v[146:147], v[132:133]\n"
    "v_pk_mul_f32 v[148:149], v[148:149], v[132:133]\n"
    "v_pk_mul_f32 v[150:151], v[150:151], v[132:133]\n"
    "v_cvt_pk_bf16_f32 v44, v168, v169\n"
    "v_cvt_pk_bf16_f32 v45, v170, v171\n"
    "v_cvt_pk_bf16_f32 v46, v172, v173\n"
    "v_cvt_pk_bf16_f32 v47, v174, v175\n"
    "v_accvgpr_read_b32 v160, a112\n"
    "v_accvgpr_read_b32 v161, a113\n"
    "v_accvgpr_read_b32 v162, a114\n"
    "v_accvgpr_read_b32 v163, a115\n"
    "v_accvgpr_read_b32 v164, a116\n"
    "v_accvgpr_read_b32 v165, a117\n"
    "v_accvgpr_read_b32 v166, a118\n"
    "v_accvgpr_read_b32 v167, a119\n"
    "v_pk_mul_f32 v[152:153], v[152:153], v[134:135]\n"
    "v_pk_mul_f32 v[154:155], v[154:155], v[134:135]\n"
    "v_pk_mul_f32 v[156:157], v[156:157], v[134:135]\n"
    "v_pk_mul_f32 v[158:159], v[158:159], v[134:135]\n"
    "v_cvt_pk_bf16_f32 v48, v144, v145\n"
    "v_cvt_pk_bf16_f32 v49, v146, v147\n"
    "v_cvt_pk_bf16_f32 v50, v148, v149\n"
    "v_cvt_pk_bf16_f32 v51, v150, v151\n"
    "v_accvgpr_read_b32 v168, a120\n"
    "v_accvgpr_read_b32 v169, a121\n"
    "v_accvgpr_read_b32 v170, a122\n"
    "v_accvgpr_read_b32 v171, a123\n"
    "v_accvgpr_read_b32 v172, a124\n"
    "v_accvgpr_read_b32 v173, a125\n"
    "v_accvgpr_read_b32 v174, a126\n"
    "v_accvgpr_read_b32 v175, a127\n"
    "v_pk_mul_f32 v[160:161], v[160:161], v[136:137]\n"
    "v_pk_mul_f32 v[162:163], v[162:163], v[136:137]\n"
    "v_pk_mul_f32 v[164:165], v[164:165], v[136:137]\n"
    "v_pk_mul_f32 v[166:167], v[166:167], v[136:137]\n"
    "v_cvt_pk_bf16_f32 v52, v152, v153\n"
    "v_cvt_pk_bf16_f32 v53, v154, v155\n"
    "v_cvt_pk_bf16_f32 v54, v156, v157\n"
    "v_cvt_pk_bf16_f32 v55, v158, v159\n"
    "v_accvgpr_read_b32 v144, a128\n"
    "v_accvgpr_read_b32 v145, a129\n"
    "v_accvgpr_read_b32 v146, a130\n"
    "v_accvgpr_read_b32 v147, a131\n"
    "v_accvgpr_read_b32 v148, a132\n"
    "v_accvgpr_read_b32 v149, a133\n"
    "v_accvgpr_read_b32 v150, a134\n"
    "v_accvgpr_read_b32 v151, a135\n"
    "v_pk_mul_f32 v[168:169], v[168:169], v[132:133]\n"
    "v_pk_mul_f32 v[170:171], v[170:171], v[132:133]\n"
    "v_pk_mul_f32 v[172:173], v[172:173], v[132:133]\n"
    "v_pk_mul_f32 v[174:175], v[174:175], v[132:133]\n"
    "v_cvt_pk_bf16_f32 v56, v160, v161\n"
    "v_cvt_pk_bf16_f32 v57, v162, v163\n"
    "v_cvt_pk_bf16_f32 v58, v164, v165\n"
    "v_cvt_pk_bf16_f32 v59, v166, v167\n"
    "v_accvgpr_read_b32 v152, a136\n"
    "v_accvgpr_read_b32 v153, a137\n"
    "v_accvgpr_read_b32 v154, a138\n"
    "v_accvgpr_read_b32 v155, a139\n"
    "v_accvgpr_read_b32 v156, a140\n"
    "v_accvgpr_read_b32 v157, a141\n"
    "v_accvgpr_read_b32 v158, a142\n"
    "v_accvgpr_read_b32 v159, a143\n"
    "v_pk_mul_f32 v[144:145], v[144:145], v[134:135]\n"
    "v_pk_mul_f32 v[146:147], v[146:147], v[134:135]\n"
    "v_pk_mul_f32 v[148:149], v[148:149], v[134:135]\n"
    "v_pk_mul_f32 v[150:151], v[150:151], v[134:135]\n"
    "v_cvt_pk_bf16_f32 v60, v168, v169\n"
    "v_cvt_pk_bf16_f32 v61, v170, v171\n"
    "v_cvt_pk_bf16_f32 v62, v172, v173\n"
    "v_cvt_pk_bf16_f32 v63, v174, v175\n"
    "v_accvgpr_read_b32 v160, a144\n"
    "v_accvgpr_read_b32 v161, a145\n"
    "v_accvgpr_read_b32 v162, a146\n"
    "v_accvgpr_read_b32 v163, a147\n"
    "v_accvgpr_read_b32 v164, a148\n"
    "v_accvgpr_read_b32 v165, a149\n"
    "v_accvgpr_read_b32 v166, a150\n"
    "v_accvgpr_read_b32 v167, a151\n"
    "v_pk_mul_f32 v[152:153], v[152:153], v[136:137]\n"
    "v_pk_mul_f32 v[154:155], v[154:155], v[136:137]\n"
    "v_pk_mul_f32 v[156:157], v[156:157], v[136:137]\n"
    "v_pk_mul_f32 v[158:159], v[158:159], v[136:137]\n"
    "v_cvt_pk_bf16_f32 v64, v144, v145\n"
    "v_cvt_pk_bf16_f32 v65, v146, v147\n"
    "v_cvt_pk_bf16_f32 v66, v148, v149\n"
    "v_cvt_pk_bf16_f32 v67, v150, v151\n"
    "v_accvgpr_read_b32 v168, a152\n"
    "v_accvgpr_read_b32 v169, a153\n"
    "v_accvgpr_read_b32 v170, a154\n"
    "v_accvgpr_read_b32 v171, a155\n"
    "v_accvgpr_read_b32 v172, a156\n"
    "v_accvgpr_read_b32 v173, a157\n"
    "v_accvgpr_read_b32 v174, a158\n"
    "v_accvgpr_read_b32 v175, a159\n"
    "v_pk_mul_f32 v[160:161], v[160:161], v[132:133]\n"
    "v_pk_mul_f32 v[162:163], v[162:163], v[132:133]\n"
    "v_pk_mul_f32 v[164:165], v[164:165], v[132:133]\n"
    "v_pk_mul_f32 v[166:167], v[166:167], v[132:133]\n"
    "v_cvt_pk_bf16_f32 v68, v152, v153\n"
    "v_cvt_pk_bf16_f32 v69, v154, v155\n"
    "v_cvt_pk_bf16_f32 v70, v156, v157\n"
    "v_cvt_pk_bf16_f32 v71, v158, v159\n"
    "v_accvgpr_read_b32 v144, a160\n"
    "v_accvgpr_read_b32 v145, a161\n"
    "v_accvgpr_read_b32 v146, a162\n"
    "v_accvgpr_read_b32 v147, a163\n"
    "v_accvgpr_read_b32 v148, a164\n"
    "v_accvgpr_read_b32 v149, a165\n"
    "v_accvgpr_read_b32 v150, a166\n"
    "v_accvgpr_read_b32 v151, a167\n"
    "v_pk_mul_f32 v[168:169], v[168:169], v[134:135]\n"
    "v_pk_mul_f32 v[170:171], v[170:171], v[134:135]\n"
    "v_pk_mul_f32 v[172:173], v[172:173], v[134:135]\n"
    "v_pk_mul_f32 v[174:175], v[174:175], v[134:135]\n"
    "v_cvt_pk_bf16_f32 v72, v160, v161\n"
    "v_cvt_pk_bf16_f32 v73, v162, v163\n"
    "v_cvt_pk_bf16_f32 v74, v164, v165\n"
    "v_cvt_pk_bf16_f32 v75, v166, v167\n"
    "v_accvgpr_read_b32 v152, a168\n"
    "v_accvgpr_read_b32 v153, a169\n"
    "v_accvgpr_read_b32 v154, a170\n"
    "v_accvgpr_read_b32 v155, a171\n"
    "v_accvgpr_read_b32 v156, a172\n"
    "v_accvgpr_read_b32 v157, a173\n"
    "v_accvgpr_read_b32 v158, a174\n"
    "v_accvgpr_read_b32 v159, a175\n"
    "v_pk_mul_f32 v[144:145], v[144:145], v[136:137]\n"
    "v_pk_mul_f32 v[146:147], v[146:147], v[136:137]\n"
    "v_pk_mul_f32 v[148:149], v[148:149], v[136:137]\n"
    "v_pk_mul_f32 v[150:151], v[150:151], v[136:137]\n"
    "v_cvt_pk_bf16_f32 v76, v168, v169\n"
    "v_cvt_pk_bf16_f32 v77, v170, v171\n"
    "v_cvt_pk_bf16_f32 v78, v172, v173\n"
    "v_cvt_pk_bf16_f32 v79, v174, v175\n"
    "v_accvgpr_read_b32 v160, a176\n"
    "v_accvgpr_read_b32 v161, a177\n"
    "v_accvgpr_read_b32 v162, a178\n"
    "v_accvgpr_read_b32 v163, a179\n"
    "v_accvgpr_read_b32 v164, a180\n"
    "v_accvgpr_read_b32 v165, a181\n"
    "v_accvgpr_read_b32 v166, a182\n"
    "v_accvgpr_read_b32 v167, a183\n"
    "v_pk_mul_f32 v[152:153], v[152:153], v[132:133]\n"
    "v_pk_mul_f32 v[154:155], v[154:155], v[132:133]\n"
    "v_pk_mul_f32 v[156:157], v[156:157], v[132:133]\n"
    "v_pk_mul_f32 v[158:159], v[158:159], v[132:133]\n"
    "v_cvt_pk_bf16_f32 v80, v144, v145\n"
    "v_cvt_pk_bf16_f32 v81, v146, v147\n"
    "v_cvt_pk_bf16_f32 v82, v148, v149\n"
    "v_cvt_pk_bf16_f32 v83, v150, v151\n"
    "v_accvgpr_read_b32 v168, a184\n"
    "v_accvgpr_read_b32 v169, a185\n"
    "v_accvgpr_read_b32 v170, a186\n"
    "v_accvgpr_read_b32 v171, a187\n"
    "v_accvgpr_read_b32 v172, a188\n"
    "v_accvgpr_read_b32 v173, a189\n"
    "v_accvgpr_read_b32 v174, a190\n"
    "v_accvgpr_read_b32 v175, a191\n"
    "v_pk_mul_f32 v[160:161], v[160:161], v[134:135]\n"
    "v_pk_mul_f32 v[162:163], v[162:163], v[134:135]\n"
    "v_pk_mul_f32 v[164:165], v[164:165], v[134:135]\n"
    "v_pk_mul_f32 v[166:167], v[166:167], v[134:135]\n"
    "v_cvt_pk_bf16_f32 v84, v152, v153\n"
    "v_cvt_pk_bf16_f32 v85, v154, v155\n"
    "v_cvt_pk_bf16_f32 v86, v156, v157\n"
    "v_cvt_pk_bf16_f32 v87, v158, v159\n"
    "v_pk_mul_f32 v[168:169], v[168:169], v[136:137]\n"
    "v_pk_mul_f32 v[170:171], v[170:171], v[136:137]\n"
    "v_pk_mul_f32 v[172:173], v[172:173], v[136:137]\n"
    "v_pk_mul_f32 v[174:175], v[174:175], v[136:137]\n"
    "v_cvt_pk_bf16_f32 v88, v160, v161\n"
    "v_cvt_pk_bf16_f32 v89, v162, v163\n"
    "v_cvt_pk_bf16_f32 v90, v164, v165\n"
    "v_cvt_pk_bf16_f32 v91, v166, v167\n"
    "v_cvt_pk_bf16_f32 v92, v168, v169\n"
    "v_cvt_pk_bf16_f32 v93, v170, v171\n"
    "v_cvt_pk_bf16_f32 v94, v172, v173\n"
    "v_cvt_pk_bf16_f32 v95, v174, v175\n"
    "s_mul_i32 s79, s65, 56\n"
    "s_add_u32 s79, s79, s66\n"
    "s_lshl_b32 s79, s79, 2\n"
    "s_add_u32 s79, s79, s64\n"
    "s_mul_i32 s94, s79, 6144\n"
    "v_add_u32 v187, 0, v231\n"
    "s_mov_b32 s38, 256\n"
    "v_cmp_ne_u32 vcc, v238, v236\n"
    "s_nop 0\n"
    "s_cbranch_vccz LACKD\n"
    "v_lshrrev_b32 v241, 4, v231\n"
    "v_lshlrev_b32 v244, 11, v241\n"
    "v_cmp_gt_u32 vcc, 7, v241\n"
    "v_mov_b32 v242, 0x7fff0000\n"
    "s_nop 0\n"
    "v_cndmask_b32 v244, v242, v244, vcc\n"
    "v_cmp_ne_u32 vcc, s66, v241\n"
    "s_nop 1\n"
    "v_cndmask_b32 v244, v242, v244, vcc\n"
    "s_mov_b32 s39, 262144\n"
    "LACK:\n"
    "s_add_u32 s79, s36, 524288\n"
    "buffer_load_dword v238, v244, s[48:51], s79 offen sc1\n"
    "s_waitcnt vmcnt(0)\n"
    "v_cmp_ne_u32 vcc, v238, v236\n"
    "s_nop 0\n"
    "s_cbranch_vccz LACKD\n"
    "s_sleep 2\n"
    "s_sub_u32 s39, s39, 1\n"
    "s_cmp_lg_u32 s39, 0\n"
    "s_cbranch_scc1 LACK\n"
    "LACKD:\n"
    "s_waitcnt vmcnt(12)\n"
    "s_barrier\n"
    "v_add_u32 v227, s68, v225\n"
    "s_add_u32 s68, s68, 16896\n"
    "s_cmp_eq_u32 s68, 152064\n"
    "s_cselect_b32 s68, 0, s68\n"
    "ds_read_b64_tr_b16 a[0:1], v227\n"
    "ds_read_b64_tr_b16 a[2:3], v227 offset:128\n"
    "ds_read_b64_tr_b16 a[4:5], v227 offset:32\n"
    "ds_read_b64_tr_b16 a[6:7], v227 offset:160\n"
    "ds_read_b64_tr_b16 a[8:9], v227 offset:64\n"
    "ds_read_b64_tr_b16 a[10:11], v227 offset:192\n"
    "ds_read_b64_tr_b16 a[12:13], v227 offset:96\n"
    "ds_read_b64_tr_b16 a[14:15], v227 offset:224\n"
    "ds_read_b64_tr_b16 a[16:17], v227 offset:256\n"
    "ds_read_b64_tr_b16 a[18:19], v227 offset:384\n"
    "ds_read_b64_tr_b16 a[20:21], v227 offset:288\n"
    "ds_read_b64_tr_b16 a[22:23], v227 offset:416\n"
    "s_waitcnt lgkmcnt(8)\n"
    "v_mfma_f32_16x16x32_bf16 v[96:99], a[0:3], a[192:195], 0\n"
    "ds_read_b64_tr_b16 a[24:25], v227 offset:320\n"
    "s_add_u32 m0, s69, s70\n"
    "v_lshrrev_b32 v241, 4, v231\n"
    "v_lshlrev_b32 v243, 8, v241\n"
    "v_mfma_f32_16x16x32_bf16 v[104:107], a[0:3], a[196:199], 0\n"
    "ds_read_b64_tr_b16 a[26:27], v227 offset:448\n"
    "s_cmp_lt_u32 s66, 7\n"
    "s_cselect_b32 s80, 8, 0\n"
    "v_cmp_gt_u32 vcc, s80, v241\n"
    "v_mfma_f32_16x16x32_bf16 v[112:115], a[0:3], a[200:203], 0\n"
    "buffer_load_dwordx4 v232, s[56:59], s73 offen lds\n"
    "v_mov_b32 v242, 0x7fff0000\n"
    "v_mfma_f32_16x16x32_bf16 v[100:103], a[4:7], a[192:195], 0\n"
    "ds_read_b64_tr_b16 a[28:29], v227 offset:352\n"
    "v_cndmask_b32 v243, v242, v243, vcc\n"
    "v_cmp_ne_u32 vcc, s66, v241\n"
    "v_mfma_f32_16x16x32_bf16 v[108:111], a[4:7], a[196:199], 0\n"
    "ds_read_b64_tr_b16 a[30:31], v227 offset:480\n"
    "v_cndmask_b32 v243, v242, v243, vcc\n"
    "v_mfma_f32_16x16x32_bf16 v[116:119], a[4:7], a[200:203], 0\n"
    "s_waitcnt lgkmcnt(8)\n"
    "v_lshrrev_b32 v240, 4, v187\n"
    "s_add_u32 s79, s66, 1\n"
    "v_mfma_f32_16x16x32_bf16 v[120:123], a[8:11], a[192:195], 0\n"
    "ds_read_b64_tr_b16 a[32:33], v227 offset:512\n"
    "s_cmp_eq_u32 s79, 7\n"
    "s_cselect_b32 s79, 0, s79\n"
    "v_cmp_eq_u32 vcc, s79, v240\n"
    "v_mfma_f32_16x16x32_bf16 v[128:131], a[8:11], a[196:199], 0\n"
    "ds_read_b64_tr_b16 a[34:35], v227 offset:640\n"
    "v_cndmask_b32 v241, 0, v240, vcc\n"
    "v_mfma_f32_16x16x32_bf16 v[136:139], a[8:11], a[200:203], 0\n"
    "buffer_load_dwordx4 v233, s[56:59], s73 offen offset:1056 lds\n"
    "v_cmp_eq_u32 s[86:87], 7, v240\n"
    "s_nop 0\n"
    "s_or_b64 vcc, vcc, s[86:87]\n"
    "v_mfma_f32_16x16x32_bf16 v[124:127], a[12:15], a[192:195], 0\n"
    "ds_read_b64_tr_b16 a[36:37], v227 offset:544\n"
    "v_mov_b32 v242, 0x7fff0000\n"
    "v_mfma_f32_16x16x32_bf16 v[132:135], a[12:15], a[196:199], 0\n"
    "ds_read_b64_tr_b16 a[38:39], v227 offset:672\n"
    "v_cndmask_b32 v176, v242, v243, vcc\n"
    "v_cndmask_b32 v174, v243, v242, vcc\n"
    "v_mfma_f32_16x16x32_bf16 v[140:143], a[12:15], a[200:203], 0\n"
    "s_waitcnt lgkmcnt(8)\n"
    "v_cndmask_b32 v177, 0, v237, vcc\n"
    "v_sub_u32 v175, v237, v177\n"
    "v_mfma_f32_16x16x32_bf16 v[96:99], a[16:19], a[204:207], v[96:99]\n"
    "ds_read_b64_tr_b16 a[40:41], v227 offset:576\n"
    "v_add_u32 v180, 4096, v187\n"
    "v_mfma_f32_16x16x32_bf16 v[104:107], a[16:19], a[208:211], v[104:107]\n"
    "ds_read_b64_tr_b16 a[42:43], v227 offset:704\n"
    "s_mul_i32 s79, s65, 7\n"
    "s_add_u32 s79, s79, s66\n"
    "v_mfma_f32_16x16x32_bf16 v[112:115], a[16:19], a[212:215], v[112:115]\n"
    "buffer_load_dwordx4 v234, s[56:59], s73 offen offset:2112 lds\n"
    "s_lshl_b32 s79, s79, 5\n"
    "v_mfma_f32_16x16x32_bf16 v[100:103], a[20:23], a[204:207], v[100:103]\n"
    "ds_read_b64_tr_b16 a[44:45], v227 offset:608\n"
    "s_add_u32 s79, s79, s64\n"
    "s_mul_i32 s79, s79, 6144\n"
    "v_mfma_f32_16x16x32_bf16 v[108:111], a[20:23], a[208:211], v[108:111]\n"
    "ds_read_b64_tr_b16 a[46:47], v227 offset:736\n"
    "s_add_u32 s40, s66, 6\n"
    "s_sub_u32 s80, s40, 7\n"
    "v_mfma_f32_16x16x32_bf16 v[116:119], a[20:23], a[212:215], v[116:119]\n"
    "s_waitcnt lgkmcnt(8)\n"
    "s_cmp_ge_u32 s40, 7\n"
    "s_cselect_b32 s40, s80, s40\n"
    "v_mfma_f32_16x16x32_bf16 v[120:123], a[24:27], a[204:207], v[120:123]\n"
    "ds_read_b64_tr_b16 a[48:49], v227 offset:768\n"
    "s_mul_i32 s40, s40, 24576\n"
    "s_add_u32 s40, s40, s79\n"
    "v_mfma_f32_16x16x32_bf16 v[128:131], a[24:27], a[208:211], v[128:131]\n"
    "ds_read_b64_tr_b16 a[50:51], v227 offset:896\n"
    "s_add_u32 s41, s66, 5\n"
    "v_mfma_f32_16x16x32_bf16 v[136:139], a[24:27], a[212:215], v[136:139]\n"
    "buffer_load_dwordx4 v235, s[56:59], s73 offen offset:3168 lds\n"
    "s_add_u32 s69, s69, 16896\n"
    "s_sub_u32 s80, s41, 7\n"
    "s_cmp_ge_u32 s41, 7\n"
    "s_cselect_b32 s41, s80, s41\n"
    "v_mfma_f32_16x16x32_bf16 v[124:127], a[28:31], a[204:207], v[124:127]\n"
    "ds_read_b64_tr_b16 a[52:53], v227 offset:800\n"
    "s_mul_i32 s41, s41, 24576\n"
    "s_add_u32 s41, s41, s79\n"
    "v_mfma_f32_16x16x32_bf16 v[132:135], a[28:31], a[208:211], v[132:135]\n"
    "ds_read_b64_tr_b16 a[54:55], v227 offset:928\n"
    "s_add_u32 s42, s66, 4\n"
    "v_mfma_f32_16x16x32_bf16 v[140:143], a[28:31], a[212:215], v[140:143]\n"
    "s_waitcnt lgkmcnt(8)\n"
    "s_sub_u32 s80, s42, 7\n"
    "s_cmp_ge_u32 s42, 7\n"
    "s_cselect_b32 s42, s80, s42\n"
    "v_mfma_f32_16x16x32_bf16 v[96:99], a[32:35], a[216:219], v[96:99]\n"
    "ds_read_b64_tr_b16 a[56:57], v227 offset:832\n"
    "s_mul_i32 s42, s42, 24576\n"
    "v_mfma_f32_16x16x32_bf16 v[104:107], a[32:35], a[220:223], v[104:107]\n"
    "ds_read_b64_tr_b16 a[58:59], v227 offset:960\n"
    "s_add_u32 s42, s42, s79\n"
    "s_add_u32 s43, s66, 3\n"
    "v_mfma_f32_16x16x32_bf16 v[112:115], a[32:35], a[224:227], v[112:115]\n"
    "s_cmp_eq_u32 s69, 152064\n"
    "s_cselect_b32 s69, 0, s69\n"
    "s_sub_u32 s80, s43, 7\n"
    "s_cmp_ge_u32 s43, 7\n"
    "s_cselect_b32 s43, s80, s43\n"
    "v_mfma_f32_16x16x32_bf16 v[100:103], a[36:39], a[216:219], v[100:103]\n"
    "ds_read_b64_tr_b16 a[60:61], v227 offset:864\n"
    "s_add_u32 s73, s73, s74\n"
    "s_mul_i32 s43, s43, 24576\n"
    "v_mfma_f32_16x16x32_bf16 v[108:111], a[36:39], a[220:223], v[108:111]\n"
    "ds_read_b64_tr_b16 a[62:63], v227 offset:992\n"
    "s_add_u32 m0, s69, s70\n"
    "s_add_u32 s43, s43, s79\n"
    "s_add_u32 s52, s66, 2\n"
    "v_mfma_f32_16x16x32_bf16 v[116:119], a[36:39], a[224:227], v[116:119]\n"
    "s_waitcnt lgkmcnt(8)\n"
    "v_add_u32 v227, s68, v225\n"
    "s_sub_u32 s80, s52, 7\n"
    "v_mfma_f32_16x16x32_bf16 v[120:123], a[40:43], a[216:219], v[120:123]\n"
    "ds_read_b64_tr_b16 a[0:1], v227\n"
    "s_add_u32 s68, s68, 16896\n"
    "s_cmp_ge_u32 s52, 7\n"
    "s_cselect_b32 s52, s80, s52\n"
    "s_mul_i32 s52, s52, 24576\n"
    "v_mfma_f32_16x16x32_bf16 v[128:131], a[40:43], a[220:223], v[128:131]\n"
    "ds_read_b64_tr_b16 a[2:3], v227 offset:128\n"
    "s_add_u32 s52, s52, s79\n"
    "s_add_u32 s53, s66, 1\n"
    "v_mfma_f32_16x16x32_bf16 v[136:139], a[40:43], a[224:227], v[136:139]\n"
    "s_cmp_eq_u32 s68, 152064\n"
    "s_cselect_b32 s68, 0, s68\n"
    "s_sub_u32 s80, s53, 7\n"
    "v_mfma_f32_16x16x32_bf16 v[124:127], a[44:47], a[216:219], v[124:127]\n"
    "ds_read_b64_tr_b16 a[4:5], v227 offset:32\n"
    "s_cmp_ge_u32 s53, 7\n"
    "s_cselect_b32 s53, s80, s53\n"
    "s_mul_i32 s53, s53, 24576\n"
    "v_mfma_f32_16x16x32_bf16 v[132:135], a[44:47], a[220:223], v[132:135]\n"
    "ds_read_b64_tr_b16 a[6:7], v227 offset:160\n"
    "s_add_u32 s53, s53, s79\n"
    "v_mfma_f32_16x16x32_bf16 v[140:143], a[44:47], a[224:227], v[140:143]\n"
    "s_waitcnt lgkmcnt(8)\n"
    "buffer_load_dwordx4 v232, s[56:59], s73 offen lds\n"
    "s_add_u32 s54, s79, 172032\n"
    "v_lshrrev_b32 v243, 4, v187\n"
    "v_mfma_f32_16x16x32_bf16 v[96:99], a[48:51], a[228:231], v[96:99]\n"
    "ds_read_b64_tr_b16 a[8:9], v227 offset:64\n"
    "v_and_b32 v241, 15, v243\n"
    "v_lshrrev_b32 v242, 4, v243\n"
    "v_mfma_f32_16x16x32_bf16 v[104:107], a[48:51], a[232:235], v[104:107]\n"
    "ds_read_b64_tr_b16 a[10:11], v227 offset:192\n"
    "v_lshrrev_b32 v243, 2, v241\n"
    "v_mfma_f32_16x16x32_bf16 v[112:115], a[48:51], a[236:239], v[112:115]\n"
    "v_cmp_eq_u32 vcc, v243, v242\n"
    "v_and_b32 v243, 1, v241\n"
    "v_mfma_f32_16x16x32_bf16 v[100:103], a[52:55], a[228:231], v[100:103]\n"
    "ds_read_b64_tr_b16 a[12:13], v227 offset:96\n"
    "v_lshlrev_b32 v243, 4, v243\n"
    "v_mfma_f32_16x16x32_bf16 v[108:111], a[52:55], a[232:235], v[108:111]\n"
    "ds_read_b64_tr_b16 a[14:15], v227 offset:224\n"
    "v_mov_b32 v168, 16256\n"
    "v_lshlrev_b32 v243, v243, v168\n"
    "v_mfma_f32_16x16x32_bf16 v[116:119], a[52:55], a[236:239], v[116:119]\n"
    "s_waitcnt lgkmcnt(8)\n"
    "buffer_load_dwordx4 v233, s[56:59], s73 offen offset:1056 lds\n"
    "v_cndmask_b32 v243, 0, v243, vcc\n"
    "v_and_b32 v241, 2, v241\n"
    "v_mfma_f32_16x16x32_bf16 v[120:123], a[56:59], a[228:231], v[120:123]\n"
    "ds_read_b64_tr_b16 a[16:17], v227 offset:256\n"
    "v_cmp_eq_u32 vcc, 0, v241\n"
    "v_mfma_f32_16x16x32_bf16 v[128:131], a[56:59], a[232:235], v[128:131]\n"
    "ds_read_b64_tr_b16 a[18:19], v227 offset:384\n"
    "v_cndmask_b32 v170, 0, v243, vcc\n"
    "v_sub_u32 v171, v243, v170\n"
    "v_mfma_f32_16x16x32_bf16 v[136:139], a[56:59], a[236:239], v[136:139]\n"
    "v_mov_b32 v168, 0\n"
    "v_mfma_f32_16x16x32_bf16 v[124:127], a[60:63], a[228:231], v[124:127]\n"
    "ds_read_b64_tr_b16 a[20:21], v227 offset:288\n"
    "v_mov_b32 v169, 0\n"
    "v_mov_b32 v172, 0\n"
    "v_mfma_f32_16x16x32_bf16 v[132:135], a[60:63], a[232:235], v[132:135]\n"
    "ds_read_b64_tr_b16 a[22:23], v227 offset:416\n"
    "v_mov_b32 v173, 0\n"
    "v_mfma_f32_16x16x32_bf16 v[140:143], a[60:63], a[236:239], v[140:143]\n"
    "s_waitcnt lgkmcnt(8)\n"
    "buffer_load_dwordx4 v234, s[56:59], s73 offen offset:2112 lds\n"
    "buffer_load_dwordx4 v235, s[56:59], s73 offen offset:3168 lds\n"
    "s_add_u32 s69, s69, 16896\n"
    "s_cmp_eq_u32 s69, 152064\n"
    "s_cselect_b32 s69, 0, s69\n"
    "s_add_u32 s73, s73, s74\n"
    "s_mov_b32 s75, 6\n"
    "LOL:\n"
    "v_mfma_f32_16x16x32_bf16 v[96:99], a[0:3], a[240:243], v[96:99]\n"
    "ds_read_b64_tr_b16 a[24:25], v227 offset:320\n"
    "s_mul_i32 s89, s93, 196608\n"
    "v_mfma_f32_16x16x32_bf16 v[104:107], a[0:3], a[244:247], v[104:107]\n"
    "ds_read_b64_tr_b16 a[26:27], v227 offset:448\n"
    "s_add_u32 s89, s89, s94\n"
    "v_mfma_f32_16x16x32_bf16 v[112:115], a[0:3], a[248:251], v[112:115]\n"
    "s_add_u32 s90, s89, 4096\n"
    "s_mov_b32 s82, s93\n"
    "v_mfma_f32_16x16x32_bf16 v[100:103], a[4:7], a[240:243], v[100:103]\n"
    "ds_read_b64_tr_b16 a[28:29], v227 offset:352\n"
    "s_add_u32 s93, s93, 1\n"
    "v_mfma_f32_16x16x32_bf16 v[108:111], a[4:7], a[244:247], v[108:111]\n"
    "ds_read_b64_tr_b16 a[30:31], v227 offset:480\n"
    "v_mfma_f32_16x16x32_bf16 v[116:119], a[4:7], a[248:251], v[116:119]\n"
    "s_waitcnt lgkmcnt(8)\n"
    "v_mfma_f32_16x16x32_bf16 v[120:123], a[8:11], a[240:243], v[120:123]\n"
    "ds_read_b64_tr_b16 a[32:33], v227 offset:512\n"
    "v_mfma_f32_16x16x32_bf16 v[128:131], a[8:11], a[244:247], v[128:131]\n"
    "ds_read_b64_tr_b16 a[34:35], v227 offset:640\n"
    "v_mfma_f32_16x16x32_bf16 v[136:139], a[8:11], a[248:251], v[136:139]\n"
    "s_cmp_eq_u32 s93, 7\n"
    "s_cselect_b32 s93, 0, s93\n"
    "v_mfma_f32_16x16x32_bf16 v[124:127], a[12:15], a[240:243], v[124:127]\n"
    "ds_read_b64_tr_b16 a[36:37], v227 offset:544\n"
    "v_mfma_f32_16x16x32_bf16 v[132:135], a[12:15], a[244:247], v[132:135]\n"
    "ds_read_b64_tr_b16 a[38:39], v227 offset:672\n"
    "v_mfma_f32_16x16x32_bf16 v[140:143], a[12:15], a[248:251], v[140:143]\n"
    "s_waitcnt lgkmcnt(8)\n"
    "v_mfma_f32_16x16x32_bf16 v[96:99], a[16:19], a[252:255], v[96:99]\n"
    "ds_read_b64_tr_b16 a[40:41], v227 offset:576\n"
    "v_mfma_f32_16x16x32_bf16 v[104:107], a[16:19], v[192:195], v[104:107]\n"
    "ds_read_b64_tr_b16 a[42:43], v227 offset:704\n"
    "v_mfma_f32_16x16x32_bf16 v[112:115], a[16:19], v[196:199], v[112:115]\n"
    "v_mfma_f32_16x16x32_bf16 v[100:103], a[20:23], a[252:255], v[100:103]\n"
    "ds_read_b64_tr_b16 a[44:45], v227 offset:608\n"
    "v_mfma_f32_16x16x32_bf16 v[108:111], a[20:23], v[192:195], v[108:111]\n"
    "ds_read_b64_tr_b16 a[46:47], v227 offset:736\n"
    "v_mfma_f32_16x16x32_bf16 v[116:119], a[20:23], v[196:199], v[116:119]\n"
    "s_waitcnt lgkmcnt(8)\n"
    "v_mfma_f32_16x16x32_bf16 v[120:123], a[24:27], a[252:255], v[120:123]\n"
    "ds_read_b64_tr_b16 a[48:49], v227 offset:768\n"
    "v_mfma_f32_16x16x32_bf16 v[128:131], a[24:27], v[192:195], v[128:131]\n"
    "ds_read_b64_tr_b16 a[50:51], v227 offset:896\n"
    "v_mfma_f32_16x16x32_bf16 v[136:139], a[24:27], v[196:199], v[136:139]\n"
    "v_mfma_f32_16x16x32_bf16 v[124:127], a[28:31], a[252:255], v[124:127]\n"
    "ds_read_b64_tr_b16 a[52:53], v227 offset:800\n"
    "v_mfma_f32_16x16x32_bf16 v[132:135], a[28:31], v[192:195], v[132:135]\n"
    "ds_read_b64_tr_b16 a[54:55], v227 offset:928\n"
    "v_mfma_f32_16x16x32_bf16 v[140:143], a[28:31], v[196:199], v[140:143]\n"
    "s_waitcnt lgkmcnt(8)\n"
    "v_mfma_f32_16x16x32_bf16 v[96:99], a[32:35], v[200:203], v[96:99]\n"
    "ds_read_b64_tr_b16 a[56:57], v227 offset:832\n"
    "v_mfma_f32_16x16x32_bf16 v[104:107], a[32:35], v[204:207], v[104:107]\n"
    "ds_read_b64_tr_b16 a[58:59], v227 offset:960\n"
    "v_mfma_f32_16x16x32_bf16 v[112:115], a[32:35], v[208:211], v[112:115]\n"
    "v_mfma_f32_16x16x32_bf16 v[100:103], a[36:39], v[200:203], v[100:103]\n"
    "ds_read_b64_tr_b16 a[60:61], v227 offset:864\n"
    "v_mfma_f32_16x16x32_bf16 v[108:111], a[36:39], v[204:207], v[108:111]\n"
    "ds_read_b64_tr_b16 a[62:63], v227 offset:992\n"
    "v_mfma_f32_16x16x32_bf16 v[116:119], a[36:39], v[208:211], v[116:119]\n"
    "s_waitcnt lgkmcnt(8)\n"
    "s_waitcnt vmcnt(12)\n"
    "s_barrier\n"
    "v_add_u32 v227, s68, v225\n"
    "v_mfma_f32_16x16x32_bf16 v[120:123], a[40:43], v[200:203], v[120:123]\n"
    "ds_read_b64_tr_b16 a[0:1], v227\n"
    "s_add_u32 s68, s68, 16896\n"
    "v_mfma_f32_16x16x32_bf16 v[128:131], a[40:43], v[204:207], v[128:131]\n"
    "ds_read_b64_tr_b16 a[2:3], v227 offset:128\n"
    "v_mfma_f32_16x16x32_bf16 v[136:139], a[40:43], v[208:211], v[136:139]\n"
    "s_cmp_eq_u32 s68, 152064\n"
    "s_cselect_b32 s68, 0, s68\n"
    "v_mfma_f32_16x16x32_bf16 v[124:127], a[44:47], v[200:203], v[124:127]\n"
    "ds_read_b64_tr_b16 a[4:5], v227 offset:32\n"
    "v_mfma_f32_16x16x32_bf16 v[132:135], a[44:47], v[204:207], v[132:135]\n"
    "ds_read_b64_tr_b16 a[6:7], v227 offset:160\n"
    "v_mfma_f32_16x16x32_bf16 v[140:143], a[44:47], v[208:211], v[140:143]\n"
    "s_waitcnt lgkmcnt(8)\n"
    "v_mfma_f32_16x16x32_bf16 v[96:99], a[48:51], v[212:215], v[96:99]\n"
    "ds_read_b64_tr_b16 a[8:9], v227 offset:64\n"
    "v_mfma_f32_16x16x32_bf16 v[104:107], a[48:51], v[216:219], v[104:107]\n"
    "ds_read_b64_tr_b16 a[10:11], v227 offset:192\n"
    "v_mfma_f32_16x16x32_bf16 v[112:115], a[48:51], v[220:223], v[112:115]\n"
    "v_mfma_f32_16x16x32_bf16 v[100:103], a[52:55], v[212:215], v[100:103]\n"
    "ds_read_b64_tr_b16 a[12:13], v227 offset:96\n"
    "v_mfma_f32_16x16x32_bf16 v[108:111], a[52:55], v[216:219], v[108:111]\n"
    "ds_read_b64_tr_b16 a[14:15], v227 offset:224\n"
    "v_mfma_f32_16x16x32_bf16 v[116:119], a[52:55], v[220:223], v[116:119]\n"
    "s_waitcnt lgkmcnt(8)\n"
    "v_mfma_f32_16x16x32_bf16 v[120:123], a[56:59], v[212:215], v[120:123]\n"
    "ds_read_b64_tr_b16 a[16:17], v227 offset:256\n"
    "s_lshl_b32 s79, s38, 4\n"
    "v_mfma_f32_16x16x32_bf16 v[128:131], a[56:59], v[216:219], v[128:131]\n"
    "ds_read_b64_tr_b16 a[18:19], v227 offset:384\n"
    "v_lshlrev_b32 v241, 7, v187\n"
    "v_mfma_f32_16x16x32_bf16 v[136:139], a[56:59], v[220:223], v[136:139]\n"
    "v_cmp_eq_u32 vcc, s79, v187\n"
    "v_mfma_f32_16x16x32_bf16 v[124:127], a[60:63], v[212:215], v[124:127]\n"
    "ds_read_b64_tr_b16 a[20:21], v227 offset:288\n"
    "v_mov_b32 v242, 0x7fff0000\n"
    "v_mfma_f32_16x16x32_bf16 v[132:135], a[60:63], v[216:219], v[132:135]\n"
    "ds_read_b64_tr_b16 a[22:23], v227 offset:416\n"
    "v_cndmask_b32 v241, v242, v241, vcc\n"
    "v_mfma_f32_16x16x32_bf16 v[140:143], a[60:63], v[220:223], v[140:143]\n"
    "s_waitcnt lgkmcnt(8)\n"
    "v_mfma_f32_16x16x32_bf16 v[96:99], a[0:3], v[0:3], v[96:99]\n"
    "ds_read_b64_tr_b16 a[24:25], v227 offset:320\n"
    "v_mfma_f32_16x16x32_bf16 v[104:107], a[0:3], v[4:7], v[104:107]\n"
    "ds_read_b64_tr_b16 a[26:27], v227 offset:448\n"
    "v_mfma_f32_16x16x32_bf16 v[112:115], a[0:3], v[8:11], v[112:115]\n"
    "v_mfma_f32_16x16x32_bf16 v[100:103], a[4:7], v[0:3], v[100:103]\n"
    "ds_read_b64_tr_b16 a[28:29], v227 offset:352\n"
    "v_mfma_f32_16x16x32_bf16 v[108:111], a[4:7], v[4:7], v[108:111]\n"
    "ds_read_b64_tr_b16 a[30:31], v227 offset:480\n"
    "v_mfma_f32_16x16x32_bf16 v[116:119], a[4:7], v[8:11], v[116:119]\n"
    "s_waitcnt lgkmcnt(8)\n"
    "v_mfma_f32_16x16x32_bf16 v[120:123], a[8:11], v[0:3], v[120:123]\n"
    "ds_read_b64_tr_b16 a[32:33], v227 offset:512\n"
    "v_mfma_f32_16x16x32_bf16 v[128:131], a[8:11], v[4:7], v[128:131]\n"
    "ds_read_b64_tr_b16 a[34:35], v227 offset:640\n"
    "v_mfma_f32_16x16x32_bf16 v[136:139], a[8:11], v[8:11], v[136:139]\n"
    "s_waitcnt vmcnt(0)\n"
    "buffer_store_dword v236, v241, s[48:51], s36 offen sc1\n"
    "v_mfma_f32_16x16x32_bf16 v[124:127], a[12:15], v[0:3], v[124:127]\n"
    "ds_read_b64_tr_b16 a[36:37], v227 offset:544\n"
    "s_mov_b32 s80, s91\n"
    "v_mfma_f32_16x16x32_bf16 v[132:135], a[12:15], v[4:7], v[132:135]\n"
    "ds_read_b64_tr_b16 a[38:39], v227 offset:672\n"
    "s_add_u32 s91, s91, 1\n"
    "v_mfma_f32_16x16x32_bf16 v[140:143], a[12:15], v[8:11], v[140:143]\n"
    "s_waitcnt lgkmcnt(8)\n"
    "v_mfma_f32_16x16x32_bf16 v[96:99], a[16:19], v[12:15], v[96:99]\n"
    "ds_read_b64_tr_b16 a[40:41], v227 offset:576\n"
    "v_mfma_f32_16x16x32_bf16 v[104:107], a[16:19], v[16:19], v[104:107]\n"
    "ds_read_b64_tr_b16 a[42:43], v227 offset:704\n"
    "v_mfma_f32_16x16x32_bf16 v[112:115], a[16:19], v[20:23], v[112:115]\n"
    "s_cmp_eq_u32 s91, 7\n"
    "s_cselect_b32 s91, 0, s91\n"
    "v_mfma_f32_16x16x32_bf16 v[100:103], a[20:23], v[12:15], v[100:103]\n"
    "ds_read_b64_tr_b16 a[44:45], v227 offset:608\n"
    "s_sub_u32 s81, s91, s80\n"
    "v_mfma_f32_16x16x32_bf16 v[108:111], a[20:23], v[16:19], v[108:111]\n"
    "ds_read_b64_tr_b16 a[46:47], v227 offset:736\n"
    "s_lshl_b32 s81, s81, 7\n"
    "v_mfma_f32_16x16x32_bf16 v[116:119], a[20:23], v[20:23], v[116:119]\n"
    "s_waitcnt lgkmcnt(8)\n"
    "s_sub_u32 s81, s81, 344064\n"
    "v_mfma_f32_16x16x32_bf16 v[120:123], a[24:27], v[12:15], v[120:123]\n"
    "ds_read_b64_tr_b16 a[48:49], v227 offset:768\n"
    "s_add_u32 m0, s69, s70\n"
    "v_mfma_f32_16x16x32_bf16 v[128:131], a[24:27], v[16:19], v[128:131]\n"
    "ds_read_b64_tr_b16 a[50:51], v227 offset:896\n"
    "v_mfma_f32_16x16x32_bf16 v[136:139], a[24:27], v[20:23], v[136:139]\n"
    "buffer_load_dwordx4 v232, s[56:59], s73 offen lds\n"
    "v_mfma_f32_16x16x32_bf16 v[124:127], a[28:31], v[12:15], v[124:127]\n"
    "ds_read_b64_tr_b16 a[52:53], v227 offset:800\n"
    "v_mfma_f32_16x16x32_bf16 v[132:135], a[28:31], v[16:19], v[132:135]\n"
    "ds_read_b64_tr_b16 a[54:55], v227 offset:928\n"
    "v_mfma_f32_16x16x32_bf16 v[140:143], a[28:31], v[20:23], v[140:143]\n"
    "s_waitcnt lgkmcnt(8)\n"
    "v_mfma_f32_16x16x32_bf16 v[96:99], a[32:35], v[24:27], v[96:99]\n"
    "ds_read_b64_tr_b16 a[56:57], v227 offset:832\n"
    "v_mfma_f32_16x16x32_bf16 v[104:107], a[32:35], v[28:31], v[104:107]\n"
    "ds_read_b64_tr_b16 a[58:59], v227 offset:960\n"
    "v_mfma_f32_16x16x32_bf16 v[112:115], a[32:35], v[32:35], v[112:115]\n"
    "buffer_load_dwordx4 v233, s[56:59], s73 offen offset:1056 lds\n"
    "v_mfma_f32_16x16x32_bf16 v[100:103], a[36:39], v[24:27], v[100:103]\n"
    "ds_read_b64_tr_b16 a[60:61], v227 offset:864\n"
    "v_mfma_f32_16x16x32_bf16 v[108:111], a[36:39], v[28:31], v[108:111]\n"
    "ds_read_b64_tr_b16 a[62:63], v227 offset:992\n"
    "v_mfma_f32_16x16x32_bf16 v[116:119], a[36:39], v[32:35], v[116:119]\n"
    "s_waitcnt lgkmcnt(8)\n"
    "v_add_u32 v227, s68, v225\n"
    "v_mfma_f32_16x16x32_bf16 v[120:123], a[40:43], v[24:27], v[120:123]\n"
    "ds_read_b64_tr_b16 a[0:1], v227\n"
    "s_add_u32 s68, s68, 16896\n"
    "v_mfma_f32_16x16x32_bf16 v[128:131], a[40:43], v[28:31], v[128:131]\n"
    "ds_read_b64_tr_b16 a[2:3], v227 offset:128\n"
    "v_mfma_f32_16x16x32_bf16 v[136:139], a[40:43], v[32:35], v[136:139]\n"
    "buffer_load_dwordx4 v234, s[56:59], s73 offen offset:2112 lds\n"
    "v_mfma_f32_16x16x32_bf16 v[124:127], a[44:47], v[24:27], v[124:127]\n"
    "ds_read_b64_tr_b16 a[4:5], v227 offset:32\n"
    "v_mfma_f32_16x16x32_bf16 v[132:135], a[44:47], v[28:31], v[132:135]\n"
    "ds_read_b64_tr_b16 a[6:7], v227 offset:160\n"
    "v_mfma_f32_16x16x32_bf16 v[140:143], a[44:47], v[32:35], v[140:143]\n"
    "s_waitcnt lgkmcnt(8)\n"
    "v_mfma_f32_16x16x32_bf16 v[96:99], a[48:51], v[36:39], v[96:99]\n"
    "ds_read_b64_tr_b16 a[8:9], v227 offset:64\n"
    "v_mfma_f32_16x16x32_bf16 v[104:107], a[48:51], v[40:43], v[104:107]\n"
    "ds_read_b64_tr_b16 a[10:11], v227 offset:192\n"
    "v_mfma_f32_16x16x32_bf16 v[112:115], a[48:51], v[44:47], v[112:115]\n"
    "buffer_load_dwordx4 v235, s[56:59], s73 offen offset:3168 lds\n"
    "s_add_u32 s69, s69, 16896\n"
    "v_mfma_f32_16x16x32_bf16 v[100:103], a[52:55], v[36:39], v[100:103]\n"
    "ds_read_b64_tr_b16 a[12:13], v227 offset:96\n"
    "v_mfma_f32_16x16x32_bf16 v[108:111], a[52:55], v[40:43], v[108:111]\n"
    "ds_read_b64_tr_b16 a[14:15], v227 offset:224\n"
    "v_mfma_f32_16x16x32_bf16 v[116:119], a[52:55], v[44:47], v[116:119]\n"
    "s_waitcnt lgkmcnt(8)\n"
    "v_mfma_f32_16x16x32_bf16 v[120:123], a[56:59], v[36:39], v[120:123]\n"
    "ds_read_b64_tr_b16 a[16:17], v227 offset:256\n"
    "v_mfma_f32_16x16x32_bf16 v[128:131], a[56:59], v[40:43], v[128:131]\n"
    "ds_read_b64_tr_b16 a[18:19], v227 offset:384\n"
    "v_mfma_f32_16x16x32_bf16 v[136:139], a[56:59], v[44:47], v[136:139]\n"
    "s_cmp_eq_u32 s69, 152064\n"
    "s_cselect_b32 s69, 0, s69\n"
    "v_mfma_f32_16x16x32_bf16 v[124:127], a[60:63], v[36:39], v[124:127]\n"
    "ds_read_b64_tr_b16 a[20:21], v227 offset:288\n"
    "s_add_u32 s73, s73, s81\n"
    "v_mfma_f32_16x16x32_bf16 v[132:135], a[60:63], v[40:43], v[132:135]\n"
    "ds_read_b64_tr_b16 a[22:23], v227 offset:416\n"
    "s_add_u32 m0, s69, s70\n"
    "v_mfma_f32_16x16x32_bf16 v[140:143], a[60:63], v[44:47], v[140:143]\n"
    "s_waitcnt lgkmcnt(8)\n"
    "buffer_load_dwordx4 v232, s[56:59], s73 offen lds\n"
    "v_mfma_f32_16x16x32_bf16 v[96:99], a[0:3], v[48:51], v[96:99]\n"
    "ds_read_b64_tr_b16 a[24:25], v227 offset:320\n"
    "v_mfma_f32_16x16x32_bf16 v[104:107], a[0:3], v[52:55], v[104:107]\n"
    "ds_read_b64_tr_b16 a[26:27], v227 offset:448\n"
    "v_mfma_f32_16x16x32_bf16 v[112:115], a[0:3], v[56:59], v[112:115]\n"
    "s_cmp_eq_u32 s68, 152064\n"
    "s_cselect_b32 s68, 0, s68\n"
    "v_mfma_f32_16x16x32_bf16 v[100:103], a[4:7], v[48:51], v[100:103]\n"
    "ds_read_b64_tr_b16 a[28:29], v227 offset:352\n"
    "v_mfma_f32_16x16x32_bf16 v[108:111], a[4:7], v[52:55], v[108:111]\n"
    "ds_read_b64_tr_b16 a[30:31], v227 offset:480\n"
    "v_mfma_f32_16x16x32_bf16 v[116:119], a[4:7], v[56:59], v[116:119]\n"
    "s_waitcnt lgkmcnt(8)\n"
    "buffer_load_dwordx4 v233, s[56:59], s73 offen offset:1056 lds\n"
    "v_mfma_f32_16x16x32_bf16 v[120:123], a[8:11], v[48:51], v[120:123]\n"
    "ds_read_b64_tr_b16 a[32:33], v227 offset:512\n"
    "v_mfma_f32_16x16x32_bf16 v[128:131], a[8:11], v[52:55], v[128:131]\n"
    "ds_read_b64_tr_b16 a[34:35], v227 offset:640\n"
    "v_mfma_f32_16x16x32_bf16 v[136:139], a[8:11], v[56:59], v[136:139]\n"
    "buffer_load_dword v178, v174, s[48:51], s37 offen sc1\n"
    "v_mfma_f32_16x16x32_bf16 v[124:127], a[12:15], v[48:51], v[124:127]\n"
    "ds_read_b64_tr_b16 a[36:37], v227 offset:544\n"
    "v_mfma_f32_16x16x32_bf16 v[132:135], a[12:15], v[52:55], v[132:135]\n"
    "ds_read_b64_tr_b16 a[38:39], v227 offset:672\n"
    "v_mfma_f32_16x16x32_bf16 v[140:143], a[12:15], v[56:59], v[140:143]\n"
    "s_waitcnt lgkmcnt(8)\n"
    "buffer_load_dwordx4 v234, s[56:59], s73 offen offset:2112 lds\n"
    "v_mfma_f32_16x16x32_bf16 v[96:99], a[16:19], v[60:63], v[96:99]\n"
    "ds_read_b64_tr_b16 a[40:41], v227 offset:576\n"
    "v_mfma_f32_16x16x32_bf16 v[104:107], a[16:19], v[64:67], v[104:107]\n"
    "ds_read_b64_tr_b16 a[42:43], v227 offset:704\n"
    "v_mfma_f32_16x16x32_bf16 v[112:115], a[16:19], v[68:71], v[112:115]\n"
    "v_mfma_f32_16x16x32_bf16 v[100:103], a[20:23], v[60:63], v[100:103]\n"
    "ds_read_b64_tr_b16 a[44:45], v227 offset:608\n"
    "v_mfma_f32_16x16x32_bf16 v[108:111], a[20:23], v[64:67], v[108:111]\n"
    "ds_read_b64_tr_b16 a[46:47], v227 offset:736\n"
    "v_mfma_f32_16x16x32_bf16 v[116:119], a[20:23], v[68:71], v[116:119]\n"
    "s_waitcnt lgkmcnt(8)\n"
    "buffer_load_dwordx4 v235, s[56:59], s73 offen offset:3168 lds\n"
    "v_mfma_f32_16x16x32_bf16 v[120:123], a[24:27], v[60:63], v[120:123]\n"
    "ds_read_b64_tr_b16 a[48:49], v227 offset:768\n"
    "s_add_u32 s69, s69, 16896\n"
    "v_mfma_f32_16x16x32_bf16 v[128:131], a[24:27], v[64:67], v[128:131]\n"
    "ds_read_b64_tr_b16 a[50:51], v227 offset:896\n"
    "v_mfma_f32_16x16x32_bf16 v[136:139], a[24:27], v[68:71], v[136:139]\n"
    "s_cmp_eq_u32 s69, 152064\n"
    "s_cselect_b32 s69, 0, s69\n"
    "v_mfma_f32_16x16x32_bf16 v[124:127], a[28:31], v[60:63], v[124:127]\n"
    "ds_read_b64_tr_b16 a[52:53], v227 offset:800\n"
    "s_add_u32 s73, s73, s74\n"
    "v_mfma_f32_16x16x32_bf16 v[132:135], a[28:31], v[64:67], v[132:135]\n"
    "ds_read_b64_tr_b16 a[54:55], v227 offset:928\n"
    "v_mfma_f32_16x16x32_bf16 v[140:143], a[28:31], v[68:71], v[140:143]\n"
    "s_waitcnt lgkmcnt(8)\n"
    "v_mfma_f32_16x16x32_bf16 v[96:99], a[32:35], v[72:75], v[96:99]\n"
    "ds_read_b64_tr_b16 a[56:57], v227 offset:832\n"
    "v_mfma_f32_16x16x32_bf16 v[104:107], a[32:35], v[76:79], v[104:107]\n"
    "ds_read_b64_tr_b16 a[58:59], v227 offset:960\n"
    "v_mfma_f32_16x16x32_bf16 v[112:115], a[32:35], v[80:83], v[112:115]\n"
    "v_mfma_f32_16x16x32_bf16 v[100:103], a[36:39], v[72:75], v[100:103]\n"
    "ds_read_b64_tr_b16 a[60:61], v227 offset:864\n"
    "v_mfma_f32_16x16x32_bf16 v[108:111], a[36:39], v[76:79], v[108:111]\n"
    "ds_read_b64_tr_b16 a[62:63], v227 offset:992\n"
    "v_mfma_f32_16x16x32_bf16 v[116:119], a[36:39], v[80:83], v[116:119]\n"
    "s_waitcnt lgkmcnt(8)\n"
    "s_barrier\n"
    "v_add_u32 v227, s68, v225\n"
    "v_mfma_f32_16x16x32_bf16 v[120:123], a[40:43], v[72:75], v[120:123]\n"
    "ds_read_b64_tr_b16 a[0:1], v227\n"
    "s_add_u32 s68, s68, 16896\n"
    "v_mfma_f32_16x16x32_bf16 v[128:131], a[40:43], v[76:79], v[128:131]\n"
    "ds_read_b64_tr_b16 a[2:3], v227 offset:128\n"
    "v_mfma_f32_16x16x32_bf16 v[136:139], a[40:43], v[80:83], v[136:139]\n"
    "s_cmp_eq_u32 s68, 152064\n"
    "s_cselect_b32 s68, 0, s68\n"
    "v_mfma_f32_16x16x32_bf16 v[124:127], a[44:47], v[72:75], v[124:127]\n"
    "ds_read_b64_tr_b16 a[4:5], v227 offset:32\n"
    "s_add_u32 m0, s69, s70\n"
    "v_mfma_f32_16x16x32_bf16 v[132:135], a[44:47], v[76:79], v[132:135]\n"
    "ds_read_b64_tr_b16 a[6:7], v227 offset:160\n"
    "v_mfma_f32_16x16x32_bf16 v[140:143], a[44:47], v[80:83], v[140:143]\n"
    "s_waitcnt lgkmcnt(8)\n"
    "buffer_load_dwordx4 v232, s[56:59], s73 offen lds\n"
    "v_mfma_f32_16x16x32_bf16 v[96:99], a[48:51], v[84:87], v[96:99]\n"
    "ds_read_b64_tr_b16 a[8:9], v227 offset:64\n"
    "v_mfma_f32_16x16x32_bf16 v[104:107], a[48:51], v[88:91], v[104:107]\n"
    "ds_read_b64_tr_b16 a[10:11], v227 offset:192\n"
    "v_mfma_f32_16x16x32_bf16 v[112:115], a[48:51], v[92:95], v[112:115]\n"
    "v_mfma_f32_16x16x32_bf16 v[100:103], a[52:55], v[84:87], v[100:103]\n"
    "ds_read_b64_tr_b16 a[12:13], v227 offset:96\n"
    "v_mfma_f32_16x16x32_bf16 v[108:111], a[52:55], v[88:91], v[108:111]\n"
    "ds_read_b64_tr_b16 a[14:15], v227 offset:224\n"
    "v_mfma_f32_16x16x32_bf16 v[116:119], a[52:55], v[92:95], v[116:119]\n"
    "s_waitcnt lgkmcnt(8)\n"
    "buffer_load_dwordx4 v233, s[56:59], s73 offen offset:1056 lds\n"
    "v_mfma_f32_16x16x32_bf16 v[120:123], a[56:59], v[84:87], v[120:123]\n"
    "ds_read_b64_tr_b16 a[16:17], v227 offset:256\n"
    "v_cvt_pk_bf16_f32 v144, v96, v97\n"
    "v_mfma_f32_16x16x32_bf16 v[128:131], a[56:59], v[88:91], v[128:131]\n"
    "ds_read_b64_tr_b16 a[18:19], v227 offset:384\n"
    "v_cvt_pk_bf16_f32 v145, v98, v99\n"
    "v_mfma_f32_16x16x32_bf16 v[136:139], a[56:59], v[92:95], v[136:139]\n"
    "v_cvt_pk_bf16_f32 v148, v104, v105\n"
    "v_cvt_pk_bf16_f32 v149, v106, v107\n"
    "v_mfma_f32_16x16x32_bf16 v[124:127], a[60:63], v[84:87], v[124:127]\n"
    "ds_read_b64_tr_b16 a[20:21], v227 offset:288\n"
    "v_cvt_pk_bf16_f32 v152, v112, v113\n"
    "v_mfma_f32_16x16x32_bf16 v[132:135], a[60:63], v[88:91], v[132:135]\n"
    "ds_read_b64_tr_b16 a[22:23], v227 offset:416\n"
    "v_cvt_pk_bf16_f32 v153, v114, v115\n"
    "v_mfma_f32_16x16x32_bf16 v[140:143], a[60:63], v[92:95], v[140:143]\n"
    "s_waitcnt lgkmcnt(8)\n"
    "buffer_load_dwordx4 v234, s[56:59], s73 offen offset:2112 lds\n"
    "v_cvt_pk_bf16_f32 v156, v120, v121\n"
    "v_mfma_f32_16x16x32_bf16 v[96:99], a[0:3], a[192:195], 0\n"
    "ds_read_b64_tr_b16 a[24:25], v227 offset:320\n"
    "v_cvt_pk_bf16_f32 v146, v100, v101\n"
    "v_cvt_pk_bf16_f32 v157, v122, v123\n"
    "v_mfma_f32_16x16x32_bf16 v[104:107], a[0:3], a[196:199], 0\n"
    "ds_read_b64_tr_b16 a[26:27], v227 offset:448\n"
    "v_cvt_pk_bf16_f32 v147, v102, v103\n"
    "v_cvt_pk_bf16_f32 v160, v128, v129\n"
    "v_mfma_f32_16x16x32_bf16 v[112:115], a[0:3], a[200:203], 0\n"
    "v_cvt_pk_bf16_f32 v150, v108, v109\n"
    "v_cvt_pk_bf16_f32 v151, v110, v111\n"
    "v_cvt_pk_bf16_f32 v161, v130, v131\n"
    "v_mfma_f32_16x16x32_bf16 v[100:103], a[4:7], a[192:195], 0\n"
    "ds_read_b64_tr_b16 a[28:29], v227 offset:352\n"
    "v_cvt_pk_bf16_f32 v154, v116, v117\n"
    "v_cvt_pk_bf16_f32 v158, v124, v125\n"
    "v_mfma_f32_16x16x32_bf16 v[108:111], a[4:7], a[196:199], 0\n"
    "ds_read_b64_tr_b16 a[30:31], v227 offset:480\n"
    "v_cvt_pk_bf16_f32 v155, v118, v119\n"
    "v_cvt_pk_bf16_f32 v159, v126, v127\n"
    "v_mfma_f32_16x16x32_bf16 v[116:119], a[4:7], a[200:203], 0\n"
    "s_waitcnt lgkmcnt(8)\n"
    "buffer_load_dwordx4 v235, s[56:59], s73 offen offset:3168 lds\n"
    "v_cvt_pk_bf16_f32 v162, v132, v133\n"
    "v_mfma_f32_16x16x32_bf16 v[120:123], a[8:11], a[192:195], 0\n"
    "ds_read_b64_tr_b16 a[32:33], v227 offset:512\n"
    "s_add_u32 s69, s69, 16896\n"
    "v_cvt_pk_bf16_f32 v163, v134, v135\n"
    "v_mfma_f32_16x16x32_bf16 v[128:131], a[8:11], a[196:199], 0\n"
    "ds_read_b64_tr_b16 a[34:35], v227 offset:640\n"
    "v_cvt_pk_bf16_f32 v164, v136, v137\n"
    "v_cvt_pk_bf16_f32 v165, v138, v139\n"
    "v_mfma_f32_16x16x32_bf16 v[136:139], a[8:11], a[200:203], 0\n"
    "s_cmp_eq_u32 s69, 152064\n"
    "s_cselect_b32 s69, 0, s69\n"
    "v_cvt_pk_bf16_f32 v166, v140, v141\n"
    "v_mfma_f32_16x16x32_bf16 v[124:127], a[12:15], a[192:195], 0\n"
    "ds_read_b64_tr_b16 a[36:37], v227 offset:544\n"
    "s_add_u32 s73, s73, s74\n"
    "v_cvt_pk_bf16_f32 v167, v142, v143\n"
    "v_mfma_f32_16x16x32_bf16 v[132:135], a[12:15], a[196:199], 0\n"
    "ds_read_b64_tr_b16 a[38:39], v227 offset:672\n"
    "s_add_u32 m0, s69, s70\n"
    "v_mfma_f32_16x16x32_bf16 v[140:143], a[12:15], a[200:203], 0\n"
    "s_waitcnt lgkmcnt(8)\n"
    "buffer_load_dwordx4 v232, s[56:59], s73 offen lds\n"
    "v_mfma_f32_16x16x32_bf16 v[96:99], a[16:19], a[204:207], v[96:99]\n"
    "ds_read_b64_tr_b16 a[40:41], v227 offset:576\n"
    "v_mfma_f32_16x16x32_bf16 v[104:107], a[16:19], a[208:211], v[104:107]\n"
    "ds_read_b64_tr_b16 a[42:43], v227 offset:704\n"
    "v_mfma_f32_16x16x32_bf16 v[112:115], a[16:19], a[212:215], v[112:115]\n"
    "buffer_store_dwordx4 v[144:147], v187, s[48:51], s89 offen sc1\n"
    "v_mfma_f32_16x16x32_bf16 v[100:103], a[20:23], a[204:207], v[100:103]\n"
    "ds_read_b64_tr_b16 a[44:45], v227 offset:608\n"
    "v_mfma_f32_16x16x32_bf16 v[108:111], a[20:23], a[208:211], v[108:111]\n"
    "ds_read_b64_tr_b16 a[46:47], v227 offset:736\n"
    "v_mfma_f32_16x16x32_bf16 v[116:119], a[20:23], a[212:215], v[116:119]\n"
    "s_waitcnt lgkmcnt(8)\n"
    "buffer_load_dwordx4 v233, s[56:59], s73 offen offset:1056 lds\n"
    "v_mfma_f32_16x16x32_bf16 v[120:123], a[24:27], a[204:207], v[120:123]\n"
    "ds_read_b64_tr_b16 a[48:49], v227 offset:768\n"
    "v_mfma_f32_16x16x32_bf16 v[128:131], a[24:27], a[208:211], v[128:131]\n"
    "ds_read_b64_tr_b16 a[50:51], v227 offset:896\n"
    "v_mfma_f32_16x16x32_bf16 v[136:139], a[24:27], a[212:215], v[136:139]\n"
    "buffer_store_dwordx4 v[148:151], v187, s[48:51], s89 offen offset:1024 sc1\n"
    "v_mfma_f32_16x16x32_bf16 v[124:127], a[28:31], a[204:207], v[124:127]\n"
    "ds_read_b64_tr_b16 a[52:53], v227 offset:800\n"
    "v_mfma_f32_16x16x32_bf16 v[132:135], a[28:31], a[208:211], v[132:135]\n"
    "ds_read_b64_tr_b16 a[54:55], v227 offset:928\n"
    "v_mfma_f32_16x16x32_bf16 v[140:143], a[28:31], a[212:215], v[140:143]\n"
    "s_waitcnt lgkmcnt(8)\n"
    "buffer_load_dwordx4 v234, s[56:59], s73 offen offset:2112 lds\n"
    "v_mfma_f32_16x16x32_bf16 v[96:99], a[32:35], a[216:219], v[96:99]\n"
    "ds_read_b64_tr_b16 a[56:57], v227 offset:832\n"
    "v_mfma_f32_16x16x32_bf16 v[104:107], a[32:35], a[220:223], v[104:107]\n"
    "ds_read_b64_tr_b16 a[58:59], v227 offset:960\n"
    "v_mfma_f32_16x16x32_bf16 v[112:115], a[32:35], a[224:227], v[112:115]\n"
    "buffer_store_dwordx4 v[152:155], v187, s[48:51], s89 offen offset:2048 sc1\n"
    "v_mfma_f32_16x16x32_bf16 v[100:103], a[36:39], a[216:219], v[100:103]\n"
    "ds_read_b64_tr_b16 a[60:61], v227 offset:864\n"
    "v_mfma_f32_16x16x32_bf16 v[108:111], a[36:39], a[220:223], v[108:111]\n"
    "ds_read_b64_tr_b16 a[62:63], v227 offset:992\n"
    "v_mfma_f32_16x16x32_bf16 v[116:119], a[36:39], a[224:227], v[116:119]\n"
    "s_waitcnt lgkmcnt(8)\n"
    "buffer_load_dwordx4 v235, s[56:59], s73 offen offset:3168 lds\n"
    "v_add_u32 v227, s68, v225\n"
    "v_mfma_f32_16x16x32_bf16 v[120:123], a[40:43], a[216:219], v[120:123]\n"
    "ds_read_b64_tr_b16 a[0:1], v227\n"
    "s_add_u32 s69, s69, 16896\n"
    "v_mfma_f32_16x16x32_bf16 v[128:131], a[40:43], a[220:223], v[128:131]\n"
    "ds_read_b64_tr_b16 a[2:3], v227 offset:128\n"
    "s_add_u32 s68, s68, 16896\n"
    "v_mfma_f32_16x16x32_bf16 v[136:139], a[40:43], a[224:227], v[136:139]\n"
    "s_cmp_eq_u32 s69, 152064\n"
    "s_cselect_b32 s69, 0, s69\n"
    "v_mfma_f32_16x16x32_bf16 v[124:127], a[44:47], a[216:219], v[124:127]\n"
    "ds_read_b64_tr_b16 a[4:5], v227 offset:32\n"
    "s_add_u32 s73, s73, s74\n"
    "v_mfma_f32_16x16x32_bf16 v[132:135], a[44:47], a[220:223], v[132:135]\n"
    "ds_read_b64_tr_b16 a[6:7], v227 offset:160\n"
    "v_mfma_f32_16x16x32_bf16 v[140:143], a[44:47], a[224:227], v[140:143]\n"
    "s_waitcnt lgkmcnt(8)\n"
    "buffer_store_dwordx4 v[156:159], v187, s[48:51], s89 offen offset:3072 sc1\n"
    "v_mfma_f32_16x16x32_bf16 v[96:99], a[48:51], a[228:231], v[96:99]\n"
    "ds_read_b64_tr_b16 a[8:9], v227 offset:64\n"
    "v_mfma_f32_16x16x32_bf16 v[104:107], a[48:51], a[232:235], v[104:107]\n"
    "ds_read_b64_tr_b16 a[10:11], v227 offset:192\n"
    "v_mfma_f32_16x16x32_bf16 v[112:115], a[48:51], a[236:239], v[112:115]\n"
    "s_cmp_eq_u32 s68, 152064\n"
    "s_cselect_b32 s68, 0, s68\n"
    "v_mfma_f32_16x16x32_bf16 v[100:103], a[52:55], a[228:231], v[100:103]\n"
    "ds_read_b64_tr_b16 a[12:13], v227 offset:96\n"
    "v_mfma_f32_16x16x32_bf16 v[108:111], a[52:55], a[232:235], v[108:111]\n"
    "ds_read_b64_tr_b16 a[14:15], v227 offset:224\n"
    "v_mfma_f32_16x16x32_bf16 v[116:119], a[52:55], a[236:239], v[116:119]\n"
    "s_waitcnt lgkmcnt(8)\n"
    "buffer_store_dwordx4 v[160:163], v187, s[48:51], s90 offen sc1\n"
    "v_mfma_f32_16x16x32_bf16 v[120:123], a[56:59], a[228:231], v[120:123]\n"
    "ds_read_b64_tr_b16 a[16:17], v227 offset:256\n"
    "v_mfma_f32_16x16x32_bf16 v[128:131], a[56:59], a[232:235], v[128:131]\n"
    "ds_read_b64_tr_b16 a[18:19], v227 offset:384\n"
    "v_mfma_f32_16x16x32_bf16 v[136:139], a[56:59], a[236:239], v[136:139]\n"
    "v_mfma_f32_16x16x32_bf16 v[124:127], a[60:63], a[228:231], v[124:127]\n"
    "ds_read_b64_tr_b16 a[20:21], v227 offset:288\n"
    "v_mfma_f32_16x16x32_bf16 v[132:135], a[60:63], a[232:235], v[132:135]\n"
    "ds_read_b64_tr_b16 a[22:23], v227 offset:416\n"
    "v_mfma_f32_16x16x32_bf16 v[140:143], a[60:63], a[236:239], v[140:143]\n"
    "s_waitcnt lgkmcnt(8)\n"
    "buffer_store_dwordx4 v[164:167], v187, s[48:51], s90 offen offset:1024 sc1\n"
    "s_mov_b32 s38, s82\n"
    "s_sub_u32 s75, s75, 1\n"
    "s_cmp_lg_u32 s75, 0\n"
    "s_cbranch_scc1 LOL\n"
    "s_waitcnt vmcnt(16)\n"
    "v_cmp_ne_u32 vcc, v178, v175\n"
    "s_nop 0\n"
    "s_cmp_eq_u64 vcc, 0\n"
    "s_cselect_b32 s55, 1, 0\n"
    "v_mfma_f32_16x16x32_bf16 v[96:99], a[0:3], a[240:243], v[96:99]\n"
    "ds_read_b64_tr_b16 a[24:25], v227 offset:320\n"
    "v_mfma_f32_16x16x32_bf16 v[104:107], a[0:3], a[244:247], v[104:107]\n"
    "ds_read_b64_tr_b16 a[26:27], v227 offset:448\n"
    "v_mfma_f32_16x16x32_bf16 v[112:115], a[0:3], a[248:251], v[112:115]\n"
    "v_mfma_f32_16x16x32_bf16 v[100:103], a[4:7], a[240:243], v[100:103]\n"
    "ds_read_b64_tr_b16 a[28:29], v227 offset:352\n"
    "v_mfma_f32_16x16x32_bf16 v[108:111], a[4:7], a[244:247], v[108:111]\n"
    "ds_read_b64_tr_b16 a[30:31], v227 offset:480\n"
    "v_mfma_f32_16x16x32_bf16 v[116:119], a[4:7], a[248:251], v[116:119]\n"
    "s_waitcnt lgkmcnt(8)\n"
    "v_mfma_f32_16x16x32_bf16 v[120:123], a[8:11], a[240:243], v[120:123]\n"
    "ds_read_b64_tr_b16 a[32:33], v227 offset:512\n"
    "v_mfma_f32_16x16x32_bf16 v[128:131], a[8:11], a[244:247], v[128:131]\n"
    "ds_read_b64_tr_b16 a[34:35], v227 offset:640\n"
    "v_mfma_f32_16x16x32_bf16 v[136:139], a[8:11], a[248:251], v[136:139]\n"
    "v_mfma_f32_16x16x32_bf16 v[124:127], a[12:15], a[240:243], v[124:127]\n"
    "ds_read_b64_tr_b16 a[36:37], v227 offset:544\n"
    "v_mfma_f32_16x16x32_bf16 v[132:135], a[12:15], a[244:247], v[132:135]\n"
    "ds_read_b64_tr_b16 a[38:39], v227 offset:672\n"
    "v_mfma_f32_16x16x32_bf16 v[140:143], a[12:15], a[248:251], v[140:143]\n"
    "s_waitcnt lgkmcnt(8)\n"
    "v_mfma_f32_16x16x32_bf16 v[96:99], a[16:19], a[252:255], v[96:99]\n"
    "ds_read_b64_tr_b16 a[40:41], v227 offset:576\n"
    "v_mfma_f32_16x16x32_bf16 v[104:107], a[16:19], v[192:195], v[104:107]\n"
    "ds_read_b64_tr_b16 a[42:43], v227 offset:704\n"
    "v_mfma_f32_16x16x32_bf16 v[112:115], a[16:19], v[196:199], v[112:115]\n"
    "v_mfma_f32_16x16x32_bf16 v[100:103], a[20:23], a[252:255], v[100:103]\n"
    "ds_read_b64_tr_b16 a[44:45], v227 offset:608\n"
    "v_mfma_f32_16x16x32_bf16 v[108:111], a[20:23], v[192:195], v[108:111]\n"
    "ds_read_b64_tr_b16 a[46:47], v227 offset:736\n"
    "v_mfma_f32_16x16x32_bf16 v[116:119], a[20:23], v[196:199], v[116:119]\n"
    "s_waitcnt lgkmcnt(8)\n"
    "v_mfma_f32_16x16x32_bf16 v[120:123], a[24:27], a[252:255], v[120:123]\n"
    "ds_read_b64_tr_b16 a[48:49], v227 offset:768\n"
    "v_mfma_f32_16x16x32_bf16 v[128:131], a[24:27], v[192:195], v[128:131]\n"
    "ds_read_b64_tr_b16 a[50:51], v227 offset:896\n"
    "v_mfma_f32_16x16x32_bf16 v[136:139], a[24:27], v[196:199], v[136:139]\n"
    "v_mfma_f32_16x16x32_bf16 v[124:127], a[28:31], a[252:255], v[124:127]\n"
    "ds_read_b64_tr_b16 a[52:53], v227 offset:800\n"
    "v_mfma_f32_16x16x32_bf16 v[132:135], a[28:31], v[192:195], v[132:135]\n"
    "ds_read_b64_tr_b16 a[54:55], v227 offset:928\n"
    "v_mfma_f32_16x16x32_bf16 v[140:143], a[28:31], v[196:199], v[140:143]\n"
    "s_waitcnt lgkmcnt(8)\n"
    "v_mfma_f32_16x16x32_bf16 v[96:99], a[32:35], v[200:203], v[96:99]\n"
    "ds_read_b64_tr_b16 a[56:57], v227 offset:832\n"
    "v_mfma_f32_16x16x32_bf16 v[104:107], a[32:35], v[204:207], v[104:107]\n"
    "ds_read_b64_tr_b16 a[58:59], v227 offset:960\n"
    "v_mfma_f32_16x16x32_bf16 v[112:115], a[32:35], v[208:211], v[112:115]\n"
    "v_mfma_f32_16x16x32_bf16 v[100:103], a[36:39], v[200:203], v[100:103]\n"
    "ds_read_b64_tr_b16 a[60:61], v227 offset:864\n"
    "s_lshl_b32 s79, s38, 4\n"
    "v_mfma_f32_16x16x32_bf16 v[108:111], a[36:39], v[204:207], v[108:111]\n"
    "ds_read_b64_tr_b16 a[62:63], v227 offset:992\n"
    "v_lshlrev_b32 v241, 7, v187\n"
    "v_mfma_f32_16x16x32_bf16 v[116:119], a[36:39], v[208:211], v[116:119]\n"
    "s_waitcnt lgkmcnt(8)\n"
    "v_cmp_eq_u32 vcc, s79, v187\n"
    "s_barrier\n"
    "v_add_u32 v227, s68, v225\n"
    "v_mfma_f32_16x16x32_bf16 v[120:123], a[40:43], v[200:203], v[120:123]\n"
    "ds_read_b64_tr_b16 a[0:1], v227\n"
    "v_mov_b32 v242, 0x7fff0000\n"
    "v_mfma_f32_16x16x32_bf16 v[128:131], a[40:43], v[204:207], v[128:131]\n"
    "ds_read_b64_tr_b16 a[2:3], v227 offset:128\n"
    "v_cndmask_b32 v241, v242, v241, vcc\n"
    "v_mfma_f32_16x16x32_bf16 v[136:139], a[40:43], v[208:211], v[136:139]\n"
    "s_add_u32 s68, s68, 16896\n"
    "v_mfma_f32_16x16x32_bf16 v[124:127], a[44:47], v[200:203], v[124:127]\n"
    "ds_read_b64_tr_b16 a[4:5], v227 offset:32\n"
    "v_mfma_f32_16x16x32_bf16 v[132:135], a[44:47], v[204:207], v[132:135]\n"
    "ds_read_b64_tr_b16 a[6:7], v227 offset:160\n"
    "v_mfma_f32_16x16x32_bf16 v[140:143], a[44:47], v[208:211], v[140:143]\n"
    "s_waitcnt lgkmcnt(8)\n"
    "v_mfma_f32_16x16x32_bf16 v[96:99], a[48:51], v[212:215], v[96:99]\n"
    "ds_read_b64_tr_b16 a[8:9], v227 offset:64\n"
    "v_mfma_f32_16x16x32_bf16 v[104:107], a[48:51], v[216:219], v[104:107]\n"
    "ds_read_b64_tr_b16 a[10:11], v227 offset:192\n"
    "v_mfma_f32_16x16x32_bf16 v[112:115], a[48:51], v[220:223], v[112:115]\n"
    "s_cmp_eq_u32 s68, 152064\n"
    "s_cselect_b32 s68, 0, s68\n"
    "v_mfma_f32_16x16x32_bf16 v[100:103], a[52:55], v[212:215], v[100:103]\n"
    "ds_read_b64_tr_b16 a[12:13], v227 offset:96\n"
    "v_mfma_f32_16x16x32_bf16 v[108:111], a[52:55], v[216:219], v[108:111]\n"
    "ds_read_b64_tr_b16 a[14:15], v227 offset:224\n"
    "v_mfma_f32_16x16x32_bf16 v[116:119], a[52:55], v[220:223], v[116:119]\n"
    "s_waitcnt lgkmcnt(8)\n"
    "s_waitcnt vmcnt(0)\n"
    "buffer_store_dword v236, v241, s[48:51], s36 offen sc1\n"
    "v_mfma_f32_16x16x32_bf16 v[120:123], a[56:59], v[212:215], v[120:123]\n"
    "ds_read_b64_tr_b16 a[16:17], v227 offset:256\n"
    "v_mfma_f32_16x16x32_bf16 v[128:131], a[56:59], v[216:219], v[128:131]\n"
    "ds_read_b64_tr_b16 a[18:19], v227 offset:384\n"
    "v_mfma_f32_16x16x32_bf16 v[136:139], a[56:59], v[220:223], v[136:139]\n"
    "buffer_load_dwordx4 a[64:67], v187, s[48:51], s40 offen sc1\n"
    "v_mfma_f32_16x16x32_bf16 v[124:127], a[60:63], v[212:215], v[124:127]\n"
    "ds_read_b64_tr_b16 a[20:21], v227 offset:288\n"
    "v_mfma_f32_16x16x32_bf16 v[132:135], a[60:63], v[216:219], v[132:135]\n"
    "ds_read_b64_tr_b16 a[22:23], v227 offset:416\n"
    "v_mfma_f32_16x16x32_bf16 v[140:143], a[60:63], v[220:223], v[140:143]\n"
    "s_waitcnt lgkmcnt(8)\n"
    "buffer_load_dwordx4 a[68:71], v187, s[48:51], s40 offen offset:1024 sc1\n"
    "v_mfma_f32_16x16x32_bf16 v[96:99], a[0:3], v[0:3], v[96:99]\n"
    "ds_read_b64_tr_b16 a[24:25], v227 offset:320\n"
    "v_mfma_f32_16x16x32_bf16 v[104:107], a[0:3], v[4:7], v[104:107]\n"
    "ds_read_b64_tr_b16 a[26:27], v227 offset:448\n"
    "v_mfma_f32_16x16x32_bf16 v[112:115], a[0:3], v[8:11], v[112:115]\n"
    "buffer_load_dwordx4 a[72:75], v187, s[48:51], s40 offen offset:2048 sc1\n"
    "v_mfma_f32_16x16x32_bf16 v[100:103], a[4:7], v[0:3], v[100:103]\n"
    "ds_read_b64_tr_b16 a[28:29], v227 offset:352\n"
    "v_mfma_f32_16x16x32_bf16 v[108:111], a[4:7], v[4:7], v[108:111]\n"
    "ds_read_b64_tr_b16 a[30:31], v227 offset:480\n"
    "v_mfma_f32_16x16x32_bf16 v[116:119], a[4:7], v[8:11], v[116:119]\n"
    "s_waitcnt lgkmcnt(8)\n"
    "buffer_load_dwordx4 a[76:79], v187, s[48:51], s40 offen offset:3072 sc1\n"
    "v_mfma_f32_16x16x32_bf16 v[120:123], a[8:11], v[0:3], v[120:123]\n"
    "ds_read_b64_tr_b16 a[32:33], v227 offset:512\n"
    "v_mfma_f32_16x16x32_bf16 v[128:131], a[8:11], v[4:7], v[128:131]\n"
    "ds_read_b64_tr_b16 a[34:35], v227 offset:640\n"
    "v_mfma_f32_16x16x32_bf16 v[136:139], a[8:11], v[8:11], v[136:139]\n"
    "buffer_load_dwordx4 a[80:83], v180, s[48:51], s40 offen sc1\n"
    "v_mfma_f32_16x16x32_bf16 v[124:127], a[12:15], v[0:3], v[124:127]\n"
    "ds_read_b64_tr_b16 a[36:37], v227 offset:544\n"
    "v_mfma_f32_16x16x32_bf16 v[132:135], a[12:15], v[4:7], v[132:135]\n"
    "ds_read_b64_tr_b16 a[38:39], v227 offset:672\n"
    "v_mfma_f32_16x16x32_bf16 v[140:143], a[12:15], v[8:11], v[140:143]\n"
    "s_waitcnt lgkmcnt(8)\n"
    "buffer_load_dwordx4 a[84:87], v180, s[48:51], s40 offen offset:1024 sc1\n"
    "v_mfma_f32_16x16x32_bf16 v[96:99], a[16:19], v[12:15], v[96:99]\n"
    "ds_read_b64_tr_b16 a[40:41], v227 offset:576\n"
    "v_mfma_f32_16x16x32_bf16 v[104:107], a[16:19], v[16:19], v[104:107]\n"
    "ds_read_b64_tr_b16 a[42:43], v227 offset:704\n"
    "v_mfma_f32_16x16x32_bf16 v[112:115], a[16:19], v[20:23], v[112:115]\n"
    "buffer_load_dwordx4 a[88:91], v187, s[48:51], s41 offen sc1\n"
    "v_mfma_f32_16x16x32_bf16 v[100:103], a[20:23], v[12:15], v[100:103]\n"
    "ds_read_b64_tr_b16 a[44:45], v227 offset:608\n"
    "v_mfma_f32_16x16x32_bf16 v[108:111], a[20:23], v[16:19], v[108:111]\n"
    "ds_read_b64_tr_b16 a[46:47], v227 offset:736\n"
    "v_mfma_f32_16x16x32_bf16 v[116:119], a[20:23], v[20:23], v[116:119]\n"
    "s_waitcnt lgkmcnt(8)\n"
    "buffer_load_dwordx4 a[92:95], v187, s[48:51], s41 offen offset:1024 sc1\n"
    "v_mfma_f32_16x16x32_bf16 v[120:123], a[24:27], v[12:15], v[120:123]\n"
    "ds_read_b64_tr_b16 a[48:49], v227 offset:768\n"
    "v_mfma_f32_16x16x32_bf16 v[128:131], a[24:27], v[16:19], v[128:131]\n"
    "ds_read_b64_tr_b16 a[50:51], v227 offset:896\n"
    "v_mfma_f32_16x16x32_bf16 v[136:139], a[24:27], v[20:23], v[136:139]\n"
    "buffer_load_dwordx4 a[96:99], v187, s[48:51], s41 offen offset:2048 sc1\n"
    "v_mfma_f32_16x16x32_bf16 v[124:127], a[28:31], v[12:15], v[124:127]\n"
    "ds_read_b64_tr_b16 a[52:53], v227 offset:800\n"
    "v_mfma_f32_16x16x32_bf16 v[132:135], a[28:31], v[16:19], v[132:135]\n"
    "ds_read_b64_tr_b16 a[54:55], v227 offset:928\n"
    "v_mfma_f32_16x16x32_bf16 v[140:143], a[28:31], v[20:23], v[140:143]\n"
    "s_waitcnt lgkmcnt(8)\n"
    "buffer_load_dwordx4 a[100:103], v187, s[48:51], s41 offen offset:3072 sc1\n"
    "v_mfma_f32_16x16x32_bf16 v[96:99], a[32:35], v[24:27], v[96:99]\n"
    "ds_read_b64_tr_b16 a[56:57], v227 offset:832\n"
    "v_mfma_f32_16x16x32_bf16 v[104:107], a[32:35], v[28:31], v[104:107]\n"
    "ds_read_b64_tr_b16 a[58:59], v227 offset:960\n"
    "v_mfma_f32_16x16x32_bf16 v[112:115], a[32:35], v[32:35], v[112:115]\n"
    "buffer_load_dwordx4 a[104:107], v180, s[48:51], s41 offen sc1\n"
    "v_mfma_f32_16x16x32_bf16 v[100:103], a[36:39], v[24:27], v[100:103]\n"
    "ds_read_b64_tr_b16 a[60:61], v227 offset:864\n"
    "v_mfma_f32_16x16x32_bf16 v[108:111], a[36:39], v[28:31], v[108:111]\n"
    "ds_read_b64_tr_b16 a[62:63], v227 offset:992\n"
    "v_mfma_f32_16x16x32_bf16 v[116:119], a[36:39], v[32:35], v[116:119]\n"
    "s_waitcnt lgkmcnt(8)\n"
    "v_add_u32 v227, s68, v225\n"
    "v_mfma_f32_16x16x32_bf16 v[120:123], a[40:43], v[24:27], v[120:123]\n"
    "ds_read_b64_tr_b16 a[0:1], v227\n"
    "s_add_u32 s68, s68, 16896\n"
    "v_mfma_f32_16x16x32_bf16 v[128:131], a[40:43], v[28:31], v[128:131]\n"
    "ds_read_b64_tr_b16 a[2:3], v227 offset:128\n"
    "v_mfma_f32_16x16x32_bf16 v[136:139], a[40:43], v[32:35], v[136:139]\n"
    "s_cmp_eq_u32 s68, 152064\n"
    "s_cselect_b32 s68, 0, s68\n"
    "v_mfma_f32_16x16x32_bf16 v[124:127], a[44:47], v[24:27], v[124:127]\n"
    "ds_read_b64_tr_b16 a[4:5], v227 offset:32\n"
    "v_mfma_f32_16x16x32_bf16 v[132:135], a[44:47], v[28:31], v[132:135]\n"
    "ds_read_b64_tr_b16 a[6:7], v227 offset:160\n"
    "v_mfma_f32_16x16x32_bf16 v[140:143], a[44:47], v[32:35], v[140:143]\n"
    "s_waitcnt lgkmcnt(8)\n"
    "buffer_load_dwordx4 a[108:111], v180, s[48:51], s41 offen offset:1024 sc1\n"
    "v_mfma_f32_16x16x32_bf16 v[96:99], a[48:51], v[36:39], v[96:99]\n"
    "ds_read_b64_tr_b16 a[8:9], v227 offset:64\n"
    "v_mfma_f32_16x16x32_bf16 v[104:107], a[48:51], v[40:43], v[104:107]\n"
    "ds_read_b64_tr_b16 a[10:11], v227 offset:192\n"
    "v_mfma_f32_16x16x32_bf16 v[112:115], a[48:51], v[44:47], v[112:115]\n"
    "buffer_load_dwordx4 a[112:115], v187, s[48:51], s42 offen sc1\n"
    "v_mfma_f32_16x16x32_bf16 v[100:103], a[52:55], v[36:39], v[100:103]\n"
    "ds_read_b64_tr_b16 a[12:13], v227 offset:96\n"
    "v_mfma_f32_16x16x32_bf16 v[108:111], a[52:55], v[40:43], v[108:111]\n"
    "ds_read_b64_tr_b16 a[14:15], v227 offset:224\n"
    "v_mfma_f32_16x16x32_bf16 v[116:119], a[52:55], v[44:47], v[116:119]\n"
    "s_waitcnt lgkmcnt(8)\n"
    "buffer_load_dwordx4 a[116:119], v187, s[48:51], s42 offen offset:1024 sc1\n"
    "v_mfma_f32_16x16x32_bf16 v[120:123], a[56:59], v[36:39], v[120:123]\n"
    "ds_read_b64_tr_b16 a[16:17], v227 offset:256\n"
    "v_mfma_f32_16x16x32_bf16 v[128:131], a[56:59], v[40:43], v[128:131]\n"
    "ds_read_b64_tr_b16 a[18:19], v227 offset:384\n"
    "v_mfma_f32_16x16x32_bf16 v[136:139], a[56:59], v[44:47], v[136:139]\n"
    "buffer_load_dwordx4 a[120:123], v187, s[48:51], s42 offen offset:2048 sc1\n"
    "v_mfma_f32_16x16x32_bf16 v[124:127], a[60:63], v[36:39], v[124:127]\n"
    "ds_read_b64_tr_b16 a[20:21], v227 offset:288\n"
    "v_mfma_f32_16x16x32_bf16 v[132:135], a[60:63], v[40:43], v[132:135]\n"
    "ds_read_b64_tr_b16 a[22:23], v227 offset:416\n"
    "v_mfma_f32_16x16x32_bf16 v[140:143], a[60:63], v[44:47], v[140:143]\n"
    "s_waitcnt lgkmcnt(8)\n"
    "buffer_load_dwordx4 a[124:127], v187, s[48:51], s42 offen offset:3072 sc1\n"
    "v_mfma_f32_16x16x32_bf16 v[96:99], a[0:3], v[48:51], v[96:99]\n"
    "ds_read_b64_tr_b16 a[24:25], v227 offset:320\n"
    "v_mfma_f32_16x16x32_bf16 v[104:107], a[0:3], v[52:55], v[104:107]\n"
    "ds_read_b64_tr_b16 a[26:27], v227 offset:448\n"
    "v_mfma_f32_16x16x32_bf16 v[112:115], a[0:3], v[56:59], v[112:115]\n"
    "buffer_load_dwordx4 a[128:131], v180, s[48:51], s42 offen sc1\n"
    "v_mfma_f32_16x16x32_bf16 v[100:103], a[4:7], v[48:51], v[100:103]\n"
    "ds_read_b64_tr_b16 a[28:29], v227 offset:352\n"
    "v_mfma_f32_16x16x32_bf16 v[108:111], a[4:7], v[52:55], v[108:111]\n"
    "ds_read_b64_tr_b16 a[30:31], v227 offset:480\n"
    "v_mfma_f32_16x16x32_bf16 v[116:119], a[4:7], v[56:59], v[116:119]\n"
    "s_waitcnt lgkmcnt(8)\n"
    "buffer_load_dwordx4 a[132:135], v180, s[48:51], s42 offen offset:1024 sc1\n"
    "v_mfma_f32_16x16x32_bf16 v[120:123], a[8:11], v[48:51], v[120:123]\n"
    "ds_read_b64_tr_b16 a[32:33], v227 offset:512\n"
    "v_mfma_f32_16x16x32_bf16 v[128:131], a[8:11], v[52:55], v[128:131]\n"
    "ds_read_b64_tr_b16 a[34:35], v227 offset:640\n"
    "v_mfma_f32_16x16x32_bf16 v[136:139], a[8:11], v[56:59], v[136:139]\n"
    "buffer_load_dwordx4 a[136:139], v187, s[48:51], s43 offen sc1\n"
    "v_mfma_f32_16x16x32_bf16 v[124:127], a[12:15], v[48:51], v[124:127]\n"
    "ds_read_b64_tr_b16 a[36:37], v227 offset:544\n"
    "v_mfma_f32_16x16x32_bf16 v[132:135], a[12:15], v[52:55], v[132:135]\n"
    "ds_read_b64_tr_b16 a[38:39], v227 offset:672\n"
    "v_mfma_f32_16x16x32_bf16 v[140:143], a[12:15], v[56:59], v[140:143]\n"
    "s_waitcnt lgkmcnt(8)\n"
    "buffer_load_dwordx4 a[140:143], v187, s[48:51], s43 offen offset:1024 sc1\n"
    "v_mfma_f32_16x16x32_bf16 v[96:99], a[16:19], v[60:63], v[96:99]\n"
    "ds_read_b64_tr_b16 a[40:41], v227 offset:576\n"
    "v_mfma_f32_16x16x32_bf16 v[104:107], a[16:19], v[64:67], v[104:107]\n"
    "ds_read_b64_tr_b16 a[42:43], v227 offset:704\n"
    "v_mfma_f32_16x16x32_bf16 v[112:115], a[16:19], v[68:71], v[112:115]\n"
    "buffer_load_dwordx4 a[144:147], v187, s[48:51], s43 offen offset:2048 sc1\n"
    "v_mfma_f32_16x16x32_bf16 v[100:103], a[20:23], v[60:63], v[100:103]\n"
    "ds_read_b64_tr_b16 a[44:45], v227 offset:608\n"
    "v_mfma_f32_16x16x32_bf16 v[108:111], a[20:23], v[64:67], v[108:111]\n"
    "ds_read_b64_tr_b16 a[46:47], v227 offset:736\n"
    "v_mfma_f32_16x16x32_bf16 v[116:119], a[20:23], v[68:71], v[116:119]\n"
    "s_waitcnt lgkmcnt(8)\n"
    "buffer_load_dwordx4 a[148:151], v187, s[48:51], s43 offen offset:3072 sc1\n"
    "v_mfma_f32_16x16x32_bf16 v[120:123], a[24:27], v[60:63], v[120:123]\n"
    "ds_read_b64_tr_b16 a[48:49], v227 offset:768\n"
    "v_mfma_f32_16x16x32_bf16 v[128:131], a[24:27], v[64:67], v[128:131]\n"
    "ds_read_b64_tr_b16 a[50:51], v227 offset:896\n"
    "v_mfma_f32_16x16x32_bf16 v[136:139], a[24:27], v[68:71], v[136:139]\n"
    "buffer_load_dwordx4 a[152:155], v180, s[48:51], s43 offen sc1\n"
    "v_mfma_f32_16x16x32_bf16 v[124:127], a[28:31], v[60:63], v[124:127]\n"
    "ds_read_b64_tr_b16 a[52:53], v227 offset:800\n"
    "v_mfma_f32_16x16x32_bf16 v[132:135], a[28:31], v[64:67], v[132:135]\n"
    "ds_read_b64_tr_b16 a[54:55], v227 offset:928\n"
    "v_mfma_f32_16x16x32_bf16 v[140:143], a[28:31], v[68:71], v[140:143]\n"
    "s_waitcnt lgkmcnt(8)\n"
    "buffer_load_dwordx4 a[156:159], v180, s[48:51], s43 offen offset:1024 sc1\n"
    "v_mfma_f32_16x16x32_bf16 v[96:99], a[32:35], v[72:75], v[96:99]\n"
    "ds_read_b64_tr_b16 a[56:57], v227 offset:832\n"
    "v_mfma_f32_16x16x32_bf16 v[104:107], a[32:35], v[76:79], v[104:107]\n"
    "ds_read_b64_tr_b16 a[58:59], v227 offset:960\n"
    "v_mfma_f32_16x16x32_bf16 v[112:115], a[32:35], v[80:83], v[112:115]\n"
    "buffer_load_dwordx4 a[160:163], v187, s[48:51], s52 offen sc1\n"
    "v_mfma_f32_16x16x32_bf16 v[100:103], a[36:39], v[72:75], v[100:103]\n"
    "ds_read_b64_tr_b16 a[60:61], v227 offset:864\n"
    "v_mfma_f32_16x16x32_bf16 v[108:111], a[36:39], v[76:79], v[108:111]\n"
    "ds_read_b64_tr_b16 a[62:63], v227 offset:992\n"
    "v_mfma_f32_16x16x32_bf16 v[116:119], a[36:39], v[80:83], v[116:119]\n"
    "s_waitcnt lgkmcnt(8)\n"
    "buffer_load_dwordx4 a[164:167], v187, s[48:51], s52 offen offset:1024 sc1\n"
    "v_mfma_f32_16x16x32_bf16 v[120:123], a[40:43], v[72:75], v[120:123]\n"
    "v_mfma_f32_16x16x32_bf16 v[128:131], a[40:43], v[76:79], v[128:131]\n"
    "v_mfma_f32_16x16x32_bf16 v[136:139], a[40:43], v[80:83], v[136:139]\n"
    "buffer_load_dwordx4 a[168:171], v187, s[48:51], s52 offen offset:2048 sc1\n"
    "v_mfma_f32_16x16x32_bf16 v[124:127], a[44:47], v[72:75], v[124:127]\n"
    "v_mfma_f32_16x16x32_bf16 v[132:135], a[44:47], v[76:79], v[132:135]\n"
    "v_mfma_f32_16x16x32_bf16 v[140:143], a[44:47], v[80:83], v[140:143]\n"
    "s_waitcnt lgkmcnt(4)\n"
    "buffer_load_dwordx4 a[172:175], v187, s[48:51], s52 offen offset:3072 sc1\n"
    "v_mfma_f32_16x16x32_bf16 v[96:99], a[48:51], v[84:87], v[96:99]\n"
    "buffer_load_dwordx4 a[176:179], v180, s[48:51], s52 offen sc1\n"
    "buffer_load_dwordx4 a[180:183], v180, s[48:51], s52 offen offset:1024 sc1\n"
    "v_mfma_f32_16x16x32_bf16 v[104:107], a[48:51], v[88:91], v[104:107]\n"
    "v_mfma_f32_16x16x32_bf16 v[112:115], a[48:51], v[92:95], v[112:115]\n"
    "buffer_load_dword v179, v176, s[48:51], s37 offen sc1\n"
    "v_mfma_f32_16x16x32_bf16 v[100:103], a[52:55], v[84:87], v[100:103]\n"
    "v_mfma_f32_16x16x32_bf16 v[108:111], a[52:55], v[88:91], v[108:111]\n"
    "v_mfma_f32_16x16x32_bf16 v[116:119], a[52:55], v[92:95], v[116:119]\n"
    "s_waitcnt lgkmcnt(0)\n"
    "v_mfma_f32_16x16x32_bf16 v[120:123], a[56:59], v[84:87], v[120:123]\n"
    "v_mfma_f32_16x16x32_bf16 v[128:131], a[56:59], v[88:91], v[128:131]\n"
    "v_mfma_f32_16x16x32_bf16 v[136:139], a[56:59], v[92:95], v[136:139]\n"
    "v_mfma_f32_16x16x32_bf16 v[124:127], a[60:63], v[84:87], v[124:127]\n"
    "v_mfma_f32_16x16x32_bf16 v[132:135], a[60:63], v[88:91], v[132:135]\n"
    "v_mfma_f32_16x16x32_bf16 v[140:143], a[60:63], v[92:95], v[140:143]\n"
    "s_cmp_eq_u32 s66, 7\n"
    "s_cbranch_scc0 LOWN\n"
    "s_mul_i32 s89, s93, 196608\n"
    "s_add_u32 s89, s89, s94\n"
    "s_add_u32 s90, s89, 4096\n"
    "s_mov_b32 s82, s93\n"
    "s_add_u32 s93, s93, 1\n"
    "s_cmp_eq_u32 s93, 7\n"
    "s_cselect_b32 s93, 0, s93\n"
    "s_nop 7\n"
    "v_cvt_pk_bf16_f32 v144, v96, v97\n"
    "v_cvt_pk_bf16_f32 v145, v98, v99\n"
    "v_cvt_pk_bf16_f32 v146, v100, v101\n"
    "v_cvt_pk_bf16_f32 v147, v102, v103\n"
    "v_cvt_pk_bf16_f32 v148, v104, v105\n"
    "v_cvt_pk_bf16_f32 v149, v106, v107\n"
    "v_cvt_pk_bf16_f32 v150, v108, v109\n"
    "v_cvt_pk_bf16_f32 v151, v110, v111\n"
    "v_cvt_pk_bf16_f32 v152, v112, v113\n"
    "v_cvt_pk_bf16_f32 v153, v114, v115\n"
    "v_cvt_pk_bf16_f32 v154, v116, v117\n"
    "v_cvt_pk_bf16_f32 v155, v118, v119\n"
    "v_cvt_pk_bf16_f32 v156, v120, v121\n"
    "v_cvt_pk_bf16_f32 v157, v122, v123\n"
    "v_cvt_pk_bf16_f32 v158, v124, v125\n"
    "v_cvt_pk_bf16_f32 v159, v126, v127\n"
    "v_cvt_pk_bf16_f32 v160, v128, v129\n"
    "v_cvt_pk_bf16_f32 v161, v130, v131\n"
    "v_cvt_pk_bf16_f32 v162, v132, v133\n"
    "v_cvt_pk_bf16_f32 v163, v134, v135\n"
    "v_cvt_pk_bf16_f32 v164, v136, v137\n"
    "v_cvt_pk_bf16_f32 v165, v138, v139\n"
    "v_cvt_pk_bf16_f32 v166, v140, v141\n"
    "v_cvt_pk_bf16_f32 v167, v142, v143\n"
    "buffer_store_dwordx4 v[144:147], v187, s[48:51], s89 offen sc1\n"
    "buffer_store_dwordx4 v[148:151], v187, s[48:51], s89 offen offset:1024 sc1\n"
    "buffer_store_dwordx4 v[152:155], v187, s[48:51], s89 offen offset:2048 sc1\n"
    "buffer_store_dwordx4 v[156:159], v187, s[48:51], s89 offen offset:3072 sc1\n"
    "buffer_store_dwordx4 v[160:163], v187, s[48:51], s90 offen sc1\n"
    "buffer_store_dwordx4 v[164:167], v187, s[48:51], s90 offen offset:1024 sc1\n"
    "s_mov_b32 s38, s82\n"
    "s_lshl_b32 s79, s38, 4\n"
    "v_lshlrev_b32 v241, 7, v187\n"
    "v_cmp_eq_u32 vcc, s79, v187\n"
    "v_mov_b32 v242, 0x7fff0000\n"
    "s_nop 0\n"
    "v_cndmask_b32 v241, v242, v241, vcc\n"
    "s_waitcnt vmcnt(0)\n"
    "buffer_store_dword v236, v241, s[48:51], s36 offen sc1\n"
    "s_branch LFIN\n"
    "LOWN:\n"
    "s_cmp_eq_u32 s55, 1\n"
    "s_cbranch_scc1 LPOL1D\n"
    "s_mov_b32 s39, 262144\n"
    "LPOL1:\n"
    "buffer_load_dword v178, v174, s[48:51], s37 offen sc1\n"
    "s_waitcnt vmcnt(0)\n"
    "v_cmp_ne_u32 vcc, v178, v175\n"
    "s_nop 0\n"
    "s_cbranch_vccz LPOL1X\n"
    "s_sleep 2\n"
    "s_sub_u32 s39, s39, 1\n"
    "s_cmp_lg_u32 s39, 0\n"
    "s_cbranch_scc1 LPOL1\n"
    "LPOL1X:\n"
    "buffer_load_dwordx4 a[64:67], v187, s[48:51], s40 offen sc1\n"
    "buffer_load_dwordx4 a[68:71], v187, s[48:51], s40 offen offset:1024 sc1\n"
    "buffer_load_dwordx4 a[72:75], v187, s[48:51], s40 offen offset:2048 sc1\n"
    "buffer_load_dwordx4 a[76:79], v187, s[48:51], s40 offen offset:3072 sc1\n"
    "buffer_load_dwordx4 a[80:83], v180, s[48:51], s40 offen sc1\n"
    "buffer_load_dwordx4 a[84:87], v180, s[48:51], s40 offen offset:1024 sc1\n"
    "buffer_load_dwordx4 a[88:91], v187, s[48:51], s41 offen sc1\n"
    "buffer_load_dwordx4 a[92:95], v187, s[48:51], s41 offen offset:1024 sc1\n"
    "buffer_load_dwordx4 a[96:99], v187, s[48:51], s41 offen offset:2048 sc1\n"
    "buffer_load_dwordx4 a[100:103], v187, s[48:51], s41 offen offset:3072 sc1\n"
    "buffer_load_dwordx4 a[104:107], v180, s[48:51], s41 offen sc1\n"
    "buffer_load_dwordx4 a[108:111], v180, s[48:51], s41 offen offset:1024 sc1\n"
    "buffer_load_dwordx4 a[112:115], v187, s[48:51], s42 offen sc1\n"
    "buffer_load_dwordx4 a[116:119], v187, s[48:51], s42 offen offset:1024 sc1\n"
    "buffer_load_dwordx4 a[120:123], v187, s[48:51], s42 offen offset:2048 sc1\n"
    "buffer_load_dwordx4 a[124:127], v187, s[48:51], s42 offen offset:3072 sc1\n"
    "buffer_load_dwordx4 a[128:131], v180, s[48:51], s42 offen sc1\n"
    "buffer_load_dwordx4 a[132:135], v180, s[48:51], s42 offen offset:1024 sc1\n"
    "buffer_load_dwordx4 a[136:139], v187, s[48:51], s43 offen sc1\n"
    "buffer_load_dwordx4 a[140:143], v187, s[48:51], s43 offen offset:1024 sc1\n"
    "buffer_load_dwordx4 a[144:147], v187, s[48:51], s43 offen offset:2048 sc1\n"
    "buffer_load_dwordx4 a[148:151], v187, s[48:51], s43 offen offset:3072 sc1\n"
    "buffer_load_dwordx4 a[152:155], v180, s[48:51], s43 offen sc1\n"
    "buffer_load_dwordx4 a[156:159], v180, s[48:51], s43 offen offset:1024 sc1\n"
    "buffer_load_dwordx4 a[160:163], v187, s[48:51], s52 offen sc1\n"
    "buffer_load_dwordx4 a[164:167], v187, s[48:51], s52 offen offset:1024 sc1\n"
    "buffer_load_dwordx4 a[168:171], v187, s[48:51], s52 offen offset:2048 sc1\n"
    "buffer_load_dwordx4 a[172:175], v187, s[48:51], s52 offen offset:3072 sc1\n"
    "buffer_load_dwordx4 a[176:179], v180, s[48:51], s52 offen sc1\n"
    "buffer_load_dwordx4 a[180:183], v180, s[48:51], s52 offen offset:1024 sc1\n"
    "LPOL1D:\n"
    "s_waitcnt vmcnt(0)\n"
    "v_cmp_ne_u32 vcc, v179, v177\n"
    "s_nop 0\n"
    "s_cbranch_vccz LPOL2D\n"
    "s_mov_b32 s39, 262144\n"
    "LPOL2:\n"
    "buffer_load_dword v179, v176, s[48:51], s37 offen sc1\n"
    "s_waitcnt vmcnt(0)\n"
    "v_cmp_ne_u32 vcc, v179, v177\n"
    "s_nop 0\n"
    "s_cbranch_vccz LPOL2D\n"
    "s_sleep 2\n"
    "s_sub_u32 s39, s39, 1\n"
    "s_cmp_lg_u32 s39, 0\n"
    "s_cbranch_scc1 LPOL2\n"
    "LPOL2D:\n"
    "buffer_load_dwordx4 a[192:195], v187, s[48:51], s53 offen sc1\n"
    "buffer_load_dwordx4 a[196:199], v187, s[48:51], s53 offen offset:1024 sc1\n"
    "buffer_load_dwordx4 a[200:203], v187, s[48:51], s53 offen offset:2048 sc1\n"
    "buffer_load_dwordx4 a[204:207], v187, s[48:51], s53 offen offset:3072 sc1\n"
    "buffer_load_dwordx4 a[208:211], v180, s[48:51], s53 offen sc1\n"
    "buffer_load_dwordx4 a[212:215], v180, s[48:51], s53 offen offset:1024 sc1\n"
    "buffer_load_dwordx4 a[216:219], v187, s[48:51], s54 offen sc1\n"
    "buffer_load_dwordx4 a[220:223], v187, s[48:51], s54 offen offset:1024 sc1\n"
    "buffer_load_dwordx4 a[224:227], v187, s[48:51], s54 offen offset:2048 sc1\n"
    "buffer_load_dwordx4 a[228:231], v187, s[48:51], s54 offen offset:3072 sc1\n"
    "buffer_load_dwordx4 a[232:235], v180, s[48:51], s54 offen sc1\n"
    "buffer_load_dwordx4 a[236:239], v180, s[48:51], s54 offen offset:1024 sc1\n"
    "s_mov_b32 s44, %[olo]\n"
    "s_and_b32 s45, %[ohi], 65535\n"
    "s_mov_b32 s46, 5505024\n"
    "s_mov_b32 s47, 131072\n"
    "v_lshrrev_b32 v243, 4, v187\n"
    "v_and_b32 v241, 15, v243\n"
    "v_lshrrev_b32 v242, 4, v243\n"
    "v_mul_u32_u24 v241, 896, v241\n"
    "v_lshl_add_u32 v241, v242, 4, v241\n"
    "s_lshl_b32 s80, s66, 7\n"
    "s_add_u32 s76, s76, s80\n"
    "s_add_u32 s77, s77, s80\n"
    "s_add_u32 s78, s78, s80\n"
    "v_mfma_f32_16x16x32_bf16 v[96:99], v[170:173], a[64:67], v[96:99]\n"
    "v_mfma_f32_16x16x32_bf16 v[100:103], v[168:171], a[64:67], v[100:103]\n"
    "v_mfma_f32_16x16x32_bf16 v[104:107], v[170:173], a[68:71], v[104:107]\n"
    "v_mfma_f32_16x16x32_bf16 v[108:111], v[168:171], a[68:71], v[108:111]\n"
    "v_mfma_f32_16x16x32_bf16 v[112:115], v[170:173], a[72:75], v[112:115]\n"
    "v_mfma_f32_16x16x32_bf16 v[116:119], v[168:171], a[72:75], v[116:119]\n"
    "v_mfma_f32_16x16x32_bf16 v[120:123], v[170:173], a[76:79], v[120:123]\n"
    "v_mfma_f32_16x16x32_bf16 v[124:127], v[168:171], a[76:79], v[124:127]\n"
    "v_mfma_f32_16x16x32_bf16 v[128:131], v[170:173], a[80:83], v[128:131]\n"
    "v_mfma_f32_16x16x32_bf16 v[132:135], v[168:171], a[80:83], v[132:135]\n"
    "v_mfma_f32_16x16x32_bf16 v[136:139], v[170:173], a[84:87], v[136:139]\n"
    "v_mfma_f32_16x16x32_bf16 v[140:143], v[168:171], a[84:87], v[140:143]\n"
    "v_mfma_f32_16x16x32_bf16 v[96:99], v[170:173], a[88:91], v[96:99]\n"
    "v_mfma_f32_16x16x32_bf16 v[100:103], v[168:171], a[88:91], v[100:103]\n"
    "v_mfma_f32_16x16x32_bf16 v[104:107], v[170:173], a[92:95], v[104:107]\n"
    "v_mfma_f32_16x16x32_bf16 v[108:111], v[168:171], a[92:95], v[108:111]\n"
    "v_mfma_f32_16x16x32_bf16 v[112:115], v[170:173], a[96:99], v[112:115]\n"
    "v_mfma_f32_16x16x32_bf16 v[116:119], v[168:171], a[96:99], v[116:119]\n"
    "v_mfma_f32_16x16x32_bf16 v[120:123], v[170:173], a[100:103], v[120:123]\n"
    "v_mfma_f32_16x16x32_bf16 v[124:127], v[168:171], a[100:103], v[124:127]\n"
    "v_mfma_f32_16x16x32_bf16 v[128:131], v[170:173], a[104:107], v[128:131]\n"
    "v_mfma_f32_16x16x32_bf16 v[132:135], v[168:171], a[104:107], v[132:135]\n"
    "v_mfma_f32_16x16x32_bf16 v[136:139], v[170:173], a[108:111], v[136:139]\n"
    "v_mfma_f32_16x16x32_bf16 v[140:143], v[168:171], a[108:111], v[140:143]\n"
    "v_mfma_f32_16x16x32_bf16 v[96:99], v[170:173], a[112:115], v[96:99]\n"
    "v_mfma_f32_16x16x32_bf16 v[100:103], v[168:171], a[112:115], v[100:103]\n"
    "v_mfma_f32_16x16x32_bf16 v[104:107], v[170:173], a[116:119], v[104:107]\n"
    "v_mfma_f32_16x16x32_bf16 v[108:111], v[168:171], a[116:119], v[108:111]\n"
    "v_mfma_f32_16x16x32_bf16 v[112:115], v[170:173], a[120:123], v[112:115]\n"
    "v_mfma_f32_16x16x32_bf16 v[116:119], v[168:171], a[120:123], v[116:119]\n"
    "v_mfma_f32_16x16x32_bf16 v[120:123], v[170:173], a[124:127], v[120:123]\n"
    "v_mfma_f32_16x16x32_bf16 v[124:127], v[168:171], a[124:127], v[124:127]\n"
    "v_mfma_f32_16x16x32_bf16 v[128:131], v[170:173], a[128:131], v[128:131]\n"
    "v_mfma_f32_16x16x32_bf16 v[132:135], v[168:171], a[128:131], v[132:135]\n"
    "v_mfma_f32_16x16x32_bf16 v[136:139], v[170:173], a[132:135], v[136:139]\n"
    "v_mfma_f32_16x16x32_bf16 v[140:143], v[168:171], a[132:135], v[140:143]\n"
    "v_mfma_f32_16x16x32_bf16 v[96:99], v[170:173], a[136:139], v[96:99]\n"
    "v_mfma_f32_16x16x32_bf16 v[100:103], v[168:171], a[136:139], v[100:103]\n"
    "v_mfma_f32_16x16x32_bf16 v[104:107], v[170:173], a[140:143], v[104:107]\n"
    "v_mfma_f32_16x16x32_bf16 v[108:111], v[168:171], a[140:143], v[108:111]\n"
    "v_mfma_f32_16x16x32_bf16 v[112:115], v[170:173], a[144:147], v[112:115]\n"
    "v_mfma_f32_16x16x32_bf16 v[116:119], v[168:171], a[144:147], v[116:119]\n"
    "v_mfma_f32_16x16x32_bf16 v[120:123], v[170:173], a[148:151], v[120:123]\n"
    "v_mfma_f32_16x16x32_bf16 v[124:127], v[168:171], a[148:151], v[124:127]\n"
    "v_mfma_f32_16x16x32_bf16 v[128:131], v[170:173], a[152:155], v[128:131]\n"
    "v_mfma_f32_16x16x32_bf16 v[132:135], v[168:171], a[152:155], v[132:135]\n"
    "v_mfma_f32_16x16x32_bf16 v[136:139], v[170:173], a[156:159], v[136:139]\n"
    "v_mfma_f32_16x16x32_bf16 v[140:143], v[168:171], a[156:159], v[140:143]\n"
    "v_mfma_f32_16x16x32_bf16 v[96:99], v[170:173], a[160:163], v[96:99]\n"
    "v_mfma_f32_16x16x32_bf16 v[100:103], v[168:171], a[160:163], v[100:103]\n"
    "v_mfma_f32_16x16x32_bf16 v[104:107], v[170:173], a[164:167], v[104:107]\n"
    "v_mfma_f32_16x16x32_bf16 v[108:111], v[168:171], a[164:167], v[108:111]\n"
    "v_mfma_f32_16x16x32_bf16 v[112:115], v[170:173], a[168:171], v[112:115]\n"
    "v_mfma_f32_16x16x32_bf16 v[116:119], v[168:171], a[168:171], v[116:119]\n"
    "v_mfma_f32_16x16x32_bf16 v[120:123], v[170:173], a[172:175], v[120:123]\n"
    "v_mfma_f32_16x16x32_bf16 v[124:127], v[168:171], a[172:175], v[124:127]\n"
    "v_mfma_f32_16x16x32_bf16 v[128:131], v[170:173], a[176:179], v[128:131]\n"
    "v_mfma_f32_16x16x32_bf16 v[132:135], v[168:171], a[176:179], v[132:135]\n"
    "v_mfma_f32_16x16x32_bf16 v[136:139], v[170:173], a[180:183], v[136:139]\n"
    "v_mfma_f32_16x16x32_bf16 v[140:143], v[168:171], a[180:183], v[140:143]\n"
    "s_waitcnt vmcnt(11)\n"
    "v_mfma_f32_16x16x32_bf16 v[96:99], v[170:173], a[192:195], v[96:99]\n"
    "v_mfma_f32_16x16x32_bf16 v[100:103], v[168:171], a[192:195], v[100:103]\n"
    "s_waitcnt vmcnt(10)\n"
    "v_mfma_f32_16x16x32_bf16 v[104:107], v[170:173], a[196:199], v[104:107]\n"
    "v_mfma_f32_16x16x32_bf16 v[108:111], v[168:171], a[196:199], v[108:111]\n"
    "s_waitcnt vmcnt(9)\n"
    "v_mfma_f32_16x16x32_bf16 v[112:115], v[170:173], a[200:203], v[112:115]\n"
    "v_mfma_f32_16x16x32_bf16 v[116:119], v[168:171], a[200:203], v[116:119]\n"
    "s_waitcnt vmcnt(8)\n"
    "v_mfma_f32_16x16x32_bf16 v[120:123], v[170:173], a[204:207], v[120:123]\n"
    "v_mfma_f32_16x16x32_bf16 v[124:127], v[168:171], a[204:207], v[124:127]\n"
    "s_waitcnt vmcnt(7)\n"
    "v_mfma_f32_16x16x32_bf16 v[128:131], v[170:173], a[208:211], v[128:131]\n"
    "v_mfma_f32_16x16x32_bf16 v[132:135], v[168:171], a[208:211], v[132:135]\n"
    "s_waitcnt vmcnt(6)\n"
    "v_mfma_f32_16x16x32_bf16 v[136:139], v[170:173], a[212:215], v[136:139]\n"
    "v_mfma_f32_16x16x32_bf16 v[140:143], v[168:171], a[212:215], v[140:143]\n"
    "s_waitcnt vmcnt(5)\n"
    "v_mfma_f32_16x16x32_bf16 v[96:99], v[170:173], a[216:219], v[96:99]\n"
    "v_mfma_f32_16x16x32_bf16 v[100:103], v[168:171], a[216:219], v[100:103]\n"
    "s_waitcnt vmcnt(4)\n"
    "v_mfma_f32_16x16x32_bf16 v[104:107], v[170:173], a[220:223], v[104:107]\n"
    "v_mfma_f32_16x16x32_bf16 v[108:111], v[168:171], a[220:223], v[108:111]\n"
    "s_waitcnt vmcnt(3)\n"
    "v_mfma_f32_16x16x32_bf16 v[112:115], v[170:173], a[224:227], v[112:115]\n"
    "v_mfma_f32_16x16x32_bf16 v[116:119], v[168:171], a[224:227], v[116:119]\n"
    "s_waitcnt vmcnt(2)\n"
    "v_mfma_f32_16x16x32_bf16 v[120:123], v[170:173], a[228:231], v[120:123]\n"
    "v_mfma_f32_16x16x32_bf16 v[124:127], v[168:171], a[228:231], v[124:127]\n"
    "s_waitcnt vmcnt(1)\n"
    "v_mfma_f32_16x16x32_bf16 v[128:131], v[170:173], a[232:235], v[128:131]\n"
    "v_mfma_f32_16x16x32_bf16 v[132:135], v[168:171], a[232:235], v[132:135]\n"
    "s_waitcnt vmcnt(0)\n"
    "v_mfma_f32_16x16x32_bf16 v[136:139], v[170:173], a[236:239], v[136:139]\n"
    "v_mfma_f32_16x16x32_bf16 v[140:143], v[168:171], a[236:239], v[140:143]\n"
    "v_cvt_pk_bf16_f32 v144, v96, v97\n"
    "v_cvt_pk_bf16_f32 v145, v98, v99\n"
    "v_cvt_pk_bf16_f32 v146, v100, v101\n"
    "v_cvt_pk_bf16_f32 v147, v102, v103\n"
    "v_cvt_pk_bf16_f32 v148, v104, v105\n"
    "v_cvt_pk_bf16_f32 v149, v106, v107\n"
    "v_cvt_pk_bf16_f32 v150, v108, v109\n"
    "v_cvt_pk_bf16_f32 v151, v110, v111\n"
    "v_cvt_pk_bf16_f32 v152, v112, v113\n"
    "v_cvt_pk_bf16_f32 v153, v114, v115\n"
    "v_cvt_pk_bf16_f32 v154, v116, v117\n"
    "v_cvt_pk_bf16_f32 v155, v118, v119\n"
    "v_cvt_pk_bf16_f32 v156, v120, v121\n"
    "v_cvt_pk_bf16_f32 v157, v122, v123\n"
    "v_cvt_pk_bf16_f32 v158, v124, v125\n"
    "v_cvt_pk_bf16_f32 v159, v126, v127\n"
    "v_cvt_pk_bf16_f32 v160, v128, v129\n"
    "v_cvt_pk_bf16_f32 v161, v130, v131\n"
    "v_cvt_pk_bf16_f32 v162, v132, v133\n"
    "v_cvt_pk_bf16_f32 v163, v134, v135\n"
    "v_cvt_pk_bf16_f32 v164, v136, v137\n"
    "v_cvt_pk_bf16_f32 v165, v138, v139\n"
    "v_cvt_pk_bf16_f32 v166, v140, v141\n"
    "v_cvt_pk_bf16_f32 v167, v142, v143\n"
    "v_permlane32_swap_b32 v144, v146\n"
    "v_permlane32_swap_b32 v145, v147\n"
    "v_permlane32_swap_b32 v148, v150\n"
    "v_permlane32_swap_b32 v149, v151\n"
    "v_permlane32_swap_b32 v152, v154\n"
    "v_permlane32_swap_b32 v153, v155\n"
    "v_permlane32_swap_b32 v156, v158\n"
    "v_permlane32_swap_b32 v157, v159\n"
    "v_permlane32_swap_b32 v160, v162\n"
    "v_permlane32_swap_b32 v161, v163\n"
    "v_permlane32_swap_b32 v164, v166\n"
    "v_permlane32_swap_b32 v165, v167\n"
    "v_permlane16_swap_b32 v144, v146\n"
    "v_permlane16_swap_b32 v145, v147\n"
    "v_permlane16_swap_b32 v148, v150\n"
    "v_permlane16_swap_b32 v149, v151\n"
    "v_permlane16_swap_b32 v152, v154\n"
    "v_permlane16_swap_b32 v153, v155\n"
    "v_permlane16_swap_b32 v156, v158\n"
    "v_permlane16_swap_b32 v157, v159\n"
    "v_permlane16_swap_b32 v160, v162\n"
    "v_permlane16_swap_b32 v161, v163\n"
    "v_permlane16_swap_b32 v164, v166\n"
    "v_permlane16_swap_b32 v165, v167\n"
    "buffer_store_dwordx4 v[144:147], v241, s[44:47], s76 offen\n"
    "buffer_store_dwordx4 v[148:151], v241, s[44:47], s77 offen\n"
    "buffer_store_dwordx4 v[152:155], v241, s[44:47], s78 offen\n"
    "buffer_store_dwordx4 v[156:159], v241, s[44:47], s76 offen offset:64\n"
    "buffer_store_dwordx4 v[160:163], v241, s[44:47], s77 offen offset:64\n"
    "buffer_store_dwordx4 v[164:167], v241, s[44:47], s78 offen offset:64\n"
    "LFIN:\n"
    "s_branch LEND\n"
    "LR_p:\n"
    "s_getpc_b64 s[86:87]\n"
    "LP_p:\n"
    "s_add_u32 s86, s86, LRR_11-LP_p\n"
    "s_addc_u32 s87, s87, 0\n"
    "s_swappc_b64 s[84:85], s[86:87]\n"
    "s_branch LB_p\n"
    "LR_s0a:\n"
    "s_getpc_b64 s[86:87]\n"
    "LP_s0a:\n"
    "s_add_u32 s86, s86, LRR_00-LP_s0a\n"
    "s_addc_u32 s87, s87, 0\n"
    "s_swappc_b64 s[84:85], s[86:87]\n"
    "s_branch LB_s0a\n"
    "LR_s0b:\n"
    "s_getpc_b64 s[86:87]\n"
    "LP_s0b:\n"
    "s_add_u32 s86, s86, LRR_10-LP_s0b\n"
    "s_addc_u32 s87, s87, 0\n"
    "s_swappc_b64 s[84:85], s[86:87]\n"
    "s_branch LB_s0b\n"
    "LR_l0a:\n"
    "s_getpc_b64 s[86:87]\n"
    "LP_l0a:\n"
    "s_add_u32 s86, s86, LRR_01-LP_l0a\n"
    "s_addc_u32 s87, s87, 0\n"
    "s_swappc_b64 s[84:85], s[86:87]\n"
    "s_branch LB_l0a\n"
    "LR_l0b:\n"
    "s_getpc_b64 s[86:87]\n"
    "LP_l0b:\n"
    "s_add_u32 s86, s86, LRR_11-LP_l0b\n"
    "s_addc_u32 s87, s87, 0\n"
    "s_swappc_b64 s[84:85], s[86:87]\n"
    "s_branch LB_l0b\n"
    "LR_l1a:\n"
    "s_getpc_b64 s[86:87]\n"
    "LP_l1a:\n"
    "s_add_u32 s86, s86, LRR_00-LP_l1a\n"
    "s_addc_u32 s87, s87, 0\n"
    "s_swappc_b64 s[84:85], s[86:87]\n"
    "s_branch LB_l1a\n"
    "LR_l1b:\n"
    "s_getpc_b64 s[86:87]\n"
    "LP_l1b:\n"
    "s_add_u32 s86, s86, LRR_10-LP_l1b\n"
    "s_addc_u32 s87, s87, 0\n"
    "s_swappc_b64 s[84:85], s[86:87]\n"
    "s_branch LB_l1b\n"
    "LR_s11a:\n"
    "s_getpc_b64 s[86:87]\n"
    "LP_s11a:\n"
    "s_add_u32 s86, s86, LRR_01-LP_s11a\n"
    "s_addc_u32 s87, s87, 0\n"
    "s_swappc_b64 s[84:85], s[86:87]\n"
    "s_branch LB_s11a\n"
    "LR_s11b:\n"
    "s_getpc_b64 s[86:87]\n"
    "LP_s11b:\n"
    "s_add_u32 s86, s86, LRR_11-LP_s11b\n"
    "s_addc_u32 s87, s87, 0\n"
    "s_swappc_b64 s[84:85], s[86:87]\n"
    "s_branch LB_s11b\n"
    "LR_s12a:\n"
    "s_getpc_b64 s[86:87]\n"
    "LP_s12a:\n"
    "s_add_u32 s86, s86, LRR_00-LP_s12a\n"
    "s_addc_u32 s87, s87, 0\n"
    "s_swappc_b64 s[84:85], s[86:87]\n"
    "s_branch LB_s12a\n"
    "LR_s12b:\n"
    "s_getpc_b64 s[86:87]\n"
    "LP_s12b:\n"
    "s_add_u32 s86, s86, LRR_10-LP_s12b\n"
    "s_addc_u32 s87, s87, 0\n"
    "s_swappc_b64 s[84:85], s[86:87]\n"
    "s_branch LB_s12b\n"
    "LR_s13a:\n"
    "s_getpc_b64 s[86:87]\n"
    "LP_s13a:\n"
    "s_add_u32 s86, s86, LRR_01-LP_s13a\n"
    "s_addc_u32 s87, s87, 0\n"
    "s_swappc_b64 s[84:85], s[86:87]\n"
    "s_branch LB_s13a\n"
    "LR_s13b:\n"
    "s_getpc_b64 s[86:87]\n"
    "LP_s13b:\n"
    "s_add_u32 s86, s86, LRR_11-LP_s13b\n"
    "s_addc_u32 s87, s87, 0\n"
    "s_swappc_b64 s[84:85], s[86:87]\n"
    "s_branch LB_s13b\n"
    "LR_s14a:\n"
    "s_getpc_b64 s[86:87]\n"
    "LP_s14a:\n"
    "s_add_u32 s86, s86, LRR_00-LP_s14a\n"
    "s_addc_u32 s87, s87, 0\n"
    "s_swappc_b64 s[84:85], s[86:87]\n"
    "s_branch LB_s14a\n"
    "LR_s14b:\n"
    "s_getpc_b64 s[86:87]\n"
    "LP_s14b:\n"
    "s_add_u32 s86, s86, LRR_10-LP_s14b\n"
    "s_addc_u32 s87, s87, 0\n"
    "s_swappc_b64 s[84:85], s[86:87]\n"
    "s_branch LB_s14b\n"
    "LRR_00:\n"
    "s_nop 15\n"
    "v_max3_f32 v108, v96, v97, v98\n"
    "v_max3_f32 v108, v108, v99, 0\n"
    "v_max3_f32 v109, v100, v101, v102\n"
    "v_max3_f32 v109, v109, v103, 0\n"
    "v_max3_f32 v110, v104, v105, v106\n"
    "v_max3_f32 v110, v110, v107, 0\n"
    "v_mov_b32 v111, v108\n"
    "v_mov_b32 v112, v109\n"
    "v_mov_b32 v113, v110\n"
    "s_nop 1\n"
    "v_permlane32_swap_b32 v108, v111\n"
    "v_permlane32_swap_b32 v109, v112\n"
    "v_permlane32_swap_b32 v110, v113\n"
    "v_max_f32 v108, v108, v111\n"
    "v_max_f32 v109, v109, v112\n"
    "v_max_f32 v110, v110, v113\n"
    "v_mov_b32 v111, v108\n"
    "v_mov_b32 v112, v109\n"
    "v_mov_b32 v113, v110\n"
    "s_nop 1\n"
    "v_permlane16_swap_b32 v108, v111\n"
    "v_permlane16_swap_b32 v109, v112\n"
    "v_permlane16_swap_b32 v110, v113\n"
    "v_max_f32 v108, v108, v111\n"
    "v_max_f32 v109, v109, v112\n"
    "v_max_f32 v110, v110, v113\n"
    "v_ceil_f32 v108, v108\n"
    "v_ceil_f32 v109, v109\n"
    "v_ceil_f32 v110, v110\n"
    "v_sub_f32 v111, 0, v108\n"
    "v_sub_f32 v112, 0, v109\n"
    "v_sub_f32 v113, 0, v110\n"
    "v_exp_f32 v111, v111\n"
    "v_exp_f32 v112, v112\n"
    "v_exp_f32 v113, v113\n"
    "s_nop 1\n"
    "v_mul_f32 v240, v240, v111\n"
    "v_mul_f32 v241, v241, v111\n"
    "v_mul_f32 v242, v242, v111\n"
    "v_mul_f32 v243, v243, v111\n"
    "v_sub_f32 v144, v144, v108\n"
    "v_sub_f32 v96, v96, v108\n"
    "v_sub_f32 v145, v145, v108\n"
    "v_sub_f32 v97, v97, v108\n"
    "v_sub_f32 v146, v146, v108\n"
    "v_sub_f32 v98, v98, v108\n"
    "v_sub_f32 v147, v147, v108\n"
    "v_sub_f32 v99, v99, v108\n"
    "v_lshlrev_b32 v114, 16, v120\n"
    "v_and_b32 v115, 4294901760, v120\n"
    "v_mul_f32 v114, v114, v111\n"
    "v_mul_f32 v115, v115, v111\n"
    "v_cvt_pk_bf16_f32 v120, v114, v115\n"
    "v_lshlrev_b32 v114, 16, v121\n"
    "v_and_b32 v115, 4294901760, v121\n"
    "v_mul_f32 v114, v114, v111\n"
    "v_mul_f32 v115, v115, v111\n"
    "v_cvt_pk_bf16_f32 v121, v114, v115\n"
    "v_lshlrev_b32 v114, 16, v122\n"
    "v_and_b32 v115, 4294901760, v122\n"
    "v_mul_f32 v114, v114, v111\n"
    "v_mul_f32 v115, v115, v111\n"
    "v_cvt_pk_bf16_f32 v122, v114, v115\n"
    "v_lshlrev_b32 v114, 16, v123\n"
    "v_and_b32 v115, 4294901760, v123\n"
    "v_mul_f32 v114, v114, v111\n"
    "v_mul_f32 v115, v115, v111\n"
    "v_cvt_pk_bf16_f32 v123, v114, v115\n"
    "v_accvgpr_read_b32 v114, a0\n"
    "v_mul_f32 v114, v114, v111\n"
    "v_accvgpr_write_b32 a0, v114\n"
    "v_accvgpr_read_b32 v114, a1\n"
    "v_mul_f32 v114, v114, v111\n"
    "v_accvgpr_write_b32 a1, v114\n"
    "v_accvgpr_read_b32 v114, a2\n"
    "v_mul_f32 v114, v114, v111\n"
    "v_accvgpr_write_b32 a2, v114\n"
    "v_accvgpr_read_b32 v114, a3\n"
    "v_mul_f32 v114, v114, v111\n"
    "v_accvgpr_write_b32 a3, v114\n"
    "v_accvgpr_read_b32 v114, a4\n"
    "v_mul_f32 v114, v114, v111\n"
    "v_accvgpr_write_b32 a4, v114\n"
    "v_accvgpr_read_b32 v114, a5\n"
    "v_mul_f32 v114, v114, v111\n"
    "v_accvgpr_write_b32 a5, v114\n"
    "v_accvgpr_read_b32 v114, a6\n"
    "v_mul_f32 v114, v114, v111\n"
    "v_accvgpr_write_b32 a6, v114\n"
    "v_accvgpr_read_b32 v114, a7\n"
    "v_mul_f32 v114, v114, v111\n"
    "v_accvgpr_write_b32 a7, v114\n"
    "v_accvgpr_read_b32 v114, a24\n"
    "v_mul_f32 v114, v114, v111\n"
    "v_accvgpr_write_b32 a24, v114\n"
    "v_accvgpr_read_b32 v114, a25\n"
    "v_mul_f32 v114, v114, v111\n"
    "v_accvgpr_write_b32 a25, v114\n"
    "v_accvgpr_read_b32 v114, a26\n"
    "v_mul_f32 v114, v114, v111\n"
    "v_accvgpr_write_b32 a26, v114\n"
    "v_accvgpr_read_b32 v114, a27\n"
    "v_mul_f32 v114, v114, v111\n"
    "v_accvgpr_write_b32 a27, v114\n"
    "v_accvgpr_read_b32 v114, a28\n"
    "v_mul_f32 v114, v114, v111\n"
    "v_accvgpr_write_b32 a28, v114\n"
    "v_accvgpr_read_b32 v114, a29\n"
    "v_mul_f32 v114, v114, v111\n"
    "v_accvgpr_write_b32 a29, v114\n"
    "v_accvgpr_read_b32 v114, a30\n"
    "v_mul_f32 v114, v114, v111\n"
    "v_accvgpr_write_b32 a30, v114\n"
    "v_accvgpr_read_b32 v114, a31\n"
    "v_mul_f32 v114, v114, v111\n"
    "v_accvgpr_write_b32 a31, v114\n"
    "v_accvgpr_read_b32 v114, a48\n"
    "v_mul_f32 v114, v114, v111\n"
    "v_accvgpr_write_b32 a48, v114\n"
    "v_accvgpr_read_b32 v114, a49\n"
    "v_mul_f32 v114, v114, v111\n"
    "v_accvgpr_write_b32 a49, v114\n"
    "v_accvgpr_read_b32 v114, a50\n"
    "v_mul_f32 v114, v114, v111\n"
    "v_accvgpr_write_b32 a50, v114\n"
    "v_accvgpr_read_b32 v114, a51\n"
    "v_mul_f32 v114, v114, v111\n"
    "v_accvgpr_write_b32 a51, v114\n"
    "v_accvgpr_read_b32 v114, a52\n"
    "v_mul_f32 v114, v114, v111\n"
    "v_accvgpr_write_b32 a52, v114\n"
    "v_accvgpr_read_b32 v114, a53\n"
    "v_mul_f32 v114, v114, v111\n"
    "v_accvgpr_write_b32 a53, v114\n"
    "v_accvgpr_read_b32 v114, a54\n"
    "v_mul_f32 v114, v114, v111\n"
    "v_accvgpr_write_b32 a54, v114\n"
    "v_accvgpr_read_b32 v114, a55\n"
    "v_mul_f32 v114, v114, v111\n"
    "v_accvgpr_write_b32 a55, v114\n"
    "v_accvgpr_read_b32 v114, a72\n"
    "v_mul_f32 v114, v114, v111\n"
    "v_accvgpr_write_b32 a72, v114\n"
    "v_accvgpr_read_b32 v114, a73\n"
    "v_mul_f32 v114, v114, v111\n"
    "v_accvgpr_write_b32 a73, v114\n"
    "v_accvgpr_read_b32 v114, a74\n"
    "v_mul_f32 v114, v114, v111\n"
    "v_accvgpr_write_b32 a74, v114\n"
    "v_accvgpr_read_b32 v114, a75\n"
    "v_mul_f32 v114, v114, v111\n"
    "v_accvgpr_write_b32 a75, v114\n"
    "v_accvgpr_read_b32 v114, a76\n"
    "v_mul_f32 v114, v114, v111\n"
    "v_accvgpr_write_b32 a76, v114\n"
    "v_accvgpr_read_b32 v114, a77\n"
    "v_mul_f32 v114, v114, v111\n"
    "v_accvgpr_write_b32 a77, v114\n"
    "v_accvgpr_read_b32 v114, a78\n"
    "v_mul_f32 v114, v114, v111\n"
    "v_accvgpr_write_b32 a78, v114\n"
    "v_accvgpr_read_b32 v114, a79\n"
    "v_mul_f32 v114, v114, v111\n"
    "v_accvgpr_write_b32 a79, v114\n"
    "v_accvgpr_read_b32 v114, a96\n"
    "v_mul_f32 v114, v114, v111\n"
    "v_accvgpr_write_b32 a96, v114\n"
    "v_accvgpr_read_b32 v114, a97\n"
    "v_mul_f32 v114, v114, v111\n"
    "v_accvgpr_write_b32 a97, v114\n"
    "v_accvgpr_read_b32 v114, a98\n"
    "v_mul_f32 v114, v114, v111\n"
    "v_accvgpr_write_b32 a98, v114\n"
    "v_accvgpr_read_b32 v114, a99\n"
    "v_mul_f32 v114, v114, v111\n"
    "v_accvgpr_write_b32 a99, v114\n"
    "v_accvgpr_read_b32 v114, a100\n"
    "v_mul_f32 v114, v114, v111\n"
    "v_accvgpr_write_b32 a100, v114\n"
    "v_accvgpr_read_b32 v114, a101\n"
    "v_mul_f32 v114, v114, v111\n"
    "v_accvgpr_write_b32 a101, v114\n"
    "v_accvgpr_read_b32 v114, a102\n"
    "v_mul_f32 v114, v114, v111\n"
    "v_accvgpr_write_b32 a102, v114\n"
    "v_accvgpr_read_b32 v114, a103\n"
    "v_mul_f32 v114, v114, v111\n"
    "v_accvgpr_write_b32 a103, v114\n"
    "v_accvgpr_read_b32 v114, a120\n"
    "v_mul_f32 v114, v114, v111\n"
    "v_accvgpr_write_b32 a120, v114\n"
    "v_accvgpr_read_b32 v114, a121\n"
    "v_mul_f32 v114, v114, v111\n"
    "v_accvgpr_write_b32 a121, v114\n"
    "v_accvgpr_read_b32 v114, a122\n"
    "v_mul_f32 v114, v114, v111\n"
    "v_accvgpr_write_b32 a122, v114\n"
    "v_accvgpr_read_b32 v114, a123\n"
    "v_mul_f32 v114, v114, v111\n"
    "v_accvgpr_write_b32 a123, v114\n"
    "v_accvgpr_read_b32 v114, a124\n"
    "v_mul_f32 v114, v114, v111\n"
    "v_accvgpr_write_b32 a124, v114\n"
    "v_accvgpr_read_b32 v114, a125\n"
    "v_mul_f32 v114, v114, v111\n"
    "v_accvgpr_write_b32 a125, v114\n"
    "v_accvgpr_read_b32 v114, a126\n"
    "v_mul_f32 v114, v114, v111\n"
    "v_accvgpr_write_b32 a126, v114\n"
    "v_accvgpr_read_b32 v114, a127\n"
    "v_mul_f32 v114, v114, v111\n"
    "v_accvgpr_write_b32 a127, v114\n"
    "v_accvgpr_read_b32 v114, a144\n"
    "v_mul_f32 v114, v114, v111\n"
    "v_accvgpr_write_b32 a144, v114\n"
    "v_accvgpr_read_b32 v114, a145\n"
    "v_mul_f32 v114, v114, v111\n"
    "v_accvgpr_write_b32 a145, v114\n"
    "v_accvgpr_read_b32 v114, a146\n"
    "v_mul_f32 v114, v114, v111\n"
    "v_accvgpr_write_b32 a146, v114\n"
    "v_accvgpr_read_b32 v114, a147\n"
    "v_mul_f32 v114, v114, v111\n"
    "v_accvgpr_write_b32 a147, v114\n"
    "v_accvgpr_read_b32 v114, a148\n"
    "v_mul_f32 v114, v114, v111\n"
    "v_accvgpr_write_b32 a148, v114\n"
    "v_accvgpr_read_b32 v114, a149\n"
    "v_mul_f32 v114, v114, v111\n"
    "v_accvgpr_write_b32 a149, v114\n"
    "v_accvgpr_read_b32 v114, a150\n"
    "v_mul_f32 v114, v114, v111\n"
    "v_accvgpr_write_b32 a150, v114\n"
    "v_accvgpr_read_b32 v114, a151\n"
    "v_mul_f32 v114, v114, v111\n"
    "v_accvgpr_write_b32 a151, v114\n"
    "v_accvgpr_read_b32 v114, a168\n"
    "v_mul_f32 v114, v114, v111\n"
    "v_accvgpr_write_b32 a168, v114\n"
    "v_accvgpr_read_b32 v114, a169\n"
    "v_mul_f32 v114, v114, v111\n"
    "v_accvgpr_write_b32 a169, v114\n"
    "v_accvgpr_read_b32 v114, a170\n"
    "v_mul_f32 v114, v114, v111\n"
    "v_accvgpr_write_b32 a170, v114\n"
    "v_accvgpr_read_b32 v114, a171\n"
    "v_mul_f32 v114, v114, v111\n"
    "v_accvgpr_write_b32 a171, v114\n"
    "v_accvgpr_read_b32 v114, a172\n"
    "v_mul_f32 v114, v114, v111\n"
    "v_accvgpr_write_b32 a172, v114\n"
    "v_accvgpr_read_b32 v114, a173\n"
    "v_mul_f32 v114, v114, v111\n"
    "v_accvgpr_write_b32 a173, v114\n"
    "v_accvgpr_read_b32 v114, a174\n"
    "v_mul_f32 v114, v114, v111\n"
    "v_accvgpr_write_b32 a174, v114\n"
    "v_accvgpr_read_b32 v114, a175\n"
    "v_mul_f32 v114, v114, v111\n"
    "v_accvgpr_write_b32 a175, v114\n"
    "v_mul_f32 v244, v244, v112\n"
    "v_mul_f32 v245, v245, v112\n"
    "v_mul_f32 v246, v246, v112\n"
    "v_mul_f32 v247, v247, v112\n"
    "v_sub_f32 v148, v148, v109\n"
    "v_sub_f32 v100, v100, v109\n"
    "v_sub_f32 v149, v149, v109\n"
    "v_sub_f32 v101, v101, v109\n"
    "v_sub_f32 v150, v150, v109\n"
    "v_sub_f32 v102, v102, v109\n"
    "v_sub_f32 v151, v151, v109\n"
    "v_sub_f32 v103, v103, v109\n"
    "v_lshlrev_b32 v114, 16, v124\n"
    "v_and_b32 v115, 4294901760, v124\n"
    "v_mul_f32 v114, v114, v112\n"
    "v_mul_f32 v115, v115, v112\n"
    "v_cvt_pk_bf16_f32 v124, v114, v115\n"
    "v_lshlrev_b32 v114, 16, v125\n"
    "v_and_b32 v115, 4294901760, v125\n"
    "v_mul_f32 v114, v114, v112\n"
    "v_mul_f32 v115, v115, v112\n"
    "v_cvt_pk_bf16_f32 v125, v114, v115\n"
    "v_lshlrev_b32 v114, 16, v126\n"
    "v_and_b32 v115, 4294901760, v126\n"
    "v_mul_f32 v114, v114, v112\n"
    "v_mul_f32 v115, v115, v112\n"
    "v_cvt_pk_bf16_f32 v126, v114, v115\n"
    "v_lshlrev_b32 v114, 16, v127\n"
    "v_and_b32 v115, 4294901760, v127\n"
    "v_mul_f32 v114, v114, v112\n"
    "v_mul_f32 v115, v115, v112\n"
    "v_cvt_pk_bf16_f32 v127, v114, v115\n"
    "v_accvgpr_read_b32 v114, a8\n"
    "v_mul_f32 v114, v114, v112\n"
    "v_accvgpr_write_b32 a8, v114\n"
    "v_accvgpr_read_b32 v114, a9\n"
    "v_mul_f32 v114, v114, v112\n"
    "v_accvgpr_write_b32 a9, v114\n"
    "v_accvgpr_read_b32 v114, a10\n"
    "v_mul_f32 v114, v114, v112\n"
    "v_accvgpr_write_b32 a10, v114\n"
    "v_accvgpr_read_b32 v114, a11\n"
    "v_mul_f32 v114, v114, v112\n"
    "v_accvgpr_write_b32 a11, v114\n"
    "v_accvgpr_read_b32 v114, a12\n"
    "v_mul_f32 v114, v114, v112\n"
    "v_accvgpr_write_b32 a12, v114\n"
    "v_accvgpr_read_b32 v114, a13\n"
    "v_mul_f32 v114, v114, v112\n"
    "v_accvgpr_write_b32 a13, v114\n"
    "v_accvgpr_read_b32 v114, a14\n"
    "v_mul_f32 v114, v114, v112\n"
    "v_accvgpr_write_b32 a14, v114\n"
    "v_accvgpr_read_b32 v114, a15\n"
    "v_mul_f32 v114, v114, v112\n"
    "v_accvgpr_write_b32 a15, v114\n"
    "v_accvgpr_read_b32 v114, a32\n"
    "v_mul_f32 v114, v114, v112\n"
    "v_accvgpr_write_b32 a32, v114\n"
    "v_accvgpr_read_b32 v114, a33\n"
    "v_mul_f32 v114, v114, v112\n"
    "v_accvgpr_write_b32 a33, v114\n"
    "v_accvgpr_read_b32 v114, a34\n"
    "v_mul_f32 v114, v114, v112\n"
    "v_accvgpr_write_b32 a34, v114\n"
    "v_accvgpr_read_b32 v114, a35\n"
    "v_mul_f32 v114, v114, v112\n"
    "v_accvgpr_write_b32 a35, v114\n"
    "v_accvgpr_read_b32 v114, a36\n"
    "v_mul_f32 v114, v114, v112\n"
    "v_accvgpr_write_b32 a36, v114\n"
    "v_accvgpr_read_b32 v114, a37\n"
    "v_mul_f32 v114, v114, v112\n"
    "v_accvgpr_write_b32 a37, v114\n"
    "v_accvgpr_read_b32 v114, a38\n"
    "v_mul_f32 v114, v114, v112\n"
    "v_accvgpr_write_b32 a38, v114\n"
    "v_accvgpr_read_b32 v114, a39\n"
    "v_mul_f32 v114, v114, v112\n"
    "v_accvgpr_write_b32 a39, v114\n"
    "v_accvgpr_read_b32 v114, a56\n"
    "v_mul_f32 v114, v114, v112\n"
    "v_accvgpr_write_b32 a56, v114\n"
    "v_accvgpr_read_b32 v114, a57\n"
    "v_mul_f32 v114, v114, v112\n"
    "v_accvgpr_write_b32 a57, v114\n"
    "v_accvgpr_read_b32 v114, a58\n"
    "v_mul_f32 v114, v114, v112\n"
    "v_accvgpr_write_b32 a58, v114\n"
    "v_accvgpr_read_b32 v114, a59\n"
    "v_mul_f32 v114, v114, v112\n"
    "v_accvgpr_write_b32 a59, v114\n"
    "v_accvgpr_read_b32 v114, a60\n"
    "v_mul_f32 v114, v114, v112\n"
    "v_accvgpr_write_b32 a60, v114\n"
    "v_accvgpr_read_b32 v114, a61\n"
    "v_mul_f32 v114, v114, v112\n"
    "v_accvgpr_write_b32 a61, v114\n"
    "v_accvgpr_read_b32 v114, a62\n"
    "v_mul_f32 v114, v114, v112\n"
    "v_accvgpr_write_b32 a62, v114\n"
    "v_accvgpr_read_b32 v114, a63\n"
    "v_mul_f32 v114, v114, v112\n"
    "v_accvgpr_write_b32 a63, v114\n"
    "v_accvgpr_read_b32 v114, a80\n"
    "v_mul_f32 v114, v114, v112\n"
    "v_accvgpr_write_b32 a80, v114\n"
    "v_accvgpr_read_b32 v114, a81\n"
    "v_mul_f32 v114, v114, v112\n"
    "v_accvgpr_write_b32 a81, v114\n"
    "v_accvgpr_read_b32 v114, a82\n"
    "v_mul_f32 v114, v114, v112\n"
    "v_accvgpr_write_b32 a82, v114\n"
    "v_accvgpr_read_b32 v114, a83\n"
    "v_mul_f32 v114, v114, v112\n"
    "v_accvgpr_write_b32 a83, v114\n"
    "v_accvgpr_read_b32 v114, a84\n"
    "v_mul_f32 v114, v114, v112\n"
    "v_accvgpr_write_b32 a84, v114\n"
    "v_accvgpr_read_b32 v114, a85\n"
    "v_mul_f32 v114, v114, v112\n"
    "v_accvgpr_write_b32 a85, v114\n"
    "v_accvgpr_read_b32 v114, a86\n"
    "v_mul_f32 v114, v114, v112\n"
    "v_accvgpr_write_b32 a86, v114\n"
    "v_accvgpr_read_b32 v114, a87\n"
    "v_mul_f32 v114, v114, v112\n"
    "v_accvgpr_write_b32 a87, v114\n"
    "v_accvgpr_read_b32 v114, a104\n"
    "v_mul_f32 v114, v114, v112\n"
    "v_accvgpr_write_b32 a104, v114\n"
    "v_accvgpr_read_b32 v114, a105\n"
    "v_mul_f32 v114, v114, v112\n"
    "v_accvgpr_write_b32 a105, v114\n"
    "v_accvgpr_read_b32 v114, a106\n"
    "v_mul_f32 v114, v114, v112\n"
    "v_accvgpr_write_b32 a106, v114\n"
    "v_accvgpr_read_b32 v114, a107\n"
    "v_mul_f32 v114, v114, v112\n"
    "v_accvgpr_write_b32 a107, v114\n"
    "v_accvgpr_read_b32 v114, a108\n"
    "v_mul_f32 v114, v114, v112\n"
    "v_accvgpr_write_b32 a108, v114\n"
    "v_accvgpr_read_b32 v114, a109\n"
    "v_mul_f32 v114, v114, v112\n"
    "v_accvgpr_write_b32 a109, v114\n"
    "v_accvgpr_read_b32 v114, a110\n"
    "v_mul_f32 v114, v114, v112\n"
    "v_accvgpr_write_b32 a110, v114\n"
    "v_accvgpr_read_b32 v114, a111\n"
    "v_mul_f32 v114, v114, v112\n"
    "v_accvgpr_write_b32 a111, v114\n"
    "v_accvgpr_read_b32 v114, a128\n"
    "v_mul_f32 v114, v114, v112\n"
    "v_accvgpr_write_b32 a128, v114\n"
    "v_accvgpr_read_b32 v114, a129\n"
    "v_mul_f32 v114, v114, v112\n"
    "v_accvgpr_write_b32 a129, v114\n"
    "v_accvgpr_read_b32 v114, a130\n"
    "v_mul_f32 v114, v114, v112\n"
    "v_accvgpr_write_b32 a130, v114\n"
    "v_accvgpr_read_b32 v114, a131\n"
    "v_mul_f32 v114, v114, v112\n"
    "v_accvgpr_write_b32 a131, v114\n"
    "v_accvgpr_read_b32 v114, a132\n"
    "v_mul_f32 v114, v114, v112\n"
    "v_accvgpr_write_b32 a132, v114\n"
    "v_accvgpr_read_b32 v114, a133\n"
    "v_mul_f32 v114, v114, v112\n"
    "v_accvgpr_write_b32 a133, v114\n"
    "v_accvgpr_read_b32 v114, a134\n"
    "v_mul_f32 v114, v114, v112\n"
    "v_accvgpr_write_b32 a134, v114\n"
    "v_accvgpr_read_b32 v114, a135\n"
    "v_mul_f32 v114, v114, v112\n"
    "v_accvgpr_write_b32 a135, v114\n"
    "v_accvgpr_read_b32 v114, a152\n"
    "v_mul_f32 v114, v114, v112\n"
    "v_accvgpr_write_b32 a152, v114\n"
    "v_accvgpr_read_b32 v114, a153\n"
    "v_mul_f32 v114, v114, v112\n"
    "v_accvgpr_write_b32 a153, v114\n"
    "v_accvgpr_read_b32 v114, a154\n"
    "v_mul_f32 v114, v114, v112\n"
    "v_accvgpr_write_b32 a154, v114\n"
    "v_accvgpr_read_b32 v114, a155\n"
    "v_mul_f32 v114, v114, v112\n"
    "v_accvgpr_write_b32 a155, v114\n"
    "v_accvgpr_read_b32 v114, a156\n"
    "v_mul_f32 v114, v114, v112\n"
    "v_accvgpr_write_b32 a156, v114\n"
    "v_accvgpr_read_b32 v114, a157\n"
    "v_mul_f32 v114, v114, v112\n"
    "v_accvgpr_write_b32 a157, v114\n"
    "v_accvgpr_read_b32 v114, a158\n"
    "v_mul_f32 v114, v114, v112\n"
    "v_accvgpr_write_b32 a158, v114\n"
    "v_accvgpr_read_b32 v114, a159\n"
    "v_mul_f32 v114, v114, v112\n"
    "v_accvgpr_write_b32 a159, v114\n"
    "v_accvgpr_read_b32 v114, a176\n"
    "v_mul_f32 v114, v114, v112\n"
    "v_accvgpr_write_b32 a176, v114\n"
    "v_accvgpr_read_b32 v114, a177\n"
    "v_mul_f32 v114, v114, v112\n"
    "v_accvgpr_write_b32 a177, v114\n"
    "v_accvgpr_read_b32 v114, a178\n"
    "v_mul_f32 v114, v114, v112\n"
    "v_accvgpr_write_b32 a178, v114\n"
    "v_accvgpr_read_b32 v114, a179\n"
    "v_mul_f32 v114, v114, v112\n"
    "v_accvgpr_write_b32 a179, v114\n"
    "v_accvgpr_read_b32 v114, a180\n"
    "v_mul_f32 v114, v114, v112\n"
    "v_accvgpr_write_b32 a180, v114\n"
    "v_accvgpr_read_b32 v114, a181\n"
    "v_mul_f32 v114, v114, v112\n"
    "v_accvgpr_write_b32 a181, v114\n"
    "v_accvgpr_read_b32 v114, a182\n"
    "v_mul_f32 v114, v114, v112\n"
    "v_accvgpr_write_b32 a182, v114\n"
    "v_accvgpr_read_b32 v114, a183\n"
    "v_mul_f32 v114, v114, v112\n"
    "v_accvgpr_write_b32 a183, v114\n"
    "v_mul_f32 v248, v248, v113\n"
    "v_mul_f32 v249, v249, v113\n"
    "v_mul_f32 v250, v250, v113\n"
    "v_mul_f32 v251, v251, v113\n"
    "v_sub_f32 v152, v152, v110\n"
    "v_sub_f32 v104, v104, v110\n"
    "v_sub_f32 v153, v153, v110\n"
    "v_sub_f32 v105, v105, v110\n"
    "v_sub_f32 v154, v154, v110\n"
    "v_sub_f32 v106, v106, v110\n"
    "v_sub_f32 v155, v155, v110\n"
    "v_sub_f32 v107, v107, v110\n"
    "v_lshlrev_b32 v114, 16, v128\n"
    "v_and_b32 v115, 4294901760, v128\n"
    "v_mul_f32 v114, v114, v113\n"
    "v_mul_f32 v115, v115, v113\n"
    "v_cvt_pk_bf16_f32 v128, v114, v115\n"
    "v_lshlrev_b32 v114, 16, v129\n"
    "v_and_b32 v115, 4294901760, v129\n"
    "v_mul_f32 v114, v114, v113\n"
    "v_mul_f32 v115, v115, v113\n"
    "v_cvt_pk_bf16_f32 v129, v114, v115\n"
    "v_lshlrev_b32 v114, 16, v130\n"
    "v_and_b32 v115, 4294901760, v130\n"
    "v_mul_f32 v114, v114, v113\n"
    "v_mul_f32 v115, v115, v113\n"
    "v_cvt_pk_bf16_f32 v130, v114, v115\n"
    "v_lshlrev_b32 v114, 16, v131\n"
    "v_and_b32 v115, 4294901760, v131\n"
    "v_mul_f32 v114, v114, v113\n"
    "v_mul_f32 v115, v115, v113\n"
    "v_cvt_pk_bf16_f32 v131, v114, v115\n"
    "v_accvgpr_read_b32 v114, a16\n"
    "v_mul_f32 v114, v114, v113\n"
    "v_accvgpr_write_b32 a16, v114\n"
    "v_accvgpr_read_b32 v114, a17\n"
    "v_mul_f32 v114, v114, v113\n"
    "v_accvgpr_write_b32 a17, v114\n"
    "v_accvgpr_read_b32 v114, a18\n"
    "v_mul_f32 v114, v114, v113\n"
    "v_accvgpr_write_b32 a18, v114\n"
    "v_accvgpr_read_b32 v114, a19\n"
    "v_mul_f32 v114, v114, v113\n"
    "v_accvgpr_write_b32 a19, v114\n"
    "v_accvgpr_read_b32 v114, a20\n"
    "v_mul_f32 v114, v114, v113\n"
    "v_accvgpr_write_b32 a20, v114\n"
    "v_accvgpr_read_b32 v114, a21\n"
    "v_mul_f32 v114, v114, v113\n"
    "v_accvgpr_write_b32 a21, v114\n"
    "v_accvgpr_read_b32 v114, a22\n"
    "v_mul_f32 v114, v114, v113\n"
    "v_accvgpr_write_b32 a22, v114\n"
    "v_accvgpr_read_b32 v114, a23\n"
    "v_mul_f32 v114, v114, v113\n"
    "v_accvgpr_write_b32 a23, v114\n"
    "v_accvgpr_read_b32 v114, a40\n"
    "v_mul_f32 v114, v114, v113\n"
    "v_accvgpr_write_b32 a40, v114\n"
    "v_accvgpr_read_b32 v114, a41\n"
    "v_mul_f32 v114, v114, v113\n"
    "v_accvgpr_write_b32 a41, v114\n"
    "v_accvgpr_read_b32 v114, a42\n"
    "v_mul_f32 v114, v114, v113\n"
    "v_accvgpr_write_b32 a42, v114\n"
    "v_accvgpr_read_b32 v114, a43\n"
    "v_mul_f32 v114, v114, v113\n"
    "v_accvgpr_write_b32 a43, v114\n"
    "v_accvgpr_read_b32 v114, a44\n"
    "v_mul_f32 v114, v114, v113\n"
    "v_accvgpr_write_b32 a44, v114\n"
    "v_accvgpr_read_b32 v114, a45\n"
    "v_mul_f32 v114, v114, v113\n"
    "v_accvgpr_write_b32 a45, v114\n"
    "v_accvgpr_read_b32 v114, a46\n"
    "v_mul_f32 v114, v114, v113\n"
    "v_accvgpr_write_b32 a46, v114\n"
    "v_accvgpr_read_b32 v114, a47\n"
    "v_mul_f32 v114, v114, v113\n"
    "v_accvgpr_write_b32 a47, v114\n"
    "v_accvgpr_read_b32 v114, a64\n"
    "v_mul_f32 v114, v114, v113\n"
    "v_accvgpr_write_b32 a64, v114\n"
    "v_accvgpr_read_b32 v114, a65\n"
    "v_mul_f32 v114, v114, v113\n"
    "v_accvgpr_write_b32 a65, v114\n"
    "v_accvgpr_read_b32 v114, a66\n"
    "v_mul_f32 v114, v114, v113\n"
    "v_accvgpr_write_b32 a66, v114\n"
    "v_accvgpr_read_b32 v114, a67\n"
    "v_mul_f32 v114, v114, v113\n"
    "v_accvgpr_write_b32 a67, v114\n"
    "v_accvgpr_read_b32 v114, a68\n"
    "v_mul_f32 v114, v114, v113\n"
    "v_accvgpr_write_b32 a68, v114\n"
    "v_accvgpr_read_b32 v114, a69\n"
    "v_mul_f32 v114, v114, v113\n"
    "v_accvgpr_write_b32 a69, v114\n"
    "v_accvgpr_read_b32 v114, a70\n"
    "v_mul_f32 v114, v114, v113\n"
    "v_accvgpr_write_b32 a70, v114\n"
    "v_accvgpr_read_b32 v114, a71\n"
    "v_mul_f32 v114, v114, v113\n"
    "v_accvgpr_write_b32 a71, v114\n"
    "v_accvgpr_read_b32 v114, a88\n"
    "v_mul_f32 v114, v114, v113\n"
    "v_accvgpr_write_b32 a88, v114\n"
    "v_accvgpr_read_b32 v114, a89\n"
    "v_mul_f32 v114, v114, v113\n"
    "v_accvgpr_write_b32 a89, v114\n"
    "v_accvgpr_read_b32 v114, a90\n"
    "v_mul_f32 v114, v114, v113\n"
    "v_accvgpr_write_b32 a90, v114\n"
    "v_accvgpr_read_b32 v114, a91\n"
    "v_mul_f32 v114, v114, v113\n"
    "v_accvgpr_write_b32 a91, v114\n"
    "v_accvgpr_read_b32 v114, a92\n"
    "v_mul_f32 v114, v114, v113\n"
    "v_accvgpr_write_b32 a92, v114\n"
    "v_accvgpr_read_b32 v114, a93\n"
    "v_mul_f32 v114, v114, v113\n"
    "v_accvgpr_write_b32 a93, v114\n"
    "v_accvgpr_read_b32 v114, a94\n"
    "v_mul_f32 v114, v114, v113\n"
    "v_accvgpr_write_b32 a94, v114\n"
    "v_accvgpr_read_b32 v114, a95\n"
    "v_mul_f32 v114, v114, v113\n"
    "v_accvgpr_write_b32 a95, v114\n"
    "v_accvgpr_read_b32 v114, a112\n"
    "v_mul_f32 v114, v114, v113\n"
    "v_accvgpr_write_b32 a112, v114\n"
    "v_accvgpr_read_b32 v114, a113\n"
    "v_mul_f32 v114, v114, v113\n"
    "v_accvgpr_write_b32 a113, v114\n"
    "v_accvgpr_read_b32 v114, a114\n"
    "v_mul_f32 v114, v114, v113\n"
    "v_accvgpr_write_b32 a114, v114\n"
    "v_accvgpr_read_b32 v114, a115\n"
    "v_mul_f32 v114, v114, v113\n"
    "v_accvgpr_write_b32 a115, v114\n"
    "v_accvgpr_read_b32 v114, a116\n"
    "v_mul_f32 v114, v114, v113\n"
    "v_accvgpr_write_b32 a116, v114\n"
    "v_accvgpr_read_b32 v114, a117\n"
    "v_mul_f32 v114, v114, v113\n"
    "v_accvgpr_write_b32 a117, v114\n"
    "v_accvgpr_read_b32 v114, a118\n"
    "v_mul_f32 v114, v114, v113\n"
    "v_accvgpr_write_b32 a118, v114\n"
    "v_accvgpr_read_b32 v114, a119\n"
    "v_mul_f32 v114, v114, v113\n"
    "v_accvgpr_write_b32 a119, v114\n"
    "v_accvgpr_read_b32 v114, a136\n"
    "v_mul_f32 v114, v114, v113\n"
    "v_accvgpr_write_b32 a136, v114\n"
    "v_accvgpr_read_b32 v114, a137\n"
    "v_mul_f32 v114, v114, v113\n"
    "v_accvgpr_write_b32 a137, v114\n"
    "v_accvgpr_read_b32 v114, a138\n"
    "v_mul_f32 v114, v114, v113\n"
    "v_accvgpr_write_b32 a138, v114\n"
    "v_accvgpr_read_b32 v114, a139\n"
    "v_mul_f32 v114, v114, v113\n"
    "v_accvgpr_write_b32 a139, v114\n"
    "v_accvgpr_read_b32 v114, a140\n"
    "v_mul_f32 v114, v114, v113\n"
    "v_accvgpr_write_b32 a140, v114\n"
    "v_accvgpr_read_b32 v114, a141\n"
    "v_mul_f32 v114, v114, v113\n"
    "v_accvgpr_write_b32 a141, v114\n"
    "v_accvgpr_read_b32 v114, a142\n"
    "v_mul_f32 v114, v114, v113\n"
    "v_accvgpr_write_b32 a142, v114\n"
    "v_accvgpr_read_b32 v114, a143\n"
    "v_mul_f32 v114, v114, v113\n"
    "v_accvgpr_write_b32 a143, v114\n"
    "v_accvgpr_read_b32 v114, a160\n"
    "v_mul_f32 v114, v114, v113\n"
    "v_accvgpr_write_b32 a160, v114\n"
    "v_accvgpr_read_b32 v114, a161\n"
    "v_mul_f32 v114, v114, v113\n"
    "v_accvgpr_write_b32 a161, v114\n"
    "v_accvgpr_read_b32 v114, a162\n"
    "v_mul_f32 v114, v114, v113\n"
    "v_accvgpr_write_b32 a162, v114\n"
    "v_accvgpr_read_b32 v114, a163\n"
    "v_mul_f32 v114, v114, v113\n"
    "v_accvgpr_write_b32 a163, v114\n"
    "v_accvgpr_read_b32 v114, a164\n"
    "v_mul_f32 v114, v114, v113\n"
    "v_accvgpr_write_b32 a164, v114\n"
    "v_accvgpr_read_b32 v114, a165\n"
    "v_mul_f32 v114, v114, v113\n"
    "v_accvgpr_write_b32 a165, v114\n"
    "v_accvgpr_read_b32 v114, a166\n"
    "v_mul_f32 v114, v114, v113\n"
    "v_accvgpr_write_b32 a166, v114\n"
    "v_accvgpr_read_b32 v114, a167\n"
    "v_mul_f32 v114, v114, v113\n"
    "v_accvgpr_write_b32 a167, v114\n"
    "v_accvgpr_read_b32 v114, a184\n"
    "v_mul_f32 v114, v114, v113\n"
    "v_accvgpr_write_b32 a184, v114\n"
    "v_accvgpr_read_b32 v114, a185\n"
    "v_mul_f32 v114, v114, v113\n"
    "v_accvgpr_write_b32 a185, v114\n"
    "v_accvgpr_read_b32 v114, a186\n"
    "v_mul_f32 v114, v114, v113\n"
    "v_accvgpr_write_b32 a186, v114\n"
    "v_accvgpr_read_b32 v114, a187\n"
    "v_mul_f32 v114, v114, v113\n"
    "v_accvgpr_write_b32 a187, v114\n"
    "v_accvgpr_read_b32 v114, a188\n"
    "v_mul_f32 v114, v114, v113\n"
    "v_accvgpr_write_b32 a188, v114\n"
    "v_accvgpr_read_b32 v114, a189\n"
    "v_mul_f32 v114, v114, v113\n"
    "v_accvgpr_write_b32 a189, v114\n"
    "v_accvgpr_read_b32 v114, a190\n"
    "v_mul_f32 v114, v114, v113\n"
    "v_accvgpr_write_b32 a190, v114\n"
    "v_accvgpr_read_b32 v114, a191\n"
    "v_mul_f32 v114, v114, v113\n"
    "v_accvgpr_write_b32 a191, v114\n"
    "s_nop 3\n"
    "s_setpc_b64 s[84:85]\n"
    "LRR_01:\n"
    "s_nop 15\n"
    "v_max3_f32 v108, v96, v97, v98\n"
    "v_max3_f32 v108, v108, v99, 0\n"
    "v_max3_f32 v109, v100, v101, v102\n"
    "v_max3_f32 v109, v109, v103, 0\n"
    "v_max3_f32 v110, v104, v105, v106\n"
    "v_max3_f32 v110, v110, v107, 0\n"
    "v_mov_b32 v111, v108\n"
    "v_mov_b32 v112, v109\n"
    "v_mov_b32 v113, v110\n"
    "s_nop 1\n"
    "v_permlane32_swap_b32 v108, v111\n"
    "v_permlane32_swap_b32 v109, v112\n"
    "v_permlane32_swap_b32 v110, v113\n"
    "v_max_f32 v108, v108, v111\n"
    "v_max_f32 v109, v109, v112\n"
    "v_max_f32 v110, v110, v113\n"
    "v_mov_b32 v111, v108\n"
    "v_mov_b32 v112, v109\n"
    "v_mov_b32 v113, v110\n"
    "s_nop 1\n"
    "v_permlane16_swap_b32 v108, v111\n"
    "v_permlane16_swap_b32 v109, v112\n"
    "v_permlane16_swap_b32 v110, v113\n"
    "v_max_f32 v108, v108, v111\n"
    "v_max_f32 v109, v109, v112\n"
    "v_max_f32 v110, v110, v113\n"
    "v_ceil_f32 v108, v108\n"
    "v_ceil_f32 v109, v109\n"
    "v_ceil_f32 v110, v110\n"
    "v_sub_f32 v111, 0, v108\n"
    "v_sub_f32 v112, 0, v109\n"
    "v_sub_f32 v113, 0, v110\n"
    "v_exp_f32 v111, v111\n"
    "v_exp_f32 v112, v112\n"
    "v_exp_f32 v113, v113\n"
    "s_nop 1\n"
    "v_mul_f32 v240, v240, v111\n"
    "v_mul_f32 v241, v241, v111\n"
    "v_mul_f32 v242, v242, v111\n"
    "v_mul_f32 v243, v243, v111\n"
    "v_sub_f32 v144, v144, v108\n"
    "v_sub_f32 v96, v96, v108\n"
    "v_sub_f32 v145, v145, v108\n"
    "v_sub_f32 v97, v97, v108\n"
    "v_sub_f32 v146, v146, v108\n"
    "v_sub_f32 v98, v98, v108\n"
    "v_sub_f32 v147, v147, v108\n"
    "v_sub_f32 v99, v99, v108\n"
    "v_lshlrev_b32 v114, 16, v132\n"
    "v_and_b32 v115, 4294901760, v132\n"
    "v_mul_f32 v114, v114, v111\n"
    "v_mul_f32 v115, v115, v111\n"
    "v_cvt_pk_bf16_f32 v132, v114, v115\n"
    "v_lshlrev_b32 v114, 16, v133\n"
    "v_and_b32 v115, 4294901760, v133\n"
    "v_mul_f32 v114, v114, v111\n"
    "v_mul_f32 v115, v115, v111\n"
    "v_cvt_pk_bf16_f32 v133, v114, v115\n"
    "v_lshlrev_b32 v114, 16, v134\n"
    "v_and_b32 v115, 4294901760, v134\n"
    "v_mul_f32 v114, v114, v111\n"
    "v_mul_f32 v115, v115, v111\n"
    "v_cvt_pk_bf16_f32 v134, v114, v115\n"
    "v_lshlrev_b32 v114, 16, v135\n"
    "v_and_b32 v115, 4294901760, v135\n"
    "v_mul_f32 v114, v114, v111\n"
    "v_mul_f32 v115, v115, v111\n"
    "v_cvt_pk_bf16_f32 v135, v114, v115\n"
    "v_accvgpr_read_b32 v114, a0\n"
    "v_mul_f32 v114, v114, v111\n"
    "v_accvgpr_write_b32 a0, v114\n"
    "v_accvgpr_read_b32 v114, a1\n"
    "v_mul_f32 v114, v114, v111\n"
    "v_accvgpr_write_b32 a1, v114\n"
    "v_accvgpr_read_b32 v114, a2\n"
    "v_mul_f32 v114, v114, v111\n"
    "v_accvgpr_write_b32 a2, v114\n"
    "v_accvgpr_read_b32 v114, a3\n"
    "v_mul_f32 v114, v114, v111\n"
    "v_accvgpr_write_b32 a3, v114\n"
    "v_accvgpr_read_b32 v114, a4\n"
    "v_mul_f32 v114, v114, v111\n"
    "v_accvgpr_write_b32 a4, v114\n"
    "v_accvgpr_read_b32 v114, a5\n"
    "v_mul_f32 v114, v114, v111\n"
    "v_accvgpr_write_b32 a5, v114\n"
    "v_accvgpr_read_b32 v114, a6\n"
    "v_mul_f32 v114, v114, v111\n"
    "v_accvgpr_write_b32 a6, v114\n"
    "v_accvgpr_read_b32 v114, a7\n"
    "v_mul_f32 v114, v114, v111\n"
    "v_accvgpr_write_b32 a7, v114\n"
    "v_accvgpr_read_b32 v114, a24\n"
    "v_mul_f32 v114, v114, v111\n"
    "v_accvgpr_write_b32 a24, v114\n"
    "v_accvgpr_read_b32 v114, a25\n"
    "v_mul_f32 v114, v114, v111\n"
    "v_accvgpr_write_b32 a25, v114\n"
    "v_accvgpr_read_b32 v114, a26\n"
    "v_mul_f32 v114, v114, v111\n"
    "v_accvgpr_write_b32 a26, v114\n"
    "v_accvgpr_read_b32 v114, a27\n"
    "v_mul_f32 v114, v114, v111\n"
    "v_accvgpr_write_b32 a27, v114\n"
    "v_accvgpr_read_b32 v114, a28\n"
    "v_mul_f32 v114, v114, v111\n"
    "v_accvgpr_write_b32 a28, v114\n"
    "v_accvgpr_read_b32 v114, a29\n"
    "v_mul_f32 v114, v114, v111\n"
    "v_accvgpr_write_b32 a29, v114\n"
    "v_accvgpr_read_b32 v114, a30\n"
    "v_mul_f32 v114, v114, v111\n"
    "v_accvgpr_write_b32 a30, v114\n"
    "v_accvgpr_read_b32 v114, a31\n"
    "v_mul_f32 v114, v114, v111\n"
    "v_accvgpr_write_b32 a31, v114\n"
    "v_accvgpr_read_b32 v114, a48\n"
    "v_mul_f32 v114, v114, v111\n"
    "v_accvgpr_write_b32 a48, v114\n"
    "v_accvgpr_read_b32 v114, a49\n"
    "v_mul_f32 v114, v114, v111\n"
    "v_accvgpr_write_b32 a49, v114\n"
    "v_accvgpr_read_b32 v114, a50\n"
    "v_mul_f32 v114, v114, v111\n"
    "v_accvgpr_write_b32 a50, v114\n"
    "v_accvgpr_read_b32 v114, a51\n"
    "v_mul_f32 v114, v114, v111\n"
    "v_accvgpr_write_b32 a51, v114\n"
    "v_accvgpr_read_b32 v114, a52\n"
    "v_mul_f32 v114, v114, v111\n"
    "v_accvgpr_write_b32 a52, v114\n"
    "v_accvgpr_read_b32 v114, a53\n"
    "v_mul_f32 v114, v114, v111\n"
    "v_accvgpr_write_b32 a53, v114\n"
    "v_accvgpr_read_b32 v114, a54\n"
    "v_mul_f32 v114, v114, v111\n"
    "v_accvgpr_write_b32 a54, v114\n"
    "v_accvgpr_read_b32 v114, a55\n"
    "v_mul_f32 v114, v114, v111\n"
    "v_accvgpr_write_b32 a55, v114\n"
    "v_accvgpr_read_b32 v114, a72\n"
    "v_mul_f32 v114, v114, v111\n"
    "v_accvgpr_write_b32 a72, v114\n"
    "v_accvgpr_read_b32 v114, a73\n"
    "v_mul_f32 v114, v114, v111\n"
    "v_accvgpr_write_b32 a73, v114\n"
    "v_accvgpr_read_b32 v114, a74\n"
    "v_mul_f32 v114, v114, v111\n"
    "v_accvgpr_write_b32 a74, v114\n"
    "v_accvgpr_read_b32 v114, a75\n"
    "v_mul_f32 v114, v114, v111\n"
    "v_accvgpr_write_b32 a75, v114\n"
    "v_accvgpr_read_b32 v114, a76\n"
    "v_mul_f32 v114, v114, v111\n"
    "v_accvgpr_write_b32 a76, v114\n"
    "v_accvgpr_read_b32 v114, a77\n"
    "v_mul_f32 v114, v114, v111\n"
    "v_accvgpr_write_b32 a77, v114\n"
    "v_accvgpr_read_b32 v114, a78\n"
    "v_mul_f32 v114, v114, v111\n"
    "v_accvgpr_write_b32 a78, v114\n"
    "v_accvgpr_read_b32 v114, a79\n"
    "v_mul_f32 v114, v114, v111\n"
    "v_accvgpr_write_b32 a79, v114\n"
    "v_accvgpr_read_b32 v114, a96\n"
    "v_mul_f32 v114, v114, v111\n"
    "v_accvgpr_write_b32 a96, v114\n"
    "v_accvgpr_read_b32 v114, a97\n"
    "v_mul_f32 v114, v114, v111\n"
    "v_accvgpr_write_b32 a97, v114\n"
    "v_accvgpr_read_b32 v114, a98\n"
    "v_mul_f32 v114, v114, v111\n"
    "v_accvgpr_write_b32 a98, v114\n"
    "v_accvgpr_read_b32 v114, a99\n"
    "v_mul_f32 v114, v114, v111\n"
    "v_accvgpr_write_b32 a99, v114\n"
    "v_accvgpr_read_b32 v114, a100\n"
    "v_mul_f32 v114, v114, v111\n"
    "v_accvgpr_write_b32 a100, v114\n"
    "v_accvgpr_read_b32 v114, a101\n"
    "v_mul_f32 v114, v114, v111\n"
    "v_accvgpr_write_b32 a101, v114\n"
    "v_accvgpr_read_b32 v114, a102\n"
    "v_mul_f32 v114, v114, v111\n"
    "v_accvgpr_write_b32 a102, v114\n"
    "v_accvgpr_read_b32 v114, a103\n"
    "v_mul_f32 v114, v114, v111\n"
    "v_accvgpr_write_b32 a103, v114\n"
    "v_accvgpr_read_b32 v114, a120\n"
    "v_mul_f32 v114, v114, v111\n"
    "v_accvgpr_write_b32 a120, v114\n"
    "v_accvgpr_read_b32 v114, a121\n"
    "v_mul_f32 v114, v114, v111\n"
    "v_accvgpr_write_b32 a121, v114\n"
    "v_accvgpr_read_b32 v114, a122\n"
    "v_mul_f32 v114, v114, v111\n"
    "v_accvgpr_write_b32 a122, v114\n"
    "v_accvgpr_read_b32 v114, a123\n"
    "v_mul_f32 v114, v114, v111\n"
    "v_accvgpr_write_b32 a123, v114\n"
    "v_accvgpr_read_b32 v114, a124\n"
    "v_mul_f32 v114, v114, v111\n"
    "v_accvgpr_write_b32 a124, v114\n"
    "v_accvgpr_read_b32 v114, a125\n"
    "v_mul_f32 v114, v114, v111\n"
    "v_accvgpr_write_b32 a125, v114\n"
    "v_accvgpr_read_b32 v114, a126\n"
    "v_mul_f32 v114, v114, v111\n"
    "v_accvgpr_write_b32 a126, v114\n"
    "v_accvgpr_read_b32 v114, a127\n"
    "v_mul_f32 v114, v114, v111\n"
    "v_accvgpr_write_b32 a127, v114\n"
    "v_accvgpr_read_b32 v114, a144\n"
    "v_mul_f32 v114, v114, v111\n"
    "v_accvgpr_write_b32 a144, v114\n"
    "v_accvgpr_read_b32 v114, a145\n"
    "v_mul_f32 v114, v114, v111\n"
    "v_accvgpr_write_b32 a145, v114\n"
    "v_accvgpr_read_b32 v114, a146\n"
    "v_mul_f32 v114, v114, v111\n"
    "v_accvgpr_write_b32 a146, v114\n"
    "v_accvgpr_read_b32 v114, a147\n"
    "v_mul_f32 v114, v114, v111\n"
    "v_accvgpr_write_b32 a147, v114\n"
    "v_accvgpr_read_b32 v114, a148\n"
    "v_mul_f32 v114, v114, v111\n"
    "v_accvgpr_write_b32 a148, v114\n"
    "v_accvgpr_read_b32 v114, a149\n"
    "v_mul_f32 v114, v114, v111\n"
    "v_accvgpr_write_b32 a149, v114\n"
    "v_accvgpr_read_b32 v114, a150\n"
    "v_mul_f32 v114, v114, v111\n"
    "v_accvgpr_write_b32 a150, v114\n"
    "v_accvgpr_read_b32 v114, a151\n"
    "v_mul_f32 v114, v114, v111\n"
    "v_accvgpr_write_b32 a151, v114\n"
    "v_accvgpr_read_b32 v114, a168\n"
    "v_mul_f32 v114, v114, v111\n"
    "v_accvgpr_write_b32 a168, v114\n"
    "v_accvgpr_read_b32 v114, a169\n"
    "v_mul_f32 v114, v114, v111\n"
    "v_accvgpr_write_b32 a169, v114\n"
    "v_accvgpr_read_b32 v114, a170\n"
    "v_mul_f32 v114, v114, v111\n"
    "v_accvgpr_write_b32 a170, v114\n"
    "v_accvgpr_read_b32 v114, a171\n"
    "v_mul_f32 v114, v114, v111\n"
    "v_accvgpr_write_b32 a171, v114\n"
    "v_accvgpr_read_b32 v114, a172\n"
    "v_mul_f32 v114, v114, v111\n"
    "v_accvgpr_write_b32 a172, v114\n"
    "v_accvgpr_read_b32 v114, a173\n"
    "v_mul_f32 v114, v114, v111\n"
    "v_accvgpr_write_b32 a173, v114\n"
    "v_accvgpr_read_b32 v114, a174\n"
    "v_mul_f32 v114, v114, v111\n"
    "v_accvgpr_write_b32 a174, v114\n"
    "v_accvgpr_read_b32 v114, a175\n"
    "v_mul_f32 v114, v114, v111\n"
    "v_accvgpr_write_b32 a175, v114\n"
    "v_mul_f32 v244, v244, v112\n"
    "v_mul_f32 v245, v245, v112\n"
    "v_mul_f32 v246, v246, v112\n"
    "v_mul_f32 v247, v247, v112\n"
    "v_sub_f32 v148, v148, v109\n"
    "v_sub_f32 v100, v100, v109\n"
    "v_sub_f32 v149, v149, v109\n"
    "v_sub_f32 v101, v101, v109\n"
    "v_sub_f32 v150, v150, v109\n"
    "v_sub_f32 v102, v102, v109\n"
    "v_sub_f32 v151, v151, v109\n"
    "v_sub_f32 v103, v103, v109\n"
    "v_lshlrev_b32 v114, 16, v136\n"
    "v_and_b32 v115, 4294901760, v136\n"
    "v_mul_f32 v114, v114, v112\n"
    "v_mul_f32 v115, v115, v112\n"
    "v_cvt_pk_bf16_f32 v136, v114, v115\n"
    "v_lshlrev_b32 v114, 16, v137\n"
    "v_and_b32 v115, 4294901760, v137\n"
    "v_mul_f32 v114, v114, v112\n"
    "v_mul_f32 v115, v115, v112\n"
    "v_cvt_pk_bf16_f32 v137, v114, v115\n"
    "v_lshlrev_b32 v114, 16, v138\n"
    "v_and_b32 v115, 4294901760, v138\n"
    "v_mul_f32 v114, v114, v112\n"
    "v_mul_f32 v115, v115, v112\n"
    "v_cvt_pk_bf16_f32 v138, v114, v115\n"
    "v_lshlrev_b32 v114, 16, v139\n"
    "v_and_b32 v115, 4294901760, v139\n"
    "v_mul_f32 v114, v114, v112\n"
    "v_mul_f32 v115, v115, v112\n"
    "v_cvt_pk_bf16_f32 v139, v114, v115\n"
    "v_accvgpr_read_b32 v114, a8\n"
    "v_mul_f32 v114, v114, v112\n"
    "v_accvgpr_write_b32 a8, v114\n"
    "v_accvgpr_read_b32 v114, a9\n"
    "v_mul_f32 v114, v114, v112\n"
    "v_accvgpr_write_b32 a9, v114\n"
    "v_accvgpr_read_b32 v114, a10\n"
    "v_mul_f32 v114, v114, v112\n"
    "v_accvgpr_write_b32 a10, v114\n"
    "v_accvgpr_read_b32 v114, a11\n"
    "v_mul_f32 v114, v114, v112\n"
    "v_accvgpr_write_b32 a11, v114\n"
    "v_accvgpr_read_b32 v114, a12\n"
    "v_mul_f32 v114, v114, v112\n"
    "v_accvgpr_write_b32 a12, v114\n"
    "v_accvgpr_read_b32 v114, a13\n"
    "v_mul_f32 v114, v114, v112\n"
    "v_accvgpr_write_b32 a13, v114\n"
    "v_accvgpr_read_b32 v114, a14\n"
    "v_mul_f32 v114, v114, v112\n"
    "v_accvgpr_write_b32 a14, v114\n"
    "v_accvgpr_read_b32 v114, a15\n"
    "v_mul_f32 v114, v114, v112\n"
    "v_accvgpr_write_b32 a15, v114\n"
    "v_accvgpr_read_b32 v114, a32\n"
    "v_mul_f32 v114, v114, v112\n"
    "v_accvgpr_write_b32 a32, v114\n"
    "v_accvgpr_read_b32 v114, a33\n"
    "v_mul_f32 v114, v114, v112\n"
    "v_accvgpr_write_b32 a33, v114\n"
    "v_accvgpr_read_b32 v114, a34\n"
    "v_mul_f32 v114, v114, v112\n"
    "v_accvgpr_write_b32 a34, v114\n"
    "v_accvgpr_read_b32 v114, a35\n"
    "v_mul_f32 v114, v114, v112\n"
    "v_accvgpr_write_b32 a35, v114\n"
    "v_accvgpr_read_b32 v114, a36\n"
    "v_mul_f32 v114, v114, v112\n"
    "v_accvgpr_write_b32 a36, v114\n"
    "v_accvgpr_read_b32 v114, a37\n"
    "v_mul_f32 v114, v114, v112\n"
    "v_accvgpr_write_b32 a37, v114\n"
    "v_accvgpr_read_b32 v114, a38\n"
    "v_mul_f32 v114, v114, v112\n"
    "v_accvgpr_write_b32 a38, v114\n"
    "v_accvgpr_read_b32 v114, a39\n"
    "v_mul_f32 v114, v114, v112\n"
    "v_accvgpr_write_b32 a39, v114\n"
    "v_accvgpr_read_b32 v114, a56\n"
    "v_mul_f32 v114, v114, v112\n"
    "v_accvgpr_write_b32 a56, v114\n"
    "v_accvgpr_read_b32 v114, a57\n"
    "v_mul_f32 v114, v114, v112\n"
    "v_accvgpr_write_b32 a57, v114\n"
    "v_accvgpr_read_b32 v114, a58\n"
    "v_mul_f32 v114, v114, v112\n"
    "v_accvgpr_write_b32 a58, v114\n"
    "v_accvgpr_read_b32 v114, a59\n"
    "v_mul_f32 v114, v114, v112\n"
    "v_accvgpr_write_b32 a59, v114\n"
    "v_accvgpr_read_b32 v114, a60\n"
    "v_mul_f32 v114, v114, v112\n"
    "v_accvgpr_write_b32 a60, v114\n"
    "v_accvgpr_read_b32 v114, a61\n"
    "v_mul_f32 v114, v114, v112\n"
    "v_accvgpr_write_b32 a61, v114\n"
    "v_accvgpr_read_b32 v114, a62\n"
    "v_mul_f32 v114, v114, v112\n"
    "v_accvgpr_write_b32 a62, v114\n"
    "v_accvgpr_read_b32 v114, a63\n"
    "v_mul_f32 v114, v114, v112\n"
    "v_accvgpr_write_b32 a63, v114\n"
    "v_accvgpr_read_b32 v114, a80\n"
    "v_mul_f32 v114, v114, v112\n"
    "v_accvgpr_write_b32 a80, v114\n"
    "v_accvgpr_read_b32 v114, a81\n"
    "v_mul_f32 v114, v114, v112\n"
    "v_accvgpr_write_b32 a81, v114\n"
    "v_accvgpr_read_b32 v114, a82\n"
    "v_mul_f32 v114, v114, v112\n"
    "v_accvgpr_write_b32 a82, v114\n"
    "v_accvgpr_read_b32 v114, a83\n"
    "v_mul_f32 v114, v114, v112\n"
    "v_accvgpr_write_b32 a83, v114\n"
    "v_accvgpr_read_b32 v114, a84\n"
    "v_mul_f32 v114, v114, v112\n"
    "v_accvgpr_write_b32 a84, v114\n"
    "v_accvgpr_read_b32 v114, a85\n"
    "v_mul_f32 v114, v114, v112\n"
    "v_accvgpr_write_b32 a85, v114\n"
    "v_accvgpr_read_b32 v114, a86\n"
    "v_mul_f32 v114, v114, v112\n"
    "v_accvgpr_write_b32 a86, v114\n"
    "v_accvgpr_read_b32 v114, a87\n"
    "v_mul_f32 v114, v114, v112\n"
    "v_accvgpr_write_b32 a87, v114\n"
    "v_accvgpr_read_b32 v114, a104\n"
    "v_mul_f32 v114, v114, v112\n"
    "v_accvgpr_write_b32 a104, v114\n"
    "v_accvgpr_read_b32 v114, a105\n"
    "v_mul_f32 v114, v114, v112\n"
    "v_accvgpr_write_b32 a105, v114\n"
    "v_accvgpr_read_b32 v114, a106\n"
    "v_mul_f32 v114, v114, v112\n"
    "v_accvgpr_write_b32 a106, v114\n"
    "v_accvgpr_read_b32 v114, a107\n"
    "v_mul_f32 v114, v114, v112\n"
    "v_accvgpr_write_b32 a107, v114\n"
    "v_accvgpr_read_b32 v114, a108\n"
    "v_mul_f32 v114, v114, v112\n"
    "v_accvgpr_write_b32 a108, v114\n"
    "v_accvgpr_read_b32 v114, a109\n"
    "v_mul_f32 v114, v114, v112\n"
    "v_accvgpr_write_b32 a109, v114\n"
    "v_accvgpr_read_b32 v114, a110\n"
    "v_mul_f32 v114, v114, v112\n"
    "v_accvgpr_write_b32 a110, v114\n"
    "v_accvgpr_read_b32 v114, a111\n"
    "v_mul_f32 v114, v114, v112\n"
    "v_accvgpr_write_b32 a111, v114\n"
    "v_accvgpr_read_b32 v114, a128\n"
    "v_mul_f32 v114, v114, v112\n"
    "v_accvgpr_write_b32 a128, v114\n"
    "v_accvgpr_read_b32 v114, a129\n"
    "v_mul_f32 v114, v114, v112\n"
    "v_accvgpr_write_b32 a129, v114\n"
    "v_accvgpr_read_b32 v114, a130\n"
    "v_mul_f32 v114, v114, v112\n"
    "v_accvgpr_write_b32 a130, v114\n"
    "v_accvgpr_read_b32 v114, a131\n"
    "v_mul_f32 v114, v114, v112\n"
    "v_accvgpr_write_b32 a131, v114\n"
    "v_accvgpr_read_b32 v114, a132\n"
    "v_mul_f32 v114, v114, v112\n"
    "v_accvgpr_write_b32 a132, v114\n"
    "v_accvgpr_read_b32 v114, a133\n"
    "v_mul_f32 v114, v114, v112\n"
    "v_accvgpr_write_b32 a133, v114\n"
    "v_accvgpr_read_b32 v114, a134\n"
    "v_mul_f32 v114, v114, v112\n"
    "v_accvgpr_write_b32 a134, v114\n"
    "v_accvgpr_read_b32 v114, a135\n"
    "v_mul_f32 v114, v114, v112\n"
    "v_accvgpr_write_b32 a135, v114\n"
    "v_accvgpr_read_b32 v114, a152\n"
    "v_mul_f32 v114, v114, v112\n"
    "v_accvgpr_write_b32 a152, v114\n"
    "v_accvgpr_read_b32 v114, a153\n"
    "v_mul_f32 v114, v114, v112\n"
    "v_accvgpr_write_b32 a153, v114\n"
    "v_accvgpr_read_b32 v114, a154\n"
    "v_mul_f32 v114, v114, v112\n"
    "v_accvgpr_write_b32 a154, v114\n"
    "v_accvgpr_read_b32 v114, a155\n"
    "v_mul_f32 v114, v114, v112\n"
    "v_accvgpr_write_b32 a155, v114\n"
    "v_accvgpr_read_b32 v114, a156\n"
    "v_mul_f32 v114, v114, v112\n"
    "v_accvgpr_write_b32 a156, v114\n"
    "v_accvgpr_read_b32 v114, a157\n"
    "v_mul_f32 v114, v114, v112\n"
    "v_accvgpr_write_b32 a157, v114\n"
    "v_accvgpr_read_b32 v114, a158\n"
    "v_mul_f32 v114, v114, v112\n"
    "v_accvgpr_write_b32 a158, v114\n"
    "v_accvgpr_read_b32 v114, a159\n"
    "v_mul_f32 v114, v114, v112\n"
    "v_accvgpr_write_b32 a159, v114\n"
    "v_accvgpr_read_b32 v114, a176\n"
    "v_mul_f32 v114, v114, v112\n"
    "v_accvgpr_write_b32 a176, v114\n"
    "v_accvgpr_read_b32 v114, a177\n"
    "v_mul_f32 v114, v114, v112\n"
    "v_accvgpr_write_b32 a177, v114\n"
    "v_accvgpr_read_b32 v114, a178\n"
    "v_mul_f32 v114, v114, v112\n"
    "v_accvgpr_write_b32 a178, v114\n"
    "v_accvgpr_read_b32 v114, a179\n"
    "v_mul_f32 v114, v114, v112\n"
    "v_accvgpr_write_b32 a179, v114\n"
    "v_accvgpr_read_b32 v114, a180\n"
    "v_mul_f32 v114, v114, v112\n"
    "v_accvgpr_write_b32 a180, v114\n"
    "v_accvgpr_read_b32 v114, a181\n"
    "v_mul_f32 v114, v114, v112\n"
    "v_accvgpr_write_b32 a181, v114\n"
    "v_accvgpr_read_b32 v114, a182\n"
    "v_mul_f32 v114, v114, v112\n"
    "v_accvgpr_write_b32 a182, v114\n"
    "v_accvgpr_read_b32 v114, a183\n"
    "v_mul_f32 v114, v114, v112\n"
    "v_accvgpr_write_b32 a183, v114\n"
    "v_mul_f32 v248, v248, v113\n"
    "v_mul_f32 v249, v249, v113\n"
    "v_mul_f32 v250, v250, v113\n"
    "v_mul_f32 v251, v251, v113\n"
    "v_sub_f32 v152, v152, v110\n"
    "v_sub_f32 v104, v104, v110\n"
    "v_sub_f32 v153, v153, v110\n"
    "v_sub_f32 v105, v105, v110\n"
    "v_sub_f32 v154, v154, v110\n"
    "v_sub_f32 v106, v106, v110\n"
    "v_sub_f32 v155, v155, v110\n"
    "v_sub_f32 v107, v107, v110\n"
    "v_lshlrev_b32 v114, 16, v140\n"
    "v_and_b32 v115, 4294901760, v140\n"
    "v_mul_f32 v114, v114, v113\n"
    "v_mul_f32 v115, v115, v113\n"
    "v_cvt_pk_bf16_f32 v140, v114, v115\n"
    "v_lshlrev_b32 v114, 16, v141\n"
    "v_and_b32 v115, 4294901760, v141\n"
    "v_mul_f32 v114, v114, v113\n"
    "v_mul_f32 v115, v115, v113\n"
    "v_cvt_pk_bf16_f32 v141, v114, v115\n"
    "v_lshlrev_b32 v114, 16, v142\n"
    "v_and_b32 v115, 4294901760, v142\n"
    "v_mul_f32 v114, v114, v113\n"
    "v_mul_f32 v115, v115, v113\n"
    "v_cvt_pk_bf16_f32 v142, v114, v115\n"
    "v_lshlrev_b32 v114, 16, v143\n"
    "v_and_b32 v115, 4294901760, v143\n"
    "v_mul_f32 v114, v114, v113\n"
    "v_mul_f32 v115, v115, v113\n"
    "v_cvt_pk_bf16_f32 v143, v114, v115\n"
    "v_accvgpr_read_b32 v114, a16\n"
    "v_mul_f32 v114, v114, v113\n"
    "v_accvgpr_write_b32 a16, v114\n"
    "v_accvgpr_read_b32 v114, a17\n"
    "v_mul_f32 v114, v114, v113\n"
    "v_accvgpr_write_b32 a17, v114\n"
    "v_accvgpr_read_b32 v114, a18\n"
    "v_mul_f32 v114, v114, v113\n"
    "v_accvgpr_write_b32 a18, v114\n"
    "v_accvgpr_read_b32 v114, a19\n"
    "v_mul_f32 v114, v114, v113\n"
    "v_accvgpr_write_b32 a19, v114\n"
    "v_accvgpr_read_b32 v114, a20\n"
    "v_mul_f32 v114, v114, v113\n"
    "v_accvgpr_write_b32 a20, v114\n"
    "v_accvgpr_read_b32 v114, a21\n"
    "v_mul_f32 v114, v114, v113\n"
    "v_accvgpr_write_b32 a21, v114\n"
    "v_accvgpr_read_b32 v114, a22\n"
    "v_mul_f32 v114, v114, v113\n"
    "v_accvgpr_write_b32 a22, v114\n"
    "v_accvgpr_read_b32 v114, a23\n"
    "v_mul_f32 v114, v114, v113\n"
    "v_accvgpr_write_b32 a23, v114\n"
    "v_accvgpr_read_b32 v114, a40\n"
    "v_mul_f32 v114, v114, v113\n"
    "v_accvgpr_write_b32 a40, v114\n"
    "v_accvgpr_read_b32 v114, a41\n"
    "v_mul_f32 v114, v114, v113\n"
    "v_accvgpr_write_b32 a41, v114\n"
    "v_accvgpr_read_b32 v114, a42\n"
    "v_mul_f32 v114, v114, v113\n"
    "v_accvgpr_write_b32 a42, v114\n"
    "v_accvgpr_read_b32 v114, a43\n"
    "v_mul_f32 v114, v114, v113\n"
    "v_accvgpr_write_b32 a43, v114\n"
    "v_accvgpr_read_b32 v114, a44\n"
    "v_mul_f32 v114, v114, v113\n"
    "v_accvgpr_write_b32 a44, v114\n"
    "v_accvgpr_read_b32 v114, a45\n"
    "v_mul_f32 v114, v114, v113\n"
    "v_accvgpr_write_b32 a45, v114\n"
    "v_accvgpr_read_b32 v114, a46\n"
    "v_mul_f32 v114, v114, v113\n"
    "v_accvgpr_write_b32 a46, v114\n"
    "v_accvgpr_read_b32 v114, a47\n"
    "v_mul_f32 v114, v114, v113\n"
    "v_accvgpr_write_b32 a47, v114\n"
    "v_accvgpr_read_b32 v114, a64\n"
    "v_mul_f32 v114, v114, v113\n"
    "v_accvgpr_write_b32 a64, v114\n"
    "v_accvgpr_read_b32 v114, a65\n"
    "v_mul_f32 v114, v114, v113\n"
    "v_accvgpr_write_b32 a65, v114\n"
    "v_accvgpr_read_b32 v114, a66\n"
    "v_mul_f32 v114, v114, v113\n"
    "v_accvgpr_write_b32 a66, v114\n"
    "v_accvgpr_read_b32 v114, a67\n"
    "v_mul_f32 v114, v114, v113\n"
    "v_accvgpr_write_b32 a67, v114\n"
    "v_accvgpr_read_b32 v114, a68\n"
    "v_mul_f32 v114, v114, v113\n"
    "v_accvgpr_write_b32 a68, v114\n"
    "v_accvgpr_read_b32 v114, a69\n"
    "v_mul_f32 v114, v114, v113\n"
    "v_accvgpr_write_b32 a69, v114\n"
    "v_accvgpr_read_b32 v114, a70\n"
    "v_mul_f32 v114, v114, v113\n"
    "v_accvgpr_write_b32 a70, v114\n"
    "v_accvgpr_read_b32 v114, a71\n"
    "v_mul_f32 v114, v114, v113\n"
    "v_accvgpr_write_b32 a71, v114\n"
    "v_accvgpr_read_b32 v114, a88\n"
    "v_mul_f32 v114, v114, v113\n"
    "v_accvgpr_write_b32 a88, v114\n"
    "v_accvgpr_read_b32 v114, a89\n"
    "v_mul_f32 v114, v114, v113\n"
    "v_accvgpr_write_b32 a89, v114\n"
    "v_accvgpr_read_b32 v114, a90\n"
    "v_mul_f32 v114, v114, v113\n"
    "v_accvgpr_write_b32 a90, v114\n"
    "v_accvgpr_read_b32 v114, a91\n"
    "v_mul_f32 v114, v114, v113\n"
    "v_accvgpr_write_b32 a91, v114\n"
    "v_accvgpr_read_b32 v114, a92\n"
    "v_mul_f32 v114, v114, v113\n"
    "v_accvgpr_write_b32 a92, v114\n"
    "v_accvgpr_read_b32 v114, a93\n"
    "v_mul_f32 v114, v114, v113\n"
    "v_accvgpr_write_b32 a93, v114\n"
    "v_accvgpr_read_b32 v114, a94\n"
    "v_mul_f32 v114, v114, v113\n"
    "v_accvgpr_write_b32 a94, v114\n"
    "v_accvgpr_read_b32 v114, a95\n"
    "v_mul_f32 v114, v114, v113\n"
    "v_accvgpr_write_b32 a95, v114\n"
    "v_accvgpr_read_b32 v114, a112\n"
    "v_mul_f32 v114, v114, v113\n"
    "v_accvgpr_write_b32 a112, v114\n"
    "v_accvgpr_read_b32 v114, a113\n"
    "v_mul_f32 v114, v114, v113\n"
    "v_accvgpr_write_b32 a113, v114\n"
    "v_accvgpr_read_b32 v114, a114\n"
    "v_mul_f32 v114, v114, v113\n"
    "v_accvgpr_write_b32 a114, v114\n"
    "v_accvgpr_read_b32 v114, a115\n"
    "v_mul_f32 v114, v114, v113\n"
    "v_accvgpr_write_b32 a115, v114\n"
    "v_accvgpr_read_b32 v114, a116\n"
    "v_mul_f32 v114, v114, v113\n"
    "v_accvgpr_write_b32 a116, v114\n"
    "v_accvgpr_read_b32 v114, a117\n"
    "v_mul_f32 v114, v114, v113\n"
    "v_accvgpr_write_b32 a117, v114\n"
    "v_accvgpr_read_b32 v114, a118\n"
    "v_mul_f32 v114, v114, v113\n"
    "v_accvgpr_write_b32 a118, v114\n"
    "v_accvgpr_read_b32 v114, a119\n"
    "v_mul_f32 v114, v114, v113\n"
    "v_accvgpr_write_b32 a119, v114\n"
    "v_accvgpr_read_b32 v114, a136\n"
    "v_mul_f32 v114, v114, v113\n"
    "v_accvgpr_write_b32 a136, v114\n"
    "v_accvgpr_read_b32 v114, a137\n"
    "v_mul_f32 v114, v114, v113\n"
    "v_accvgpr_write_b32 a137, v114\n"
    "v_accvgpr_read_b32 v114, a138\n"
    "v_mul_f32 v114, v114, v113\n"
    "v_accvgpr_write_b32 a138, v114\n"
    "v_accvgpr_read_b32 v114, a139\n"
    "v_mul_f32 v114, v114, v113\n"
    "v_accvgpr_write_b32 a139, v114\n"
    "v_accvgpr_read_b32 v114, a140\n"
    "v_mul_f32 v114, v114, v113\n"
    "v_accvgpr_write_b32 a140, v114\n"
    "v_accvgpr_read_b32 v114, a141\n"
    "v_mul_f32 v114, v114, v113\n"
    "v_accvgpr_write_b32 a141, v114\n"
    "v_accvgpr_read_b32 v114, a142\n"
    "v_mul_f32 v114, v114, v113\n"
    "v_accvgpr_write_b32 a142, v114\n"
    "v_accvgpr_read_b32 v114, a143\n"
    "v_mul_f32 v114, v114, v113\n"
    "v_accvgpr_write_b32 a143, v114\n"
    "v_accvgpr_read_b32 v114, a160\n"
    "v_mul_f32 v114, v114, v113\n"
    "v_accvgpr_write_b32 a160, v114\n"
    "v_accvgpr_read_b32 v114, a161\n"
    "v_mul_f32 v114, v114, v113\n"
    "v_accvgpr_write_b32 a161, v114\n"
    "v_accvgpr_read_b32 v114, a162\n"
    "v_mul_f32 v114, v114, v113\n"
    "v_accvgpr_write_b32 a162, v114\n"
    "v_accvgpr_read_b32 v114, a163\n"
    "v_mul_f32 v114, v114, v113\n"
    "v_accvgpr_write_b32 a163, v114\n"
    "v_accvgpr_read_b32 v114, a164\n"
    "v_mul_f32 v114, v114, v113\n"
    "v_accvgpr_write_b32 a164, v114\n"
    "v_accvgpr_read_b32 v114, a165\n"
    "v_mul_f32 v114, v114, v113\n"
    "v_accvgpr_write_b32 a165, v114\n"
    "v_accvgpr_read_b32 v114, a166\n"
    "v_mul_f32 v114, v114, v113\n"
    "v_accvgpr_write_b32 a166, v114\n"
    "v_accvgpr_read_b32 v114, a167\n"
    "v_mul_f32 v114, v114, v113\n"
    "v_accvgpr_write_b32 a167, v114\n"
    "v_accvgpr_read_b32 v114, a184\n"
    "v_mul_f32 v114, v114, v113\n"
    "v_accvgpr_write_b32 a184, v114\n"
    "v_accvgpr_read_b32 v114, a185\n"
    "v_mul_f32 v114, v114, v113\n"
    "v_accvgpr_write_b32 a185, v114\n"
    "v_accvgpr_read_b32 v114, a186\n"
    "v_mul_f32 v114, v114, v113\n"
    "v_accvgpr_write_b32 a186, v114\n"
    "v_accvgpr_read_b32 v114, a187\n"
    "v_mul_f32 v114, v114, v113\n"
    "v_accvgpr_write_b32 a187, v114\n"
    "v_accvgpr_read_b32 v114, a188\n"
    "v_mul_f32 v114, v114, v113\n"
    "v_accvgpr_write_b32 a188, v114\n"
    "v_accvgpr_read_b32 v114, a189\n"
    "v_mul_f32 v114, v114, v113\n"
    "v_accvgpr_write_b32 a189, v114\n"
    "v_accvgpr_read_b32 v114, a190\n"
    "v_mul_f32 v114, v114, v113\n"
    "v_accvgpr_write_b32 a190, v114\n"
    "v_accvgpr_read_b32 v114, a191\n"
    "v_mul_f32 v114, v114, v113\n"
    "v_accvgpr_write_b32 a191, v114\n"
    "s_nop 3\n"
    "s_setpc_b64 s[84:85]\n"
    "LRR_10:\n"
    "s_nop 15\n"
    "v_max3_f32 v96, v108, v109, v110\n"
    "v_max3_f32 v96, v96, v111, 0\n"
    "v_max3_f32 v97, v112, v113, v114\n"
    "v_max3_f32 v97, v97, v115, 0\n"
    "v_max3_f32 v98, v116, v117, v118\n"
    "v_max3_f32 v98, v98, v119, 0\n"
    "v_mov_b32 v99, v96\n"
    "v_mov_b32 v100, v97\n"
    "v_mov_b32 v101, v98\n"
    "s_nop 1\n"
    "v_permlane32_swap_b32 v96, v99\n"
    "v_permlane32_swap_b32 v97, v100\n"
    "v_permlane32_swap_b32 v98, v101\n"
    "v_max_f32 v96, v96, v99\n"
    "v_max_f32 v97, v97, v100\n"
    "v_max_f32 v98, v98, v101\n"
    "v_mov_b32 v99, v96\n"
    "v_mov_b32 v100, v97\n"
    "v_mov_b32 v101, v98\n"
    "s_nop 1\n"
    "v_permlane16_swap_b32 v96, v99\n"
    "v_permlane16_swap_b32 v97, v100\n"
    "v_permlane16_swap_b32 v98, v101\n"
    "v_max_f32 v96, v96, v99\n"
    "v_max_f32 v97, v97, v100\n"
    "v_max_f32 v98, v98, v101\n"
    "v_ceil_f32 v96, v96\n"
    "v_ceil_f32 v97, v97\n"
    "v_ceil_f32 v98, v98\n"
    "v_sub_f32 v99, 0, v96\n"
    "v_sub_f32 v100, 0, v97\n"
    "v_sub_f32 v101, 0, v98\n"
    "v_exp_f32 v99, v99\n"
    "v_exp_f32 v100, v100\n"
    "v_exp_f32 v101, v101\n"
    "s_nop 1\n"
    "v_mul_f32 v240, v240, v99\n"
    "v_mul_f32 v241, v241, v99\n"
    "v_mul_f32 v242, v242, v99\n"
    "v_mul_f32 v243, v243, v99\n"
    "v_sub_f32 v144, v144, v96\n"
    "v_sub_f32 v108, v108, v96\n"
    "v_sub_f32 v145, v145, v96\n"
    "v_sub_f32 v109, v109, v96\n"
    "v_sub_f32 v146, v146, v96\n"
    "v_sub_f32 v110, v110, v96\n"
    "v_sub_f32 v147, v147, v96\n"
    "v_sub_f32 v111, v111, v96\n"
    "v_lshlrev_b32 v102, 16, v120\n"
    "v_and_b32 v103, 4294901760, v120\n"
    "v_mul_f32 v102, v102, v99\n"
    "v_mul_f32 v103, v103, v99\n"
    "v_cvt_pk_bf16_f32 v120, v102, v103\n"
    "v_lshlrev_b32 v102, 16, v121\n"
    "v_and_b32 v103, 4294901760, v121\n"
    "v_mul_f32 v102, v102, v99\n"
    "v_mul_f32 v103, v103, v99\n"
    "v_cvt_pk_bf16_f32 v121, v102, v103\n"
    "v_lshlrev_b32 v102, 16, v122\n"
    "v_and_b32 v103, 4294901760, v122\n"
    "v_mul_f32 v102, v102, v99\n"
    "v_mul_f32 v103, v103, v99\n"
    "v_cvt_pk_bf16_f32 v122, v102, v103\n"
    "v_lshlrev_b32 v102, 16, v123\n"
    "v_and_b32 v103, 4294901760, v123\n"
    "v_mul_f32 v102, v102, v99\n"
    "v_mul_f32 v103, v103, v99\n"
    "v_cvt_pk_bf16_f32 v123, v102, v103\n"
    "v_lshlrev_b32 v102, 16, v132\n"
    "v_and_b32 v103, 4294901760, v132\n"
    "v_mul_f32 v102, v102, v99\n"
    "v_mul_f32 v103, v103, v99\n"
    "v_cvt_pk_bf16_f32 v132, v102, v103\n"
    "v_lshlrev_b32 v102, 16, v133\n"
    "v_and_b32 v103, 4294901760, v133\n"
    "v_mul_f32 v102, v102, v99\n"
    "v_mul_f32 v103, v103, v99\n"
    "v_cvt_pk_bf16_f32 v133, v102, v103\n"
    "v_accvgpr_read_b32 v102, a0\n"
    "v_mul_f32 v102, v102, v99\n"
    "v_accvgpr_write_b32 a0, v102\n"
    "v_accvgpr_read_b32 v102, a1\n"
    "v_mul_f32 v102, v102, v99\n"
    "v_accvgpr_write_b32 a1, v102\n"
    "v_accvgpr_read_b32 v102, a2\n"
    "v_mul_f32 v102, v102, v99\n"
    "v_accvgpr_write_b32 a2, v102\n"
    "v_accvgpr_read_b32 v102, a3\n"
    "v_mul_f32 v102, v102, v99\n"
    "v_accvgpr_write_b32 a3, v102\n"
    "v_accvgpr_read_b32 v102, a4\n"
    "v_mul_f32 v102, v102, v99\n"
    "v_accvgpr_write_b32 a4, v102\n"
    "v_accvgpr_read_b32 v102, a5\n"
    "v_mul_f32 v102, v102, v99\n"
    "v_accvgpr_write_b32 a5, v102\n"
    "v_accvgpr_read_b32 v102, a6\n"
    "v_mul_f32 v102, v102, v99\n"
    "v_accvgpr_write_b32 a6, v102\n"
    "v_accvgpr_read_b32 v102, a7\n"
    "v_mul_f32 v102, v102, v99\n"
    "v_accvgpr_write_b32 a7, v102\n"
    "v_accvgpr_read_b32 v102, a24\n"
    "v_mul_f32 v102, v102, v99\n"
    "v_accvgpr_write_b32 a24, v102\n"
    "v_accvgpr_read_b32 v102, a25\n"
    "v_mul_f32 v102, v102, v99\n"
    "v_accvgpr_write_b32 a25, v102\n"
    "v_accvgpr_read_b32 v102, a26\n"
    "v_mul_f32 v102, v102, v99\n"
    "v_accvgpr_write_b32 a26, v102\n"
    "v_accvgpr_read_b32 v102, a27\n"
    "v_mul_f32 v102, v102, v99\n"
    "v_accvgpr_write_b32 a27, v102\n"
    "v_accvgpr_read_b32 v102, a28\n"
    "v_mul_f32 v102, v102, v99\n"
    "v_accvgpr_write_b32 a28, v102\n"
    "v_accvgpr_read_b32 v102, a29\n"
    "v_mul_f32 v102, v102, v99\n"
    "v_accvgpr_write_b32 a29, v102\n"
    "v_accvgpr_read_b32 v102, a30\n"
    "v_mul_f32 v102, v102, v99\n"
    "v_accvgpr_write_b32 a30, v102\n"
    "v_accvgpr_read_b32 v102, a31\n"
    "v_mul_f32 v102, v102, v99\n"
    "v_accvgpr_write_b32 a31, v102\n"
    "v_accvgpr_read_b32 v102, a48\n"
    "v_mul_f32 v102, v102, v99\n"
    "v_accvgpr_write_b32 a48, v102\n"
    "v_accvgpr_read_b32 v102, a49\n"
    "v_mul_f32 v102, v102, v99\n"
    "v_accvgpr_write_b32 a49, v102\n"
    "v_accvgpr_read_b32 v102, a50\n"
    "v_mul_f32 v102, v102, v99\n"
    "v_accvgpr_write_b32 a50, v102\n"
    "v_accvgpr_read_b32 v102, a51\n"
    "v_mul_f32 v102, v102, v99\n"
    "v_accvgpr_write_b32 a51, v102\n"
    "v_accvgpr_read_b32 v102, a52\n"
    "v_mul_f32 v102, v102, v99\n"
    "v_accvgpr_write_b32 a52, v102\n"
    "v_accvgpr_read_b32 v102, a53\n"
    "v_mul_f32 v102, v102, v99\n"
    "v_accvgpr_write_b32 a53, v102\n"
    "v_accvgpr_read_b32 v102, a54\n"
    "v_mul_f32 v102, v102, v99\n"
    "v_accvgpr_write_b32 a54, v102\n"
    "v_accvgpr_read_b32 v102, a55\n"
    "v_mul_f32 v102, v102, v99\n"
    "v_accvgpr_write_b32 a55, v102\n"
    "v_accvgpr_read_b32 v102, a72\n"
    "v_mul_f32 v102, v102, v99\n"
    "v_accvgpr_write_b32 a72, v102\n"
    "v_accvgpr_read_b32 v102, a73\n"
    "v_mul_f32 v102, v102, v99\n"
    "v_accvgpr_write_b32 a73, v102\n"
    "v_accvgpr_read_b32 v102, a74\n"
    "v_mul_f32 v102, v102, v99\n"
    "v_accvgpr_write_b32 a74, v102\n"
    "v_accvgpr_read_b32 v102, a75\n"
    "v_mul_f32 v102, v102, v99\n"
    "v_accvgpr_write_b32 a75, v102\n"
    "v_accvgpr_read_b32 v102, a76\n"
    "v_mul_f32 v102, v102, v99\n"
    "v_accvgpr_write_b32 a76, v102\n"
    "v_accvgpr_read_b32 v102, a77\n"
    "v_mul_f32 v102, v102, v99\n"
    "v_accvgpr_write_b32 a77, v102\n"
    "v_accvgpr_read_b32 v102, a78\n"
    "v_mul_f32 v102, v102, v99\n"
    "v_accvgpr_write_b32 a78, v102\n"
    "v_accvgpr_read_b32 v102, a79\n"
    "v_mul_f32 v102, v102, v99\n"
    "v_accvgpr_write_b32 a79, v102\n"
    "v_accvgpr_read_b32 v102, a96\n"
    "v_mul_f32 v102, v102, v99\n"
    "v_accvgpr_write_b32 a96, v102\n"
    "v_accvgpr_read_b32 v102, a97\n"
    "v_mul_f32 v102, v102, v99\n"
    "v_accvgpr_write_b32 a97, v102\n"
    "v_accvgpr_read_b32 v102, a98\n"
    "v_mul_f32 v102, v102, v99\n"
    "v_accvgpr_write_b32 a98, v102\n"
    "v_accvgpr_read_b32 v102, a99\n"
    "v_mul_f32 v102, v102, v99\n"
    "v_accvgpr_write_b32 a99, v102\n"
    "v_accvgpr_read_b32 v102, a100\n"
    "v_mul_f32 v102, v102, v99\n"
    "v_accvgpr_write_b32 a100, v102\n"
    "v_accvgpr_read_b32 v102, a101\n"
    "v_mul_f32 v102, v102, v99\n"
    "v_accvgpr_write_b32 a101, v102\n"
    "v_accvgpr_read_b32 v102, a102\n"
    "v_mul_f32 v102, v102, v99\n"
    "v_accvgpr_write_b32 a102, v102\n"
    "v_accvgpr_read_b32 v102, a103\n"
    "v_mul_f32 v102, v102, v99\n"
    "v_accvgpr_write_b32 a103, v102\n"
    "v_accvgpr_read_b32 v102, a120\n"
    "v_mul_f32 v102, v102, v99\n"
    "v_accvgpr_write_b32 a120, v102\n"
    "v_accvgpr_read_b32 v102, a121\n"
    "v_mul_f32 v102, v102, v99\n"
    "v_accvgpr_write_b32 a121, v102\n"
    "v_accvgpr_read_b32 v102, a122\n"
    "v_mul_f32 v102, v102, v99\n"
    "v_accvgpr_write_b32 a122, v102\n"
    "v_accvgpr_read_b32 v102, a123\n"
    "v_mul_f32 v102, v102, v99\n"
    "v_accvgpr_write_b32 a123, v102\n"
    "v_accvgpr_read_b32 v102, a124\n"
    "v_mul_f32 v102, v102, v99\n"
    "v_accvgpr_write_b32 a124, v102\n"
    "v_accvgpr_read_b32 v102, a125\n"
    "v_mul_f32 v102, v102, v99\n"
    "v_accvgpr_write_b32 a125, v102\n"
    "v_accvgpr_read_b32 v102, a126\n"
    "v_mul_f32 v102, v102, v99\n"
    "v_accvgpr_write_b32 a126, v102\n"
    "v_accvgpr_read_b32 v102, a127\n"
    "v_mul_f32 v102, v102, v99\n"
    "v_accvgpr_write_b32 a127, v102\n"
    "v_accvgpr_read_b32 v102, a144\n"
    "v_mul_f32 v102, v102, v99\n"
    "v_accvgpr_write_b32 a144, v102\n"
    "v_accvgpr_read_b32 v102, a145\n"
    "v_mul_f32 v102, v102, v99\n"
    "v_accvgpr_write_b32 a145, v102\n"
    "v_accvgpr_read_b32 v102, a146\n"
    "v_mul_f32 v102, v102, v99\n"
    "v_accvgpr_write_b32 a146, v102\n"
    "v_accvgpr_read_b32 v102, a147\n"
    "v_mul_f32 v102, v102, v99\n"
    "v_accvgpr_write_b32 a147, v102\n"
    "v_accvgpr_read_b32 v102, a148\n"
    "v_mul_f32 v102, v102, v99\n"
    "v_accvgpr_write_b32 a148, v102\n"
    "v_accvgpr_read_b32 v102, a149\n"
    "v_mul_f32 v102, v102, v99\n"
    "v_accvgpr_write_b32 a149, v102\n"
    "v_accvgpr_read_b32 v102, a150\n"
    "v_mul_f32 v102, v102, v99\n"
    "v_accvgpr_write_b32 a150, v102\n"
    "v_accvgpr_read_b32 v102, a151\n"
    "v_mul_f32 v102, v102, v99\n"
    "v_accvgpr_write_b32 a151, v102\n"
    "v_accvgpr_read_b32 v102, a168\n"
    "v_mul_f32 v102, v102, v99\n"
    "v_accvgpr_write_b32 a168, v102\n"
    "v_accvgpr_read_b32 v102, a169\n"
    "v_mul_f32 v102, v102, v99\n"
    "v_accvgpr_write_b32 a169, v102\n"
    "v_accvgpr_read_b32 v102, a170\n"
    "v_mul_f32 v102, v102, v99\n"
    "v_accvgpr_write_b32 a170, v102\n"
    "v_accvgpr_read_b32 v102, a171\n"
    "v_mul_f32 v102, v102, v99\n"
    "v_accvgpr_write_b32 a171, v102\n"
    "v_accvgpr_read_b32 v102, a172\n"
    "v_mul_f32 v102, v102, v99\n"
    "v_accvgpr_write_b32 a172, v102\n"
    "v_accvgpr_read_b32 v102, a173\n"
    "v_mul_f32 v102, v102, v99\n"
    "v_accvgpr_write_b32 a173, v102\n"
    "v_accvgpr_read_b32 v102, a174\n"
    "v_mul_f32 v102, v102, v99\n"
    "v_accvgpr_write_b32 a174, v102\n"
    "v_accvgpr_read_b32 v102, a175\n"
    "v_mul_f32 v102, v102, v99\n"
    "v_accvgpr_write_b32 a175, v102\n"
    "v_mul_f32 v244, v244, v100\n"
    "v_mul_f32 v245, v245, v100\n"
    "v_mul_f32 v246, v246, v100\n"
    "v_mul_f32 v247, v247, v100\n"
    "v_sub_f32 v148, v148, v97\n"
    "v_sub_f32 v112, v112, v97\n"
    "v_sub_f32 v149, v149, v97\n"
    "v_sub_f32 v113, v113, v97\n"
    "v_sub_f32 v150, v150, v97\n"
    "v_sub_f32 v114, v114, v97\n"
    "v_sub_f32 v151, v151, v97\n"
    "v_sub_f32 v115, v115, v97\n"
    "v_lshlrev_b32 v102, 16, v124\n"
    "v_and_b32 v103, 4294901760, v124\n"
    "v_mul_f32 v102, v102, v100\n"
    "v_mul_f32 v103, v103, v100\n"
    "v_cvt_pk_bf16_f32 v124, v102, v103\n"
    "v_lshlrev_b32 v102, 16, v125\n"
    "v_and_b32 v103, 4294901760, v125\n"
    "v_mul_f32 v102, v102, v100\n"
    "v_mul_f32 v103, v103, v100\n"
    "v_cvt_pk_bf16_f32 v125, v102, v103\n"
    "v_lshlrev_b32 v102, 16, v126\n"
    "v_and_b32 v103, 4294901760, v126\n"
    "v_mul_f32 v102, v102, v100\n"
    "v_mul_f32 v103, v103, v100\n"
    "v_cvt_pk_bf16_f32 v126, v102, v103\n"
    "v_lshlrev_b32 v102, 16, v127\n"
    "v_and_b32 v103, 4294901760, v127\n"
    "v_mul_f32 v102, v102, v100\n"
    "v_mul_f32 v103, v103, v100\n"
    "v_cvt_pk_bf16_f32 v127, v102, v103\n"
    "v_lshlrev_b32 v102, 16, v136\n"
    "v_and_b32 v103, 4294901760, v136\n"
    "v_mul_f32 v102, v102, v100\n"
    "v_mul_f32 v103, v103, v100\n"
    "v_cvt_pk_bf16_f32 v136, v102, v103\n"
    "v_lshlrev_b32 v102, 16, v137\n"
    "v_and_b32 v103, 4294901760, v137\n"
    "v_mul_f32 v102, v102, v100\n"
    "v_mul_f32 v103, v103, v100\n"
    "v_cvt_pk_bf16_f32 v137, v102, v103\n"
    "v_accvgpr_read_b32 v102, a8\n"
    "v_mul_f32 v102, v102, v100\n"
    "v_accvgpr_write_b32 a8, v102\n"
    "v_accvgpr_read_b32 v102, a9\n"
    "v_mul_f32 v102, v102, v100\n"
    "v_accvgpr_write_b32 a9, v102\n"
    "v_accvgpr_read_b32 v102, a10\n"
    "v_mul_f32 v102, v102, v100\n"
    "v_accvgpr_write_b32 a10, v102\n"
    "v_accvgpr_read_b32 v102, a11\n"
    "v_mul_f32 v102, v102, v100\n"
    "v_accvgpr_write_b32 a11, v102\n"
    "v_accvgpr_read_b32 v102, a12\n"
    "v_mul_f32 v102, v102, v100\n"
    "v_accvgpr_write_b32 a12, v102\n"
    "v_accvgpr_read_b32 v102, a13\n"
    "v_mul_f32 v102, v102, v100\n"
    "v_accvgpr_write_b32 a13, v102\n"
    "v_accvgpr_read_b32 v102, a14\n"
    "v_mul_f32 v102, v102, v100\n"
    "v_accvgpr_write_b32 a14, v102\n"
    "v_accvgpr_read_b32 v102, a15\n"
    "v_mul_f32 v102, v102, v100\n"
    "v_accvgpr_write_b32 a15, v102\n"
    "v_accvgpr_read_b32 v102, a32\n"
    "v_mul_f32 v102, v102, v100\n"
    "v_accvgpr_write_b32 a32, v102\n"
    "v_accvgpr_read_b32 v102, a33\n"
    "v_mul_f32 v102, v102, v100\n"
    "v_accvgpr_write_b32 a33, v102\n"
    "v_accvgpr_read_b32 v102, a34\n"
    "v_mul_f32 v102, v102, v100\n"
    "v_accvgpr_write_b32 a34, v102\n"
    "v_accvgpr_read_b32 v102, a35\n"
    "v_mul_f32 v102, v102, v100\n"
    "v_accvgpr_write_b32 a35, v102\n"
    "v_accvgpr_read_b32 v102, a36\n"
    "v_mul_f32 v102, v102, v100\n"
    "v_accvgpr_write_b32 a36, v102\n"
    "v_accvgpr_read_b32 v102, a37\n"
    "v_mul_f32 v102, v102, v100\n"
    "v_accvgpr_write_b32 a37, v102\n"
    "v_accvgpr_read_b32 v102, a38\n"
    "v_mul_f32 v102, v102, v100\n"
    "v_accvgpr_write_b32 a38, v102\n"
    "v_accvgpr_read_b32 v102, a39\n"
    "v_mul_f32 v102, v102, v100\n"
    "v_accvgpr_write_b32 a39, v102\n"
    "v_accvgpr_read_b32 v102, a56\n"
    "v_mul_f32 v102, v102, v100\n"
    "v_accvgpr_write_b32 a56, v102\n"
    "v_accvgpr_read_b32 v102, a57\n"
    "v_mul_f32 v102, v102, v100\n"
    "v_accvgpr_write_b32 a57, v102\n"
    "v_accvgpr_read_b32 v102, a58\n"
    "v_mul_f32 v102, v102, v100\n"
    "v_accvgpr_write_b32 a58, v102\n"
    "v_accvgpr_read_b32 v102, a59\n"
    "v_mul_f32 v102, v102, v100\n"
    "v_accvgpr_write_b32 a59, v102\n"
    "v_accvgpr_read_b32 v102, a60\n"
    "v_mul_f32 v102, v102, v100\n"
    "v_accvgpr_write_b32 a60, v102\n"
    "v_accvgpr_read_b32 v102, a61\n"
    "v_mul_f32 v102, v102, v100\n"
    "v_accvgpr_write_b32 a61, v102\n"
    "v_accvgpr_read_b32 v102, a62\n"
    "v_mul_f32 v102, v102, v100\n"
    "v_accvgpr_write_b32 a62, v102\n"
    "v_accvgpr_read_b32 v102, a63\n"
    "v_mul_f32 v102, v102, v100\n"
    "v_accvgpr_write_b32 a63, v102\n"
    "v_accvgpr_read_b32 v102, a80\n"
    "v_mul_f32 v102, v102, v100\n"
    "v_accvgpr_write_b32 a80, v102\n"
    "v_accvgpr_read_b32 v102, a81\n"
    "v_mul_f32 v102, v102, v100\n"
    "v_accvgpr_write_b32 a81, v102\n"
    "v_accvgpr_read_b32 v102, a82\n"
    "v_mul_f32 v102, v102, v100\n"
    "v_accvgpr_write_b32 a82, v102\n"
    "v_accvgpr_read_b32 v102, a83\n"
    "v_mul_f32 v102, v102, v100\n"
    "v_accvgpr_write_b32 a83, v102\n"
    "v_accvgpr_read_b32 v102, a84\n"
    "v_mul_f32 v102, v102, v100\n"
    "v_accvgpr_write_b32 a84, v102\n"
    "v_accvgpr_read_b32 v102, a85\n"
    "v_mul_f32 v102, v102, v100\n"
    "v_accvgpr_write_b32 a85, v102\n"
    "v_accvgpr_read_b32 v102, a86\n"
    "v_mul_f32 v102, v102, v100\n"
    "v_accvgpr_write_b32 a86, v102\n"
    "v_accvgpr_read_b32 v102, a87\n"
    "v_mul_f32 v102, v102, v100\n"
    "v_accvgpr_write_b32 a87, v102\n"
    "v_accvgpr_read_b32 v102, a104\n"
    "v_mul_f32 v102, v102, v100\n"
    "v_accvgpr_write_b32 a104, v102\n"
    "v_accvgpr_read_b32 v102, a105\n"
    "v_mul_f32 v102, v102, v100\n"
    "v_accvgpr_write_b32 a105, v102\n"
    "v_accvgpr_read_b32 v102, a106\n"
    "v_mul_f32 v102, v102, v100\n"
    "v_accvgpr_write_b32 a106, v102\n"
    "v_accvgpr_read_b32 v102, a107\n"
    "v_mul_f32 v102, v102, v100\n"
    "v_accvgpr_write_b32 a107, v102\n"
    "v_accvgpr_read_b32 v102, a108\n"
    "v_mul_f32 v102, v102, v100\n"
    "v_accvgpr_write_b32 a108, v102\n"
    "v_accvgpr_read_b32 v102, a109\n"
    "v_mul_f32 v102, v102, v100\n"
    "v_accvgpr_write_b32 a109, v102\n"
    "v_accvgpr_read_b32 v102, a110\n"
    "v_mul_f32 v102, v102, v100\n"
    "v_accvgpr_write_b32 a110, v102\n"
    "v_accvgpr_read_b32 v102, a111\n"
    "v_mul_f32 v102, v102, v100\n"
    "v_accvgpr_write_b32 a111, v102\n"
    "v_accvgpr_read_b32 v102, a128\n"
    "v_mul_f32 v102, v102, v100\n"
    "v_accvgpr_write_b32 a128, v102\n"
    "v_accvgpr_read_b32 v102, a129\n"
    "v_mul_f32 v102, v102, v100\n"
    "v_accvgpr_write_b32 a129, v102\n"
    "v_accvgpr_read_b32 v102, a130\n"
    "v_mul_f32 v102, v102, v100\n"
    "v_accvgpr_write_b32 a130, v102\n"
    "v_accvgpr_read_b32 v102, a131\n"
    "v_mul_f32 v102, v102, v100\n"
    "v_accvgpr_write_b32 a131, v102\n"
    "v_accvgpr_read_b32 v102, a132\n"
    "v_mul_f32 v102, v102, v100\n"
    "v_accvgpr_write_b32 a132, v102\n"
    "v_accvgpr_read_b32 v102, a133\n"
    "v_mul_f32 v102, v102, v100\n"
    "v_accvgpr_write_b32 a133, v102\n"
    "v_accvgpr_read_b32 v102, a134\n"
    "v_mul_f32 v102, v102, v100\n"
    "v_accvgpr_write_b32 a134, v102\n"
    "v_accvgpr_read_b32 v102, a135\n"
    "v_mul_f32 v102, v102, v100\n"
    "v_accvgpr_write_b32 a135, v102\n"
    "v_accvgpr_read_b32 v102, a152\n"
    "v_mul_f32 v102, v102, v100\n"
    "v_accvgpr_write_b32 a152, v102\n"
    "v_accvgpr_read_b32 v102, a153\n"
    "v_mul_f32 v102, v102, v100\n"
    "v_accvgpr_write_b32 a153, v102\n"
    "v_accvgpr_read_b32 v102, a154\n"
    "v_mul_f32 v102, v102, v100\n"
    "v_accvgpr_write_b32 a154, v102\n"
    "v_accvgpr_read_b32 v102, a155\n"
    "v_mul_f32 v102, v102, v100\n"
    "v_accvgpr_write_b32 a155, v102\n"
    "v_accvgpr_read_b32 v102, a156\n"
    "v_mul_f32 v102, v102, v100\n"
    "v_accvgpr_write_b32 a156, v102\n"
    "v_accvgpr_read_b32 v102, a157\n"
    "v_mul_f32 v102, v102, v100\n"
    "v_accvgpr_write_b32 a157, v102\n"
    "v_accvgpr_read_b32 v102, a158\n"
    "v_mul_f32 v102, v102, v100\n"
    "v_accvgpr_write_b32 a158, v102\n"
    "v_accvgpr_read_b32 v102, a159\n"
    "v_mul_f32 v102, v102, v100\n"
    "v_accvgpr_write_b32 a159, v102\n"
    "v_accvgpr_read_b32 v102, a176\n"
    "v_mul_f32 v102, v102, v100\n"
    "v_accvgpr_write_b32 a176, v102\n"
    "v_accvgpr_read_b32 v102, a177\n"
    "v_mul_f32 v102, v102, v100\n"
    "v_accvgpr_write_b32 a177, v102\n"
    "v_accvgpr_read_b32 v102, a178\n"
    "v_mul_f32 v102, v102, v100\n"
    "v_accvgpr_write_b32 a178, v102\n"
    "v_accvgpr_read_b32 v102, a179\n"
    "v_mul_f32 v102, v102, v100\n"
    "v_accvgpr_write_b32 a179, v102\n"
    "v_accvgpr_read_b32 v102, a180\n"
    "v_mul_f32 v102, v102, v100\n"
    "v_accvgpr_write_b32 a180, v102\n"
    "v_accvgpr_read_b32 v102, a181\n"
    "v_mul_f32 v102, v102, v100\n"
    "v_accvgpr_write_b32 a181, v102\n"
    "v_accvgpr_read_b32 v102, a182\n"
    "v_mul_f32 v102, v102, v100\n"
    "v_accvgpr_write_b32 a182, v102\n"
    "v_accvgpr_read_b32 v102, a183\n"
    "v_mul_f32 v102, v102, v100\n"
    "v_accvgpr_write_b32 a183, v102\n"
    "v_mul_f32 v248, v248, v101\n"
    "v_mul_f32 v249, v249, v101\n"
    "v_mul_f32 v250, v250, v101\n"
    "v_mul_f32 v251, v251, v101\n"
    "v_sub_f32 v152, v152, v98\n"
    "v_sub_f32 v116, v116, v98\n"
    "v_sub_f32 v153, v153, v98\n"
    "v_sub_f32 v117, v117, v98\n"
    "v_sub_f32 v154, v154, v98\n"
    "v_sub_f32 v118, v118, v98\n"
    "v_sub_f32 v155, v155, v98\n"
    "v_sub_f32 v119, v119, v98\n"
    "v_lshlrev_b32 v102, 16, v128\n"
    "v_and_b32 v103, 4294901760, v128\n"
    "v_mul_f32 v102, v102, v101\n"
    "v_mul_f32 v103, v103, v101\n"
    "v_cvt_pk_bf16_f32 v128, v102, v103\n"
    "v_lshlrev_b32 v102, 16, v129\n"
    "v_and_b32 v103, 4294901760, v129\n"
    "v_mul_f32 v102, v102, v101\n"
    "v_mul_f32 v103, v103, v101\n"
    "v_cvt_pk_bf16_f32 v129, v102, v103\n"
    "v_lshlrev_b32 v102, 16, v130\n"
    "v_and_b32 v103, 4294901760, v130\n"
    "v_mul_f32 v102, v102, v101\n"
    "v_mul_f32 v103, v103, v101\n"
    "v_cvt_pk_bf16_f32 v130, v102, v103\n"
    "v_lshlrev_b32 v102, 16, v131\n"
    "v_and_b32 v103, 4294901760, v131\n"
    "v_mul_f32 v102, v102, v101\n"
    "v_mul_f32 v103, v103, v101\n"
    "v_cvt_pk_bf16_f32 v131, v102, v103\n"
    "v_lshlrev_b32 v102, 16, v140\n"
    "v_and_b32 v103, 4294901760, v140\n"
    "v_mul_f32 v102, v102, v101\n"
    "v_mul_f32 v103, v103, v101\n"
    "v_cvt_pk_bf16_f32 v140, v102, v103\n"
    "v_lshlrev_b32 v102, 16, v141\n"
    "v_and_b32 v103, 4294901760, v141\n"
    "v_mul_f32 v102, v102, v101\n"
    "v_mul_f32 v103, v103, v101\n"
    "v_cvt_pk_bf16_f32 v141, v102, v103\n"
    "v_accvgpr_read_b32 v102, a16\n"
    "v_mul_f32 v102, v102, v101\n"
    "v_accvgpr_write_b32 a16, v102\n"
    "v_accvgpr_read_b32 v102, a17\n"
    "v_mul_f32 v102, v102, v101\n"
    "v_accvgpr_write_b32 a17, v102\n"
    "v_accvgpr_read_b32 v102, a18\n"
    "v_mul_f32 v102, v102, v101\n"
    "v_accvgpr_write_b32 a18, v102\n"
    "v_accvgpr_read_b32 v102, a19\n"
    "v_mul_f32 v102, v102, v101\n"
    "v_accvgpr_write_b32 a19, v102\n"
    "v_accvgpr_read_b32 v102, a20\n"
    "v_mul_f32 v102, v102, v101\n"
    "v_accvgpr_write_b32 a20, v102\n"
    "v_accvgpr_read_b32 v102, a21\n"
    "v_mul_f32 v102, v102, v101\n"
    "v_accvgpr_write_b32 a21, v102\n"
    "v_accvgpr_read_b32 v102, a22\n"
    "v_mul_f32 v102, v102, v101\n"
    "v_accvgpr_write_b32 a22, v102\n"
    "v_accvgpr_read_b32 v102, a23\n"
    "v_mul_f32 v102, v102, v101\n"
    "v_accvgpr_write_b32 a23, v102\n"
    "v_accvgpr_read_b32 v102, a40\n"
    "v_mul_f32 v102, v102, v101\n"
    "v_accvgpr_write_b32 a40, v102\n"
    "v_accvgpr_read_b32 v102, a41\n"
    "v_mul_f32 v102, v102, v101\n"
    "v_accvgpr_write_b32 a41, v102\n"
    "v_accvgpr_read_b32 v102, a42\n"
    "v_mul_f32 v102, v102, v101\n"
    "v_accvgpr_write_b32 a42, v102\n"
    "v_accvgpr_read_b32 v102, a43\n"
    "v_mul_f32 v102, v102, v101\n"
    "v_accvgpr_write_b32 a43, v102\n"
    "v_accvgpr_read_b32 v102, a44\n"
    "v_mul_f32 v102, v102, v101\n"
    "v_accvgpr_write_b32 a44, v102\n"
    "v_accvgpr_read_b32 v102, a45\n"
    "v_mul_f32 v102, v102, v101\n"
    "v_accvgpr_write_b32 a45, v102\n"
    "v_accvgpr_read_b32 v102, a46\n"
    "v_mul_f32 v102, v102, v101\n"
    "v_accvgpr_write_b32 a46, v102\n"
    "v_accvgpr_read_b32 v102, a47\n"
    "v_mul_f32 v102, v102, v101\n"
    "v_accvgpr_write_b32 a47, v102\n"
    "v_accvgpr_read_b32 v102, a64\n"
    "v_mul_f32 v102, v102, v101\n"
    "v_accvgpr_write_b32 a64, v102\n"
    "v_accvgpr_read_b32 v102, a65\n"
    "v_mul_f32 v102, v102, v101\n"
    "v_accvgpr_write_b32 a65, v102\n"
    "v_accvgpr_read_b32 v102, a66\n"
    "v_mul_f32 v102, v102, v101\n"
    "v_accvgpr_write_b32 a66, v102\n"
    "v_accvgpr_read_b32 v102, a67\n"
    "v_mul_f32 v102, v102, v101\n"
    "v_accvgpr_write_b32 a67, v102\n"
    "v_accvgpr_read_b32 v102, a68\n"
    "v_mul_f32 v102, v102, v101\n"
    "v_accvgpr_write_b32 a68, v102\n"
    "v_accvgpr_read_b32 v102, a69\n"
    "v_mul_f32 v102, v102, v101\n"
    "v_accvgpr_write_b32 a69, v102\n"
    "v_accvgpr_read_b32 v102, a70\n"
    "v_mul_f32 v102, v102, v101\n"
    "v_accvgpr_write_b32 a70, v102\n"
    "v_accvgpr_read_b32 v102, a71\n"
    "v_mul_f32 v102, v102, v101\n"
    "v_accvgpr_write_b32 a71, v102\n"
    "v_accvgpr_read_b32 v102, a88\n"
    "v_mul_f32 v102, v102, v101\n"
    "v_accvgpr_write_b32 a88, v102\n"
    "v_accvgpr_read_b32 v102, a89\n"
    "v_mul_f32 v102, v102, v101\n"
    "v_accvgpr_write_b32 a89, v102\n"
    "v_accvgpr_read_b32 v102, a90\n"
    "v_mul_f32 v102, v102, v101\n"
    "v_accvgpr_write_b32 a90, v102\n"
    "v_accvgpr_read_b32 v102, a91\n"
    "v_mul_f32 v102, v102, v101\n"
    "v_accvgpr_write_b32 a91, v102\n"
    "v_accvgpr_read_b32 v102, a92\n"
    "v_mul_f32 v102, v102, v101\n"
    "v_accvgpr_write_b32 a92, v102\n"
    "v_accvgpr_read_b32 v102, a93\n"
    "v_mul_f32 v102, v102, v101\n"
    "v_accvgpr_write_b32 a93, v102\n"
    "v_accvgpr_read_b32 v102, a94\n"
    "v_mul_f32 v102, v102, v101\n"
    "v_accvgpr_write_b32 a94, v102\n"
    "v_accvgpr_read_b32 v102, a95\n"
    "v_mul_f32 v102, v102, v101\n"
    "v_accvgpr_write_b32 a95, v102\n"
    "v_accvgpr_read_b32 v102, a112\n"
    "v_mul_f32 v102, v102, v101\n"
    "v_accvgpr_write_b32 a112, v102\n"
    "v_accvgpr_read_b32 v102, a113\n"
    "v_mul_f32 v102, v102, v101\n"
    "v_accvgpr_write_b32 a113, v102\n"
    "v_accvgpr_read_b32 v102, a114\n"
    "v_mul_f32 v102, v102, v101\n"
    "v_accvgpr_write_b32 a114, v102\n"
    "v_accvgpr_read_b32 v102, a115\n"
    "v_mul_f32 v102, v102, v101\n"
    "v_accvgpr_write_b32 a115, v102\n"
    "v_accvgpr_read_b32 v102, a116\n"
    "v_mul_f32 v102, v102, v101\n"
    "v_accvgpr_write_b32 a116, v102\n"
    "v_accvgpr_read_b32 v102, a117\n"
    "v_mul_f32 v102, v102, v101\n"
    "v_accvgpr_write_b32 a117, v102\n"
    "v_accvgpr_read_b32 v102, a118\n"
    "v_mul_f32 v102, v102, v101\n"
    "v_accvgpr_write_b32 a118, v102\n"
    "v_accvgpr_read_b32 v102, a119\n"
    "v_mul_f32 v102, v102, v101\n"
    "v_accvgpr_write_b32 a119, v102\n"
    "v_accvgpr_read_b32 v102, a136\n"
    "v_mul_f32 v102, v102, v101\n"
    "v_accvgpr_write_b32 a136, v102\n"
    "v_accvgpr_read_b32 v102, a137\n"
    "v_mul_f32 v102, v102, v101\n"
    "v_accvgpr_write_b32 a137, v102\n"
    "v_accvgpr_read_b32 v102, a138\n"
    "v_mul_f32 v102, v102, v101\n"
    "v_accvgpr_write_b32 a138, v102\n"
    "v_accvgpr_read_b32 v102, a139\n"
    "v_mul_f32 v102, v102, v101\n"
    "v_accvgpr_write_b32 a139, v102\n"
    "v_accvgpr_read_b32 v102, a140\n"
    "v_mul_f32 v102, v102, v101\n"
    "v_accvgpr_write_b32 a140, v102\n"
    "v_accvgpr_read_b32 v102, a141\n"
    "v_mul_f32 v102, v102, v101\n"
    "v_accvgpr_write_b32 a141, v102\n"
    "v_accvgpr_read_b32 v102, a142\n"
    "v_mul_f32 v102, v102, v101\n"
    "v_accvgpr_write_b32 a142, v102\n"
    "v_accvgpr_read_b32 v102, a143\n"
    "v_mul_f32 v102, v102, v101\n"
    "v_accvgpr_write_b32 a143, v102\n"
    "v_accvgpr_read_b32 v102, a160\n"
    "v_mul_f32 v102, v102, v101\n"
    "v_accvgpr_write_b32 a160, v102\n"
    "v_accvgpr_read_b32 v102, a161\n"
    "v_mul_f32 v102, v102, v101\n"
    "v_accvgpr_write_b32 a161, v102\n"
    "v_accvgpr_read_b32 v102, a162\n"
    "v_mul_f32 v102, v102, v101\n"
    "v_accvgpr_write_b32 a162, v102\n"
    "v_accvgpr_read_b32 v102, a163\n"
    "v_mul_f32 v102, v102, v101\n"
    "v_accvgpr_write_b32 a163, v102\n"
    "v_accvgpr_read_b32 v102, a164\n"
    "v_mul_f32 v102, v102, v101\n"
    "v_accvgpr_write_b32 a164, v102\n"
    "v_accvgpr_read_b32 v102, a165\n"
    "v_mul_f32 v102, v102, v101\n"
    "v_accvgpr_write_b32 a165, v102\n"
    "v_accvgpr_read_b32 v102, a166\n"
    "v_mul_f32 v102, v102, v101\n"
    "v_accvgpr_write_b32 a166, v102\n"
    "v_accvgpr_read_b32 v102, a167\n"
    "v_mul_f32 v102, v102, v101\n"
    "v_accvgpr_write_b32 a167, v102\n"
    "v_accvgpr_read_b32 v102, a184\n"
    "v_mul_f32 v102, v102, v101\n"
    "v_accvgpr_write_b32 a184, v102\n"
    "v_accvgpr_read_b32 v102, a185\n"
    "v_mul_f32 v102, v102, v101\n"
    "v_accvgpr_write_b32 a185, v102\n"
    "v_accvgpr_read_b32 v102, a186\n"
    "v_mul_f32 v102, v102, v101\n"
    "v_accvgpr_write_b32 a186, v102\n"
    "v_accvgpr_read_b32 v102, a187\n"
    "v_mul_f32 v102, v102, v101\n"
    "v_accvgpr_write_b32 a187, v102\n"
    "v_accvgpr_read_b32 v102, a188\n"
    "v_mul_f32 v102, v102, v101\n"
    "v_accvgpr_write_b32 a188, v102\n"
    "v_accvgpr_read_b32 v102, a189\n"
    "v_mul_f32 v102, v102, v101\n"
    "v_accvgpr_write_b32 a189, v102\n"
    "v_accvgpr_read_b32 v102, a190\n"
    "v_mul_f32 v102, v102, v101\n"
    "v_accvgpr_write_b32 a190, v102\n"
    "v_accvgpr_read_b32 v102, a191\n"
    "v_mul_f32 v102, v102, v101\n"
    "v_accvgpr_write_b32 a191, v102\n"
    "s_nop 3\n"
    "s_setpc_b64 s[84:85]\n"
    "LRR_11:\n"
    "s_nop 15\n"
    "v_max3_f32 v96, v108, v109, v110\n"
    "v_max3_f32 v96, v96, v111, 0\n"
    "v_max3_f32 v97, v112, v113, v114\n"
    "v_max3_f32 v97, v97, v115, 0\n"
    "v_max3_f32 v98, v116, v117, v118\n"
    "v_max3_f32 v98, v98, v119, 0\n"
    "v_mov_b32 v99, v96\n"
    "v_mov_b32 v100, v97\n"
    "v_mov_b32 v101, v98\n"
    "s_nop 1\n"
    "v_permlane32_swap_b32 v96, v99\n"
    "v_permlane32_swap_b32 v97, v100\n"
    "v_permlane32_swap_b32 v98, v101\n"
    "v_max_f32 v96, v96, v99\n"
    "v_max_f32 v97, v97, v100\n"
    "v_max_f32 v98, v98, v101\n"
    "v_mov_b32 v99, v96\n"
    "v_mov_b32 v100, v97\n"
    "v_mov_b32 v101, v98\n"
    "s_nop 1\n"
    "v_permlane16_swap_b32 v96, v99\n"
    "v_permlane16_swap_b32 v97, v100\n"
    "v_permlane16_swap_b32 v98, v101\n"
    "v_max_f32 v96, v96, v99\n"
    "v_max_f32 v97, v97, v100\n"
    "v_max_f32 v98, v98, v101\n"
    "v_ceil_f32 v96, v96\n"
    "v_ceil_f32 v97, v97\n"
    "v_ceil_f32 v98, v98\n"
    "v_sub_f32 v99, 0, v96\n"
    "v_sub_f32 v100, 0, v97\n"
    "v_sub_f32 v101, 0, v98\n"
    "v_exp_f32 v99, v99\n"
    "v_exp_f32 v100, v100\n"
    "v_exp_f32 v101, v101\n"
    "s_nop 1\n"
    "v_mul_f32 v240, v240, v99\n"
    "v_mul_f32 v241, v241, v99\n"
    "v_mul_f32 v242, v242, v99\n"
    "v_mul_f32 v243, v243, v99\n"
    "v_sub_f32 v144, v144, v96\n"
    "v_sub_f32 v108, v108, v96\n"
    "v_sub_f32 v145, v145, v96\n"
    "v_sub_f32 v109, v109, v96\n"
    "v_sub_f32 v146, v146, v96\n"
    "v_sub_f32 v110, v110, v96\n"
    "v_sub_f32 v147, v147, v96\n"
    "v_sub_f32 v111, v111, v96\n"
    "v_lshlrev_b32 v102, 16, v132\n"
    "v_and_b32 v103, 4294901760, v132\n"
    "v_mul_f32 v102, v102, v99\n"
    "v_mul_f32 v103, v103, v99\n"
    "v_cvt_pk_bf16_f32 v132, v102, v103\n"
    "v_lshlrev_b32 v102, 16, v133\n"
    "v_and_b32 v103, 4294901760, v133\n"
    "v_mul_f32 v102, v102, v99\n"
    "v_mul_f32 v103, v103, v99\n"
    "v_cvt_pk_bf16_f32 v133, v102, v103\n"
    "v_lshlrev_b32 v102, 16, v134\n"
    "v_and_b32 v103, 4294901760, v134\n"
    "v_mul_f32 v102, v102, v99\n"
    "v_mul_f32 v103, v103, v99\n"
    "v_cvt_pk_bf16_f32 v134, v102, v103\n"
    "v_lshlrev_b32 v102, 16, v135\n"
    "v_and_b32 v103, 4294901760, v135\n"
    "v_mul_f32 v102, v102, v99\n"
    "v_mul_f32 v103, v103, v99\n"
    "v_cvt_pk_bf16_f32 v135, v102, v103\n"
    "v_lshlrev_b32 v102, 16, v120\n"
    "v_and_b32 v103, 4294901760, v120\n"
    "v_mul_f32 v102, v102, v99\n"
    "v_mul_f32 v103, v103, v99\n"
    "v_cvt_pk_bf16_f32 v120, v102, v103\n"
    "v_lshlrev_b32 v102, 16, v121\n"
    "v_and_b32 v103, 4294901760, v121\n"
    "v_mul_f32 v102, v102, v99\n"
    "v_mul_f32 v103, v103, v99\n"
    "v_cvt_pk_bf16_f32 v121, v102, v103\n"
    "v_accvgpr_read_b32 v102, a0\n"
    "v_mul_f32 v102, v102, v99\n"
    "v_accvgpr_write_b32 a0, v102\n"
    "v_accvgpr_read_b32 v102, a1\n"
    "v_mul_f32 v102, v102, v99\n"
    "v_accvgpr_write_b32 a1, v102\n"
    "v_accvgpr_read_b32 v102, a2\n"
    "v_mul_f32 v102, v102, v99\n"
    "v_accvgpr_write_b32 a2, v102\n"
    "v_accvgpr_read_b32 v102, a3\n"
    "v_mul_f32 v102, v102, v99\n"
    "v_accvgpr_write_b32 a3, v102\n"
    "v_accvgpr_read_b32 v102, a4\n"
    "v_mul_f32 v102, v102, v99\n"
    "v_accvgpr_write_b32 a4, v102\n"
    "v_accvgpr_read_b32 v102, a5\n"
    "v_mul_f32 v102, v102, v99\n"
    "v_accvgpr_write_b32 a5, v102\n"
    "v_accvgpr_read_b32 v102, a6\n"
    "v_mul_f32 v102, v102, v99\n"
    "v_accvgpr_write_b32 a6, v102\n"
    "v_accvgpr_read_b32 v102, a7\n"
    "v_mul_f32 v102, v102, v99\n"
    "v_accvgpr_write_b32 a7, v102\n"
    "v_accvgpr_read_b32 v102, a24\n"
    "v_mul_f32 v102, v102, v99\n"
    "v_accvgpr_write_b32 a24, v102\n"
    "v_accvgpr_read_b32 v102, a25\n"
    "v_mul_f32 v102, v102, v99\n"
    "v_accvgpr_write_b32 a25, v102\n"
    "v_accvgpr_read_b32 v102, a26\n"
    "v_mul_f32 v102, v102, v99\n"
    "v_accvgpr_write_b32 a26, v102\n"
    "v_accvgpr_read_b32 v102, a27\n"
    "v_mul_f32 v102, v102, v99\n"
    "v_accvgpr_write_b32 a27, v102\n"
    "v_accvgpr_read_b32 v102, a28\n"
    "v_mul_f32 v102, v102, v99\n"
    "v_accvgpr_write_b32 a28, v102\n"
    "v_accvgpr_read_b32 v102, a29\n"
    "v_mul_f32 v102, v102, v99\n"
    "v_accvgpr_write_b32 a29, v102\n"
    "v_accvgpr_read_b32 v102, a30\n"
    "v_mul_f32 v102, v102, v99\n"
    "v_accvgpr_write_b32 a30, v102\n"
    "v_accvgpr_read_b32 v102, a31\n"
    "v_mul_f32 v102, v102, v99\n"
    "v_accvgpr_write_b32 a31, v102\n"
    "v_accvgpr_read_b32 v102, a48\n"
    "v_mul_f32 v102, v102, v99\n"
    "v_accvgpr_write_b32 a48, v102\n"
    "v_accvgpr_read_b32 v102, a49\n"
    "v_mul_f32 v102, v102, v99\n"
    "v_accvgpr_write_b32 a49, v102\n"
    "v_accvgpr_read_b32 v102, a50\n"
    "v_mul_f32 v102, v102, v99\n"
    "v_accvgpr_write_b32 a50, v102\n"
    "v_accvgpr_read_b32 v102, a51\n"
    "v_mul_f32 v102, v102, v99\n"
    "v_accvgpr_write_b32 a51, v102\n"
    "v_accvgpr_read_b32 v102, a52\n"
    "v_mul_f32 v102, v102, v99\n"
    "v_accvgpr_write_b32 a52, v102\n"
    "v_accvgpr_read_b32 v102, a53\n"
    "v_mul_f32 v102, v102, v99\n"
    "v_accvgpr_write_b32 a53, v102\n"
    "v_accvgpr_read_b32 v102, a54\n"
    "v_mul_f32 v102, v102, v99\n"
    "v_accvgpr_write_b32 a54, v102\n"
    "v_accvgpr_read_b32 v102, a55\n"
    "v_mul_f32 v102, v102, v99\n"
    "v_accvgpr_write_b32 a55, v102\n"
    "v_accvgpr_read_b32 v102, a72\n"
    "v_mul_f32 v102, v102, v99\n"
    "v_accvgpr_write_b32 a72, v102\n"
    "v_accvgpr_read_b32 v102, a73\n"
    "v_mul_f32 v102, v102, v99\n"
    "v_accvgpr_write_b32 a73, v102\n"
    "v_accvgpr_read_b32 v102, a74\n"
    "v_mul_f32 v102, v102, v99\n"
    "v_accvgpr_write_b32 a74, v102\n"
    "v_accvgpr_read_b32 v102, a75\n"
    "v_mul_f32 v102, v102, v99\n"
    "v_accvgpr_write_b32 a75, v102\n"
    "v_accvgpr_read_b32 v102, a76\n"
    "v_mul_f32 v102, v102, v99\n"
    "v_accvgpr_write_b32 a76, v102\n"
    "v_accvgpr_read_b32 v102, a77\n"
    "v_mul_f32 v102, v102, v99\n"
    "v_accvgpr_write_b32 a77, v102\n"
    "v_accvgpr_read_b32 v102, a78\n"
    "v_mul_f32 v102, v102, v99\n"
    "v_accvgpr_write_b32 a78, v102\n"
    "v_accvgpr_read_b32 v102, a79\n"
    "v_mul_f32 v102, v102, v99\n"
    "v_accvgpr_write_b32 a79, v102\n"
    "v_accvgpr_read_b32 v102, a96\n"
    "v_mul_f32 v102, v102, v99\n"
    "v_accvgpr_write_b32 a96, v102\n"
    "v_accvgpr_read_b32 v102, a97\n"
    "v_mul_f32 v102, v102, v99\n"
    "v_accvgpr_write_b32 a97, v102\n"
    "v_accvgpr_read_b32 v102, a98\n"
    "v_mul_f32 v102, v102, v99\n"
    "v_accvgpr_write_b32 a98, v102\n"
    "v_accvgpr_read_b32 v102, a99\n"
    "v_mul_f32 v102, v102, v99\n"
    "v_accvgpr_write_b32 a99, v102\n"
    "v_accvgpr_read_b32 v102, a100\n"
    "v_mul_f32 v102, v102, v99\n"
    "v_accvgpr_write_b32 a100, v102\n"
    "v_accvgpr_read_b32 v102, a101\n"
    "v_mul_f32 v102, v102, v99\n"
    "v_accvgpr_write_b32 a101, v102\n"
    "v_accvgpr_read_b32 v102, a102\n"
    "v_mul_f32 v102, v102, v99\n"
    "v_accvgpr_write_b32 a102, v102\n"
    "v_accvgpr_read_b32 v102, a103\n"
    "v_mul_f32 v102, v102, v99\n"
    "v_accvgpr_write_b32 a103, v102\n"
    "v_accvgpr_read_b32 v102, a120\n"
    "v_mul_f32 v102, v102, v99\n"
    "v_accvgpr_write_b32 a120, v102\n"
    "v_accvgpr_read_b32 v102, a121\n"
    "v_mul_f32 v102, v102, v99\n"
    "v_accvgpr_write_b32 a121, v102\n"
    "v_accvgpr_read_b32 v102, a122\n"
    "v_mul_f32 v102, v102, v99\n"
    "v_accvgpr_write_b32 a122, v102\n"
    "v_accvgpr_read_b32 v102, a123\n"
    "v_mul_f32 v102, v102, v99\n"
    "v_accvgpr_write_b32 a123, v102\n"
    "v_accvgpr_read_b32 v102, a124\n"
    "v_mul_f32 v102, v102, v99\n"
    "v_accvgpr_write_b32 a124, v102\n"
    "v_accvgpr_read_b32 v102, a125\n"
    "v_mul_f32 v102, v102, v99\n"
    "v_accvgpr_write_b32 a125, v102\n"
    "v_accvgpr_read_b32 v102, a126\n"
    "v_mul_f32 v102, v102, v99\n"
    "v_accvgpr_write_b32 a126, v102\n"
    "v_accvgpr_read_b32 v102, a127\n"
    "v_mul_f32 v102, v102, v99\n"
    "v_accvgpr_write_b32 a127, v102\n"
    "v_accvgpr_read_b32 v102, a144\n"
    "v_mul_f32 v102, v102, v99\n"
    "v_accvgpr_write_b32 a144, v102\n"
    "v_accvgpr_read_b32 v102, a145\n"
    "v_mul_f32 v102, v102, v99\n"
    "v_accvgpr_write_b32 a145, v102\n"
    "v_accvgpr_read_b32 v102, a146\n"
    "v_mul_f32 v102, v102, v99\n"
    "v_accvgpr_write_b32 a146, v102\n"
    "v_accvgpr_read_b32 v102, a147\n"
    "v_mul_f32 v102, v102, v99\n"
    "v_accvgpr_write_b32 a147, v102\n"
    "v_accvgpr_read_b32 v102, a148\n"
    "v_mul_f32 v102, v102, v99\n"
    "v_accvgpr_write_b32 a148, v102\n"
    "v_accvgpr_read_b32 v102, a149\n"
    "v_mul_f32 v102, v102, v99\n"
    "v_accvgpr_write_b32 a149, v102\n"
    "v_accvgpr_read_b32 v102, a150\n"
    "v_mul_f32 v102, v102, v99\n"
    "v_accvgpr_write_b32 a150, v102\n"
    "v_accvgpr_read_b32 v102, a151\n"
    "v_mul_f32 v102, v102, v99\n"
    "v_accvgpr_write_b32 a151, v102\n"
    "v_accvgpr_read_b32 v102, a168\n"
    "v_mul_f32 v102, v102, v99\n"
    "v_accvgpr_write_b32 a168, v102\n"
    "v_accvgpr_read_b32 v102, a169\n"
    "v_mul_f32 v102, v102, v99\n"
    "v_accvgpr_write_b32 a169, v102\n"
    "v_accvgpr_read_b32 v102, a170\n"
    "v_mul_f32 v102, v102, v99\n"
    "v_accvgpr_write_b32 a170, v102\n"
    "v_accvgpr_read_b32 v102, a171\n"
    "v_mul_f32 v102, v102, v99\n"
    "v_accvgpr_write_b32 a171, v102\n"
    "v_accvgpr_read_b32 v102, a172\n"
    "v_mul_f32 v102, v102, v99\n"
    "v_accvgpr_write_b32 a172, v102\n"
    "v_accvgpr_read_b32 v102, a173\n"
    "v_mul_f32 v102, v102, v99\n"
    "v_accvgpr_write_b32 a173, v102\n"
    "v_accvgpr_read_b32 v102, a174\n"
    "v_mul_f32 v102, v102, v99\n"
    "v_accvgpr_write_b32 a174, v102\n"
    "v_accvgpr_read_b32 v102, a175\n"
    "v_mul_f32 v102, v102, v99\n"
    "v_accvgpr_write_b32 a175, v102\n"
    "v_mul_f32 v244, v244, v100\n"
    "v_mul_f32 v245, v245, v100\n"
    "v_mul_f32 v246, v246, v100\n"
    "v_mul_f32 v247, v247, v100\n"
    "v_sub_f32 v148, v148, v97\n"
    "v_sub_f32 v112, v112, v97\n"
    "v_sub_f32 v149, v149, v97\n"
    "v_sub_f32 v113, v113, v97\n"
    "v_sub_f32 v150, v150, v97\n"
    "v_sub_f32 v114, v114, v97\n"
    "v_sub_f32 v151, v151, v97\n"
    "v_sub_f32 v115, v115, v97\n"
    "v_lshlrev_b32 v102, 16, v136\n"
    "v_and_b32 v103, 4294901760, v136\n"
    "v_mul_f32 v102, v102, v100\n"
    "v_mul_f32 v103, v103, v100\n"
    "v_cvt_pk_bf16_f32 v136, v102, v103\n"
    "v_lshlrev_b32 v102, 16, v137\n"
    "v_and_b32 v103, 4294901760, v137\n"
    "v_mul_f32 v102, v102, v100\n"
    "v_mul_f32 v103, v103, v100\n"
    "v_cvt_pk_bf16_f32 v137, v102, v103\n"
    "v_lshlrev_b32 v102, 16, v138\n"
    "v_and_b32 v103, 4294901760, v138\n"
    "v_mul_f32 v102, v102, v100\n"
    "v_mul_f32 v103, v103, v100\n"
    "v_cvt_pk_bf16_f32 v138, v102, v103\n"
    "v_lshlrev_b32 v102, 16, v139\n"
    "v_and_b32 v103, 4294901760, v139\n"
    "v_mul_f32 v102, v102, v100\n"
    "v_mul_f32 v103, v103, v100\n"
    "v_cvt_pk_bf16_f32 v139, v102, v103\n"
    "v_lshlrev_b32 v102, 16, v124\n"
    "v_and_b32 v103, 4294901760, v124\n"
    "v_mul_f32 v102, v102, v100\n"
    "v_mul_f32 v103, v103, v100\n"
    "v_cvt_pk_bf16_f32 v124, v102, v103\n"
    "v_lshlrev_b32 v102, 16, v125\n"
    "v_and_b32 v103, 4294901760, v125\n"
    "v_mul_f32 v102, v102, v100\n"
    "v_mul_f32 v103, v103, v100\n"
    "v_cvt_pk_bf16_f32 v125, v102, v103\n"
    "v_accvgpr_read_b32 v102, a8\n"
    "v_mul_f32 v102, v102, v100\n"
    "v_accvgpr_write_b32 a8, v102\n"
    "v_accvgpr_read_b32 v102, a9\n"
    "v_mul_f32 v102, v102, v100\n"
    "v_accvgpr_write_b32 a9, v102\n"
    "v_accvgpr_read_b32 v102, a10\n"
    "v_mul_f32 v102, v102, v100\n"
    "v_accvgpr_write_b32 a10, v102\n"
    "v_accvgpr_read_b32 v102, a11\n"
    "v_mul_f32 v102, v102, v100\n"
    "v_accvgpr_write_b32 a11, v102\n"
    "v_accvgpr_read_b32 v102, a12\n"
    "v_mul_f32 v102, v102, v100\n"
    "v_accvgpr_write_b32 a12, v102\n"
    "v_accvgpr_read_b32 v102, a13\n"
    "v_mul_f32 v102, v102, v100\n"
    "v_accvgpr_write_b32 a13, v102\n"
    "v_accvgpr_read_b32 v102, a14\n"
    "v_mul_f32 v102, v102, v100\n"
    "v_accvgpr_write_b32 a14, v102\n"
    "v_accvgpr_read_b32 v102, a15\n"
    "v_mul_f32 v102, v102, v100\n"
    "v_accvgpr_write_b32 a15, v102\n"
    "v_accvgpr_read_b32 v102, a32\n"
    "v_mul_f32 v102, v102, v100\n"
    "v_accvgpr_write_b32 a32, v102\n"
    "v_accvgpr_read_b32 v102, a33\n"
    "v_mul_f32 v102, v102, v100\n"
    "v_accvgpr_write_b32 a33, v102\n"
    "v_accvgpr_read_b32 v102, a34\n"
    "v_mul_f32 v102, v102, v100\n"
    "v_accvgpr_write_b32 a34, v102\n"
    "v_accvgpr_read_b32 v102, a35\n"
    "v_mul_f32 v102, v102, v100\n"
    "v_accvgpr_write_b32 a35, v102\n"
    "v_accvgpr_read_b32 v102, a36\n"
    "v_mul_f32 v102, v102, v100\n"
    "v_accvgpr_write_b32 a36, v102\n"
    "v_accvgpr_read_b32 v102, a37\n"
    "v_mul_f32 v102, v102, v100\n"
    "v_accvgpr_write_b32 a37, v102\n"
    "v_accvgpr_read_b32 v102, a38\n"
    "v_mul_f32 v102, v102, v100\n"
    "v_accvgpr_write_b32 a38, v102\n"
    "v_accvgpr_read_b32 v102, a39\n"
    "v_mul_f32 v102, v102, v100\n"
    "v_accvgpr_write_b32 a39, v102\n"
    "v_accvgpr_read_b32 v102, a56\n"
    "v_mul_f32 v102, v102, v100\n"
    "v_accvgpr_write_b32 a56, v102\n"
    "v_accvgpr_read_b32 v102, a57\n"
    "v_mul_f32 v102, v102, v100\n"
    "v_accvgpr_write_b32 a57, v102\n"
    "v_accvgpr_read_b32 v102, a58\n"
    "v_mul_f32 v102, v102, v100\n"
    "v_accvgpr_write_b32 a58, v102\n"
    "v_accvgpr_read_b32 v102, a59\n"
    "v_mul_f32 v102, v102, v100\n"
    "v_accvgpr_write_b32 a59, v102\n"
    "v_accvgpr_read_b32 v102, a60\n"
    "v_mul_f32 v102, v102, v100\n"
    "v_accvgpr_write_b32 a60, v102\n"
    "v_accvgpr_read_b32 v102, a61\n"
    "v_mul_f32 v102, v102, v100\n"
    "v_accvgpr_write_b32 a61, v102\n"
    "v_accvgpr_read_b32 v102, a62\n"
    "v_mul_f32 v102, v102, v100\n"
    "v_accvgpr_write_b32 a62, v102\n"
    "v_accvgpr_read_b32 v102, a63\n"
    "v_mul_f32 v102, v102, v100\n"
    "v_accvgpr_write_b32 a63, v102\n"
    "v_accvgpr_read_b32 v102, a80\n"
    "v_mul_f32 v102, v102, v100\n"
    "v_accvgpr_write_b32 a80, v102\n"
    "v_accvgpr_read_b32 v102, a81\n"
    "v_mul_f32 v102, v102, v100\n"
    "v_accvgpr_write_b32 a81, v102\n"
    "v_accvgpr_read_b32 v102, a82\n"
    "v_mul_f32 v102, v102, v100\n"
    "v_accvgpr_write_b32 a82, v102\n"
    "v_accvgpr_read_b32 v102, a83\n"
    "v_mul_f32 v102, v102, v100\n"
    "v_accvgpr_write_b32 a83, v102\n"
    "v_accvgpr_read_b32 v102, a84\n"
    "v_mul_f32 v102, v102, v100\n"
    "v_accvgpr_write_b32 a84, v102\n"
    "v_accvgpr_read_b32 v102, a85\n"
    "v_mul_f32 v102, v102, v100\n"
    "v_accvgpr_write_b32 a85, v102\n"
    "v_accvgpr_read_b32 v102, a86\n"
    "v_mul_f32 v102, v102, v100\n"
    "v_accvgpr_write_b32 a86, v102\n"
    "v_accvgpr_read_b32 v102, a87\n"
    "v_mul_f32 v102, v102, v100\n"
    "v_accvgpr_write_b32 a87, v102\n"
    "v_accvgpr_read_b32 v102, a104\n"
    "v_mul_f32 v102, v102, v100\n"
    "v_accvgpr_write_b32 a104, v102\n"
    "v_accvgpr_read_b32 v102, a105\n"
    "v_mul_f32 v102, v102, v100\n"
    "v_accvgpr_write_b32 a105, v102\n"
    "v_accvgpr_read_b32 v102, a106\n"
    "v_mul_f32 v102, v102, v100\n"
    "v_accvgpr_write_b32 a106, v102\n"
    "v_accvgpr_read_b32 v102, a107\n"
    "v_mul_f32 v102, v102, v100\n"
    "v_accvgpr_write_b32 a107, v102\n"
    "v_accvgpr_read_b32 v102, a108\n"
    "v_mul_f32 v102, v102, v100\n"
    "v_accvgpr_write_b32 a108, v102\n"
    "v_accvgpr_read_b32 v102, a109\n"
    "v_mul_f32 v102, v102, v100\n"
    "v_accvgpr_write_b32 a109, v102\n"
    "v_accvgpr_read_b32 v102, a110\n"
    "v_mul_f32 v102, v102, v100\n"
    "v_accvgpr_write_b32 a110, v102\n"
    "v_accvgpr_read_b32 v102, a111\n"
    "v_mul_f32 v102, v102, v100\n"
    "v_accvgpr_write_b32 a111, v102\n"
    "v_accvgpr_read_b32 v102, a128\n"
    "v_mul_f32 v102, v102, v100\n"
    "v_accvgpr_write_b32 a128, v102\n"
    "v_accvgpr_read_b32 v102, a129\n"
    "v_mul_f32 v102, v102, v100\n"
    "v_accvgpr_write_b32 a129, v102\n"
    "v_accvgpr_read_b32 v102, a130\n"
    "v_mul_f32 v102, v102, v100\n"
    "v_accvgpr_write_b32 a130, v102\n"
    "v_accvgpr_read_b32 v102, a131\n"
    "v_mul_f32 v102, v102, v100\n"
    "v_accvgpr_write_b32 a131, v102\n"
    "v_accvgpr_read_b32 v102, a132\n"
    "v_mul_f32 v102, v102, v100\n"
    "v_accvgpr_write_b32 a132, v102\n"
    "v_accvgpr_read_b32 v102, a133\n"
    "v_mul_f32 v102, v102, v100\n"
    "v_accvgpr_write_b32 a133, v102\n"
    "v_accvgpr_read_b32 v102, a134\n"
    "v_mul_f32 v102, v102, v100\n"
    "v_accvgpr_write_b32 a134, v102\n"
    "v_accvgpr_read_b32 v102, a135\n"
    "v_mul_f32 v102, v102, v100\n"
    "v_accvgpr_write_b32 a135, v102\n"
    "v_accvgpr_read_b32 v102, a152\n"
    "v_mul_f32 v102, v102, v100\n"
    "v_accvgpr_write_b32 a152, v102\n"
    "v_accvgpr_read_b32 v102, a153\n"
    "v_mul_f32 v102, v102, v100\n"
    "v_accvgpr_write_b32 a153, v102\n"
    "v_accvgpr_read_b32 v102, a154\n"
    "v_mul_f32 v102, v102, v100\n"
    "v_accvgpr_write_b32 a154, v102\n"
    "v_accvgpr_read_b32 v102, a155\n"
    "v_mul_f32 v102, v102, v100\n"
    "v_accvgpr_write_b32 a155, v102\n"
    "v_accvgpr_read_b32 v102, a156\n"
    "v_mul_f32 v102, v102, v100\n"
    "v_accvgpr_write_b32 a156, v102\n"
    "v_accvgpr_read_b32 v102, a157\n"
    "v_mul_f32 v102, v102, v100\n"
    "v_accvgpr_write_b32 a157, v102\n"
    "v_accvgpr_read_b32 v102, a158\n"
    "v_mul_f32 v102, v102, v100\n"
    "v_accvgpr_write_b32 a158, v102\n"
    "v_accvgpr_read_b32 v102, a159\n"
    "v_mul_f32 v102, v102, v100\n"
    "v_accvgpr_write_b32 a159, v102\n"
    "v_accvgpr_read_b32 v102, a176\n"
    "v_mul_f32 v102, v102, v100\n"
    "v_accvgpr_write_b32 a176, v102\n"
    "v_accvgpr_read_b32 v102, a177\n"
    "v_mul_f32 v102, v102, v100\n"
    "v_accvgpr_write_b32 a177, v102\n"
    "v_accvgpr_read_b32 v102, a178\n"
    "v_mul_f32 v102, v102, v100\n"
    "v_accvgpr_write_b32 a178, v102\n"
    "v_accvgpr_read_b32 v102, a179\n"
    "v_mul_f32 v102, v102, v100\n"
    "v_accvgpr_write_b32 a179, v102\n"
    "v_accvgpr_read_b32 v102, a180\n"
    "v_mul_f32 v102, v102, v100\n"
    "v_accvgpr_write_b32 a180, v102\n"
    "v_accvgpr_read_b32 v102, a181\n"
    "v_mul_f32 v102, v102, v100\n"
    "v_accvgpr_write_b32 a181, v102\n"
    "v_accvgpr_read_b32 v102, a182\n"
    "v_mul_f32 v102, v102, v100\n"
    "v_accvgpr_write_b32 a182, v102\n"
    "v_accvgpr_read_b32 v102, a183\n"
    "v_mul_f32 v102, v102, v100\n"
    "v_accvgpr_write_b32 a183, v102\n"
    "v_mul_f32 v248, v248, v101\n"
    "v_mul_f32 v249, v249, v101\n"
    "v_mul_f32 v250, v250, v101\n"
    "v_mul_f32 v251, v251, v101\n"
    "v_sub_f32 v152, v152, v98\n"
    "v_sub_f32 v116, v116, v98\n"
    "v_sub_f32 v153, v153, v98\n"
    "v_sub_f32 v117, v117, v98\n"
    "v_sub_f32 v154, v154, v98\n"
    "v_sub_f32 v118, v118, v98\n"
    "v_sub_f32 v155, v155, v98\n"
    "v_sub_f32 v119, v119, v98\n"
    "v_lshlrev_b32 v102, 16, v140\n"
    "v_and_b32 v103, 4294901760, v140\n"
    "v_mul_f32 v102, v102, v101\n"
    "v_mul_f32 v103, v103, v101\n"
    "v_cvt_pk_bf16_f32 v140, v102, v103\n"
    "v_lshlrev_b32 v102, 16, v141\n"
    "v_and_b32 v103, 4294901760, v141\n"
    "v_mul_f32 v102, v102, v101\n"
    "v_mul_f32 v103, v103, v101\n"
    "v_cvt_pk_bf16_f32 v141, v102, v103\n"
    "v_lshlrev_b32 v102, 16, v142\n"
    "v_and_b32 v103, 4294901760, v142\n"
    "v_mul_f32 v102, v102, v101\n"
    "v_mul_f32 v103, v103, v101\n"
    "v_cvt_pk_bf16_f32 v142, v102, v103\n"
    "v_lshlrev_b32 v102, 16, v143\n"
    "v_and_b32 v103, 4294901760, v143\n"
    "v_mul_f32 v102, v102, v101\n"
    "v_mul_f32 v103, v103, v101\n"
    "v_cvt_pk_bf16_f32 v143, v102, v103\n"
    "v_lshlrev_b32 v102, 16, v128\n"
    "v_and_b32 v103, 4294901760, v128\n"
    "v_mul_f32 v102, v102, v101\n"
    "v_mul_f32 v103, v103, v101\n"
    "v_cvt_pk_bf16_f32 v128, v102, v103\n"
    "v_lshlrev_b32 v102, 16, v129\n"
    "v_and_b32 v103, 4294901760, v129\n"
    "v_mul_f32 v102, v102, v101\n"
    "v_mul_f32 v103, v103, v101\n"
    "v_cvt_pk_bf16_f32 v129, v102, v103\n"
    "v_accvgpr_read_b32 v102, a16\n"
    "v_mul_f32 v102, v102, v101\n"
    "v_accvgpr_write_b32 a16, v102\n"
    "v_accvgpr_read_b32 v102, a17\n"
    "v_mul_f32 v102, v102, v101\n"
    "v_accvgpr_write_b32 a17, v102\n"
    "v_accvgpr_read_b32 v102, a18\n"
    "v_mul_f32 v102, v102, v101\n"
    "v_accvgpr_write_b32 a18, v102\n"
    "v_accvgpr_read_b32 v102, a19\n"
    "v_mul_f32 v102, v102, v101\n"
    "v_accvgpr_write_b32 a19, v102\n"
    "v_accvgpr_read_b32 v102, a20\n"
    "v_mul_f32 v102, v102, v101\n"
    "v_accvgpr_write_b32 a20, v102\n"
    "v_accvgpr_read_b32 v102, a21\n"
    "v_mul_f32 v102, v102, v101\n"
    "v_accvgpr_write_b32 a21, v102\n"
    "v_accvgpr_read_b32 v102, a22\n"
    "v_mul_f32 v102, v102, v101\n"
    "v_accvgpr_write_b32 a22, v102\n"
    "v_accvgpr_read_b32 v102, a23\n"
    "v_mul_f32 v102, v102, v101\n"
    "v_accvgpr_write_b32 a23, v102\n"
    "v_accvgpr_read_b32 v102, a40\n"
    "v_mul_f32 v102, v102, v101\n"
    "v_accvgpr_write_b32 a40, v102\n"
    "v_accvgpr_read_b32 v102, a41\n"
    "v_mul_f32 v102, v102, v101\n"
    "v_accvgpr_write_b32 a41, v102\n"
    "v_accvgpr_read_b32 v102, a42\n"
    "v_mul_f32 v102, v102, v101\n"
    "v_accvgpr_write_b32 a42, v102\n"
    "v_accvgpr_read_b32 v102, a43\n"
    "v_mul_f32 v102, v102, v101\n"
    "v_accvgpr_write_b32 a43, v102\n"
    "v_accvgpr_read_b32 v102, a44\n"
    "v_mul_f32 v102, v102, v101\n"
    "v_accvgpr_write_b32 a44, v102\n"
    "v_accvgpr_read_b32 v102, a45\n"
    "v_mul_f32 v102, v102, v101\n"
    "v_accvgpr_write_b32 a45, v102\n"
    "v_accvgpr_read_b32 v102, a46\n"
    "v_mul_f32 v102, v102, v101\n"
    "v_accvgpr_write_b32 a46, v102\n"
    "v_accvgpr_read_b32 v102, a47\n"
    "v_mul_f32 v102, v102, v101\n"
    "v_accvgpr_write_b32 a47, v102\n"
    "v_accvgpr_read_b32 v102, a64\n"
    "v_mul_f32 v102, v102, v101\n"
    "v_accvgpr_write_b32 a64, v102\n"
    "v_accvgpr_read_b32 v102, a65\n"
    "v_mul_f32 v102, v102, v101\n"
    "v_accvgpr_write_b32 a65, v102\n"
    "v_accvgpr_read_b32 v102, a66\n"
    "v_mul_f32 v102, v102, v101\n"
    "v_accvgpr_write_b32 a66, v102\n"
    "v_accvgpr_read_b32 v102, a67\n"
    "v_mul_f32 v102, v102, v101\n"
    "v_accvgpr_write_b32 a67, v102\n"
    "v_accvgpr_read_b32 v102, a68\n"
    "v_mul_f32 v102, v102, v101\n"
    "v_accvgpr_write_b32 a68, v102\n"
    "v_accvgpr_read_b32 v102, a69\n"
    "v_mul_f32 v102, v102, v101\n"
    "v_accvgpr_write_b32 a69, v102\n"
    "v_accvgpr_read_b32 v102, a70\n"
    "v_mul_f32 v102, v102, v101\n"
    "v_accvgpr_write_b32 a70, v102\n"
    "v_accvgpr_read_b32 v102, a71\n"
    "v_mul_f32 v102, v102, v101\n"
    "v_accvgpr_write_b32 a71, v102\n"
    "v_accvgpr_read_b32 v102, a88\n"
    "v_mul_f32 v102, v102, v101\n"
    "v_accvgpr_write_b32 a88, v102\n"
    "v_accvgpr_read_b32 v102, a89\n"
    "v_mul_f32 v102, v102, v101\n"
    "v_accvgpr_write_b32 a89, v102\n"
    "v_accvgpr_read_b32 v102, a90\n"
    "v_mul_f32 v102, v102, v101\n"
    "v_accvgpr_write_b32 a90, v102\n"
    "v_accvgpr_read_b32 v102, a91\n"
    "v_mul_f32 v102, v102, v101\n"
    "v_accvgpr_write_b32 a91, v102\n"
    "v_accvgpr_read_b32 v102, a92\n"
    "v_mul_f32 v102, v102, v101\n"
    "v_accvgpr_write_b32 a92, v102\n"
    "v_accvgpr_read_b32 v102, a93\n"
    "v_mul_f32 v102, v102, v101\n"
    "v_accvgpr_write_b32 a93, v102\n"
    "v_accvgpr_read_b32 v102, a94\n"
    "v_mul_f32 v102, v102, v101\n"
    "v_accvgpr_write_b32 a94, v102\n"
    "v_accvgpr_read_b32 v102, a95\n"
    "v_mul_f32 v102, v102, v101\n"
    "v_accvgpr_write_b32 a95, v102\n"
    "v_accvgpr_read_b32 v102, a112\n"
    "v_mul_f32 v102, v102, v101\n"
    "v_accvgpr_write_b32 a112, v102\n"
    "v_accvgpr_read_b32 v102, a113\n"
    "v_mul_f32 v102, v102, v101\n"
    "v_accvgpr_write_b32 a113, v102\n"
    "v_accvgpr_read_b32 v102, a114\n"
    "v_mul_f32 v102, v102, v101\n"
    "v_accvgpr_write_b32 a114, v102\n"
    "v_accvgpr_read_b32 v102, a115\n"
    "v_mul_f32 v102, v102, v101\n"
    "v_accvgpr_write_b32 a115, v102\n"
    "v_accvgpr_read_b32 v102, a116\n"
    "v_mul_f32 v102, v102, v101\n"
    "v_accvgpr_write_b32 a116, v102\n"
    "v_accvgpr_read_b32 v102, a117\n"
    "v_mul_f32 v102, v102, v101\n"
    "v_accvgpr_write_b32 a117, v102\n"
    "v_accvgpr_read_b32 v102, a118\n"
    "v_mul_f32 v102, v102, v101\n"
    "v_accvgpr_write_b32 a118, v102\n"
    "v_accvgpr_read_b32 v102, a119\n"
    "v_mul_f32 v102, v102, v101\n"
    "v_accvgpr_write_b32 a119, v102\n"
    "v_accvgpr_read_b32 v102, a136\n"
    "v_mul_f32 v102, v102, v101\n"
    "v_accvgpr_write_b32 a136, v102\n"
    "v_accvgpr_read_b32 v102, a137\n"
    "v_mul_f32 v102, v102, v101\n"
    "v_accvgpr_write_b32 a137, v102\n"
    "v_accvgpr_read_b32 v102, a138\n"
    "v_mul_f32 v102, v102, v101\n"
    "v_accvgpr_write_b32 a138, v102\n"
    "v_accvgpr_read_b32 v102, a139\n"
    "v_mul_f32 v102, v102, v101\n"
    "v_accvgpr_write_b32 a139, v102\n"
    "v_accvgpr_read_b32 v102, a140\n"
    "v_mul_f32 v102, v102, v101\n"
    "v_accvgpr_write_b32 a140, v102\n"
    "v_accvgpr_read_b32 v102, a141\n"
    "v_mul_f32 v102, v102, v101\n"
    "v_accvgpr_write_b32 a141, v102\n"
    "v_accvgpr_read_b32 v102, a142\n"
    "v_mul_f32 v102, v102, v101\n"
    "v_accvgpr_write_b32 a142, v102\n"
    "v_accvgpr_read_b32 v102, a143\n"
    "v_mul_f32 v102, v102, v101\n"
    "v_accvgpr_write_b32 a143, v102\n"
    "v_accvgpr_read_b32 v102, a160\n"
    "v_mul_f32 v102, v102, v101\n"
    "v_accvgpr_write_b32 a160, v102\n"
    "v_accvgpr_read_b32 v102, a161\n"
    "v_mul_f32 v102, v102, v101\n"
    "v_accvgpr_write_b32 a161, v102\n"
    "v_accvgpr_read_b32 v102, a162\n"
    "v_mul_f32 v102, v102, v101\n"
    "v_accvgpr_write_b32 a162, v102\n"
    "v_accvgpr_read_b32 v102, a163\n"
    "v_mul_f32 v102, v102, v101\n"
    "v_accvgpr_write_b32 a163, v102\n"
    "v_accvgpr_read_b32 v102, a164\n"
    "v_mul_f32 v102, v102, v101\n"
    "v_accvgpr_write_b32 a164, v102\n"
    "v_accvgpr_read_b32 v102, a165\n"
    "v_mul_f32 v102, v102, v101\n"
    "v_accvgpr_write_b32 a165, v102\n"
    "v_accvgpr_read_b32 v102, a166\n"
    "v_mul_f32 v102, v102, v101\n"
    "v_accvgpr_write_b32 a166, v102\n"
    "v_accvgpr_read_b32 v102, a167\n"
    "v_mul_f32 v102, v102, v101\n"
    "v_accvgpr_write_b32 a167, v102\n"
    "v_accvgpr_read_b32 v102, a184\n"
    "v_mul_f32 v102, v102, v101\n"
    "v_accvgpr_write_b32 a184, v102\n"
    "v_accvgpr_read_b32 v102, a185\n"
    "v_mul_f32 v102, v102, v101\n"
    "v_accvgpr_write_b32 a185, v102\n"
    "v_accvgpr_read_b32 v102, a186\n"
    "v_mul_f32 v102, v102, v101\n"
    "v_accvgpr_write_b32 a186, v102\n"
    "v_accvgpr_read_b32 v102, a187\n"
    "v_mul_f32 v102, v102, v101\n"
    "v_accvgpr_write_b32 a187, v102\n"
    "v_accvgpr_read_b32 v102, a188\n"
    "v_mul_f32 v102, v102, v101\n"
    "v_accvgpr_write_b32 a188, v102\n"
    "v_accvgpr_read_b32 v102, a189\n"
    "v_mul_f32 v102, v102, v101\n"
    "v_accvgpr_write_b32 a189, v102\n"
    "v_accvgpr_read_b32 v102, a190\n"
    "v_mul_f32 v102, v102, v101\n"
    "v_accvgpr_write_b32 a190, v102\n"
    "v_accvgpr_read_b32 v102, a191\n"
    "v_mul_f32 v102, v102, v101\n"
    "v_accvgpr_write_b32 a191, v102\n"
    "s_nop 3\n"
    "s_setpc_b64 s[84:85]\n"
    "LEND:\n"
    : "+v"(tid)
    : [bid] "s"(blockIdx.x), [ldsb] "s"(ldsb),
      [xlo] "s"((unsigned)(size_t)x), [xhi] "s"((unsigned)((size_t)x >> 32)),
      [klo] "s"((unsigned)(size_t)k), [khi] "s"((unsigned)((size_t)k >> 32)),
      [vlo] "s"((unsigned)(size_t)v), [vhi] "s"((unsigned)((size_t)v >> 32)),
      [wqlo] "s"((unsigned)(size_t)wq), [wqhi] "s"((unsigned)((size_t)wq >> 32)),
      [wolo] "s"((unsigned)(size_t)wo), [wohi] "s"((unsigned)((size_t)wo >> 32)),
      [olo] "s"((unsigned)(size_t)out), [ohi] "s"((unsigned)((size_t)out >> 32)),
      [wslo] "s"((unsigned)(size_t)ws), [wshi] "s"((unsigned)((size_t)ws >> 32))
    : "v1", "v2", "v3", "v4", "v5", "v6", "v7", "v8", "v9", "v10", "v11", "v12", "v13", "v14", "v15", "v16", "v17", "v18", "v19", "v20", "v21", "v22", "v23", "v24", "v25", "v26", "v27", "v28", "v29", "v30", "v31", "v32", "v33", "v34", "v35", "v36", "v37", "v38", "v39", "v40", "v41", "v42", "v43", "v44", "v45", "v46", "v47", "v48", "v49", "v50", "v51", "v52", "v53", "v54", "v55", "v56", "v57", "v58", "v59", "v60", "v61", "v62", "v63", "v64", "v65", "v66", "v67", "v68", "v69", "v70", "v71", "v72", "v73", "v74", "v75", "v76", "v77", "v78", "v79", "v80", "v81", "v82", "v83", "v84", "v85", "v86", "v87", "v88", "v89", "v90", "v91", "v92", "v93", "v94", "v95", "v96", "v97", "v98", "v99", "v100", "v101", "v102", "v103", "v104", "v105", "v106", "v107", "v108", "v109", "v110", "v111", "v112", "v113", "v114", "v115", "v116", "v117", "v118", "v119", "v120", "v121", "v122", "v123", "v124", "v125", "v126", "v127", "v128", "v129", "v130", "v131", "v132", "v133", "v134", "v135", "v136", "v137", "v138", "v139", "v140", "v141", "v142", "v143", "v144", "v145", "v146", "v147", "v148", "v149", "v150", "v151", "v152", "v153", "v154", "v155", "v156", "v157", "v158", "v159", "v160", "v161", "v162", "v163", "v164", "v165", "v166", "v167", "v168", "v169", "v170", "v171", "v172", "v173", "v174", "v175", "v176", "v177", "v178", "v179", "v180", "v181", "v182", "v183", "v184", "v185", "v186", "v187", "v188", "v189", "v190", "v191", "v192", "v193", "v194", "v195", "v196", "v197", "v198", "v199", "v200", "v201", "v202", "v203", "v204", "v205", "v206", "v207", "v208", "v209", "v210", "v211", "v212", "v213", "v214", "v215", "v216", "v217", "v218", "v219", "v220", "v221", "v222", "v223", "v224", "v225", "v226", "v227", "v228", "v229", "v230", "v231", "v232", "v233", "v234", "v235", "v236", "v237", "v238", "v239", "v240", "v241", "v242", "v243", "v244", "v245", "v246", "v247", "v248", "v249", "v250", "v251", "v252", "v253", "v254", "v255", "a0", "a1", "a2", "a3", "a4", "a5", "a6", "a7", "a8", "a9", "a10", "a11", "a12", "a13", "a14", "a15", "a16", "a17", "a18", "a19", "a20", "a21", "a22", "a23", "a24", "a25", "a26", "a27", "a28", "a29", "a30", "a31", "a32", "a33", "a34", "a35", "a36", "a37", "a38", "a39", "a40", "a41", "a42", "a43", "a44", "a45", "a46", "a47", "a48", "a49", "a50", "a51", "a52", "a53", "a54", "a55", "a56", "a57", "a58", "a59", "a60", "a61", "a62", "a63", "a64", "a65", "a66", "a67", "a68", "a69", "a70", "a71", "a72", "a73", "a74", "a75", "a76", "a77", "a78", "a79", "a80", "a81", "a82", "a83", "a84", "a85", "a86", "a87", "a88", "a89", "a90", "a91", "a92", "a93", "a94", "a95", "a96", "a97", "a98", "a99", "a100", "a101", "a102", "a103", "a104", "a105", "a106", "a107", "a108", "a109", "a110", "a111", "a112", "a113", "a114", "a115", "a116", "a117", "a118", "a119", "a120", "a121", "a122", "a123", "a124", "a125", "a126", "a127", "a128", "a129", "a130", "a131", "a132", "a133", "a134", "a135", "a136", "a137", "a138", "a139", "a140", "a141", "a142", "a143", "a144", "a145", "a146", "a147", "a148", "a149", "a150", "a151", "a152", "a153", "a154", "a155", "a156", "a157", "a158", "a159", "a160", "a161", "a162", "a163", "a164", "a165", "a166", "a167", "a168", "a169", "a170", "a171", "a172", "a173", "a174", "a175", "a176", "a177", "a178", "a179", "a180", "a181", "a182", "a183", "a184", "a185", "a186", "a187", "a188", "a189", "a190", "a191", "a192", "a193", "a194", "a195", "a196", "a197", "a198", "a199", "a200", "a201", "a202", "a203", "a204", "a205", "a206", "a207", "a208", "a209", "a210", "a211", "a212", "a213", "a214", "a215", "a216", "a217", "a218", "a219", "a220", "a221", "a222", "a223", "a224", "a225", "a226", "a227", "a228", "a229", "a230", "a231", "a232", "a233", "a234", "a235", "a236", "a237", "a238", "a239", "a240", "a241", "a242", "a243", "a244", "a245", "a246", "a247", "a248", "a249", "a250", "a251", "a252", "a253", "a254", "a255", "s24", "s25", "s26", "s27", "s28", "s29", "s30", "s31", "s33", "s34", "s35", "s36", "s37", "s38", "s39", "s40", "s41", "s42", "s43", "s44", "s45", "s46", "s47", "s48", "s49", "s50", "s51", "s52", "s53", "s54", "s55", "s56", "s57", "s58", "s59", "s60", "s61", "s62", "s63", "s64", "s65", "s66", "s67", "s68", "s69", "s70", "s71", "s72", "s73", "s74", "s75", "s76", "s77", "s78", "s79", "s80", "s81", "s82", "s83", "s84", "s85", "s86", "s87", "s88", "s89", "s90", "s91", "s92", "s93", "s94", "s95", "s96", "s97", "s98", "s99", "vcc", "scc", "memory");
}


extern "C" void kernel_launch(void* const* d_in, const int* in_sizes, int n_in, void* d_out, int out_size, void* d_ws, size_t ws_size,
                              hipStream_t stream) {
  mha_fused_kernel<<<256, 256, 0, stream>>>(d_in[0], d_in[1], d_in[2], d_in[3], d_in[4], d_out, d_ws);

}
```
